```python
import torch

def _softmax(x, axis=-1):
    return torch.softmax(x, dim=axis)
def _gelu(x):
    return torch.nn.functional.gelu(x, approximate="tanh")
def _silu(x):
    return torch.nn.functional.silu(x)
def _sigmoid(x):
    return torch.sigmoid(x)
def _layernorm(x, axis=-1, eps=1e-5):
    m = x.mean(dim=axis, keepdim=True)
    v = x.var(dim=axis, keepdim=True, unbiased=False)
    return (x - m) / torch.sqrt(v + eps)
def _scatter_add(tgt, idx, src, axis=0):
    return tgt.clone().index_add_(axis, idx, src)
def _segment_sum(src, seg, n):
    out = torch.zeros((n,) + src.shape[1:], dtype=src.dtype)
    return out.index_add_(0, seg, src)
def _pad(x, pw):
    flat = tuple(v for pair in reversed(pw) for v in pair)
    return torch.nn.functional.pad(x, flat)

kernel_name = 'parallel_sum__spectral_gfnet__glu_gated_bilinear__prompt'

def setup_inputs():

    _w = (lambda a: (lambda t: t.to(torch.bfloat16) if t.is_floating_point() else t)(torch.as_tensor(a)))
    def _A_setup():
        B, S, D = 1, 2048, 640
        Df = D // 2 + 1
        return {
            "x": _w((lambda *s: torch.randn(*s, dtype=torch.bfloat16) * 0.05)(B, S, D)),
            "w_r": _w((lambda *s: torch.randn(*s, dtype=torch.bfloat16) * 0.4)(S, Df) * 0.02),
            "w_i": _w((lambda *s: torch.randn(*s, dtype=torch.bfloat16) * 0.4)(S, Df) * 0.02),
        }
    _A_in = _A_setup()

    def _B_setup():
        B, S, D, F = 1, 2048, 640, 1536
        return {
            "x": _w((lambda *s: torch.randn(*s, dtype=torch.bfloat16) * 0.05)(B, S, D)),
            "w_gate": _w((lambda *s: torch.randn(*s, dtype=torch.bfloat16) * 0.2)(D, F) * (D ** -0.5)),
            "w_up": _w((lambda *s: torch.randn(*s, dtype=torch.bfloat16) * 0.2)(D, F) * (D ** -0.5)),
            "w_down": _w((lambda *s: torch.randn(*s, dtype=torch.bfloat16) * 0.2)(F, D) * (F ** -0.5)),
        }
    _B_in = _B_setup()

    return {
        "A_x": _A_in["x"],
        "A_w_r": _A_in["w_r"],
        "A_w_i": _A_in["w_i"],
        "B_x": _B_in["x"],
        "B_w_gate": _B_in["w_gate"],
        "B_w_up": _B_in["w_up"],
        "B_w_down": _B_in["w_down"],
    }

def reference(A_x, A_w_r, A_w_i, B_x, B_w_gate, B_w_up, B_w_down):
    def _A_ref(x, w_r, w_i):
        B, S, D = x.shape
        X = (lambda x, axes=(-2,-1): torch.fft.rfft2(x.float(), dim=axes))(x, axes=(-2, -1))
        W = w_r + 1j * w_i
        X = X * W
        y = (lambda x, s, axes=(-2,-1): torch.fft.irfft2(x, s=s, dim=axes).to(torch.bfloat16))(X, (S, D), axes=(-2, -1))
        return y

    def _B_ref(x, w_gate, w_up, w_down):
        g = torch.einsum("bsd,df->bsf", x, w_gate)
        u = torch.einsum("bsd,df->bsf", x, w_up)
        h = g * u
        return torch.einsum("bsf,fd->bsd", h, w_down)

    x = A_x
    xn = _layernorm(x, axis=-1)
    return x + _A_ref(xn, A_w_r, A_w_i) + _B_ref(xn, B_w_gate, B_w_up, B_w_down)
```

```cpp
#include <hip/hip_runtime.h>
#include <hip/hip_bf16.h>
#include <utility>

typedef unsigned short u16;
typedef __attribute__((ext_vector_type(8))) __bf16 bf16x8;
typedef __attribute__((ext_vector_type(2))) __bf16 bf16x2;
typedef __attribute__((ext_vector_type(8))) _Float16 f16x8;
typedef __attribute__((ext_vector_type(2))) _Float16 f16x2;
typedef __attribute__((ext_vector_type(4))) short s16x4;
typedef __attribute__((ext_vector_type(2))) float f32x2;
typedef __attribute__((ext_vector_type(4))) float f32x4;
typedef __attribute__((ext_vector_type(16))) float f32x16;
typedef __attribute__((ext_vector_type(4))) unsigned u32x4;
typedef __attribute__((ext_vector_type(2))) unsigned u32x2;
#define DI __device__ __forceinline__
#define LDS_AS __attribute__((address_space(3)))

namespace tb {
constexpr double kPi = 3.14159265358979323846264338327950288;
constexpr double sinT(double x) { double x2 = x * x, t = x, s = x; for (int i = 1; i < 12; ++i) { t *= -x2 / ((2 * i) * (2 * i + 1)); s += t; } return s; }
constexpr double cosT(double x) { double x2 = x * x, t = 1, s = 1; for (int i = 1; i < 12; ++i) { t *= -x2 / ((2 * i - 1) * (2 * i)); s += t; } return s; }
constexpr double cos2pi(int q, int den) {
  q %= den; if (2 * q > den) q = den - q;
  double sg = 1; if (4 * q > den) { q = den / 2 - q; sg = -1; }
  if (8 * q > den) return sg * sinT(2 * kPi * (den / 4 - q) / den);
  return sg * cosT(2 * kPi * q / den);
}
constexpr double sin2pi(int q, int den) { return cos2pi(q + 3 * (den / 4), den); }
constexpr u16 bfbits(double d) { unsigned u = __builtin_bit_cast(unsigned, (float)d); u += 0x7fffu + ((u >> 16) & 1u); return (u16)(u >> 16); }
constexpr u16 hbits(double d) { return __builtin_bit_cast(u16, (_Float16)d); }
struct C640 { u16 v[640]; };
constexpr C640 mk640() { C640 c{}; for (int i = 0; i < 640; ++i) c.v[i] = bfbits(cos2pi(i, 640)); return c; }
constexpr C640 c640 = mk640();
struct Blk { u16 v[2560]; };
constexpr Blk mkRF(int I) {
  Blk b{}; const int ntg = I / 20, ks = I % 20;
  for (int q = 0; q < 320; ++q) {
    const int l = q & 63, nt = q >> 6, d0 = 32 * ks + 8 * (l >> 4), kk = 80 * ntg + 16 * nt + (l & 15);
    const int c = kk == 1 ? 320 : (kk >> 1);
    int idx = (c * d0 + ((kk & 1) && kk != 1 ? 160 : 0)) % 640;
    for (int j = 0; j < 8; ++j) { b.v[q * 8 + j] = c640.v[idx]; idx += c; if (idx >= 640) idx -= 640; }
  }
  return b;
}
template <int I> struct RFp { static constexpr Blk v = mkRF(I); };
struct alignas(16) TabRF { Blk b[160]; };
template <int... I> constexpr TabRF mkTabRF(std::integer_sequence<int, I...>) { return TabRF{{RFp<I>::v...}}; }

constexpr int krow(int s, int h, int j) { return 16 * s + 8 * (j >> 2) + 4 * h + (j & 3); }
constexpr int crow(int reg, int h) { return (reg & 3) + 8 * (reg >> 2) + 4 * h; }
struct alignas(16) TabF {
  u16 g1[2][2][2][64][8];
  u16 f2[2][2][2][2][64][8];
  u16 g3[2][2][2][64][8];
  float tw[8][64][4];
};
struct C64 { double c[64]; };
constexpr C64 mk64() { C64 c{}; for (int i = 0; i < 64; ++i) c.c[i] = cos2pi(i, 64); return c; }
constexpr C64 c64 = mk64();
constexpr TabF mkTabF() {
  TabF T{};
  for (int t = 0; t < 2; ++t) for (int ks = 0; ks < 2; ++ks) for (int l = 0; l < 64; ++l) for (int j = 0; j < 8; ++j) {
    const int r = l & 31, k = krow(ks, l >> 5, j);
    const int q1 = (r * (2 * k + t)) & 63, q3 = (k * (2 * r + t)) & 63;
    T.g1[t][ks][0][l][j] = hbits(c64.c[q1] * 0.125); T.g1[t][ks][1][l][j] = hbits(c64.c[(q1 + 48) & 63] * 0.125);
    T.g3[t][ks][0][l][j] = hbits(c64.c[q3]);         T.g3[t][ks][1][l][j] = hbits(c64.c[(q3 + 48) & 63]);
    for (int a = 0; a < 2; ++a) {
      const int q2 = ((32 * a + r) * (32 * t + k)) & 63;
      T.f2[a][t][ks][0][l][j] = hbits(c64.c[q2]); T.f2[a][t][ks][1][l][j] = hbits(c64.c[(q2 + 48) & 63]);
    }
  }
  for (int reg = 0; reg < 16; ++reg) for (int l = 0; l < 64; ++l) {
    const int q = crow(reg, l >> 5) * (l & 31);
    T.tw[reg >> 1][l][(reg & 1) * 2] = (float)cos2pi(q, 2048);
    T.tw[reg >> 1][l][(reg & 1) * 2 + 1] = (float)sin2pi(q, 2048);
  }
  return T;
}
}

#if defined(__HIP_DEVICE_COMPILE__)
static __device__ const tb::TabRF g_RF = tb::mkTabRF(std::make_integer_sequence<int, 160>{});
static __device__ const tb::TabF g_TF = tb::mkTabF();
static __device__ const tb::C640 g_c640 = tb::c640;
#else
static __device__ const tb::TabRF g_RF = {};
static __device__ const tb::TabF g_TF = {};
static __device__ const tb::C640 g_c640 = {};
#endif

constexpr size_t WS_XF = 0;
constexpr size_t WS_T1 = WS_XF + 2048 * 640 * 2;
constexpr size_t WS_RS = WS_T1 + 2048 * 640 * 2;
constexpr size_t WS_WT = WS_RS + 2048 * 4;
constexpr size_t WS_AF = WS_WT + 321 * 2048 * 4;
constexpr size_t WS_BF = WS_AF + 32 * 68 * 4096;
constexpr size_t WS_END = WS_BF + 8 * 68 * 5120;

DI int s_of_rho(int rho) {
  const int t = rho >> 10, ks = (rho >> 9) & 1, h = (rho >> 8) & 1, r = (rho >> 3) & 31, jh = (rho >> 2) & 1, jl = rho & 3;
  return 64 * (16 * ks + 8 * jh + 4 * h + jl) + 32 * t + r;
}
DI float bf_lo(unsigned u) { return __uint_as_float(u << 16); }
DI float bf_hi(unsigned u) { return __uint_as_float(u & 0xffff0000u); }
DI unsigned pk_bf(float a, float b) { f32x2 f = {a, b}; return __builtin_bit_cast(unsigned, __builtin_convertvector(f, bf16x2)); }
DI unsigned pk_h(float a, float b) { f32x2 f = {a, b}; return __builtin_bit_cast(unsigned, __builtin_convertvector(f, f16x2)); }
DI float rnd_bf(float a) { return bf_lo(pk_bf(a, 0.f)); }
#define MFMA16_BF(a, b, c) __builtin_amdgcn_mfma_f32_16x16x32_bf16(__builtin_bit_cast(bf16x8, a), __builtin_bit_cast(bf16x8, b), c, 0, 0, 0)
#define MFMA32_BF(a, b, c) __builtin_amdgcn_mfma_f32_32x32x16_bf16(__builtin_bit_cast(bf16x8, a), __builtin_bit_cast(bf16x8, b), c, 0, 0, 0)
#define MFMA32_H(a, b, c) __builtin_amdgcn_mfma_f32_32x32x16_f16(__builtin_bit_cast(f16x8, a), __builtin_bit_cast(f16x8, b), c, 0, 0, 0)
DI u32x2 tr_read(const char* p) {
  s16x4 v = __builtin_amdgcn_ds_read_tr16_b64_v4i16((LDS_AS s16x4*)(p));
  return __builtin_bit_cast(u32x2, v);
}
#define WAIT_V(n) asm volatile("s_waitcnt vmcnt(%0)" ::"n"(n) : "memory")
#define WAIT_L(n) asm volatile("s_waitcnt lgkmcnt(%0)" ::"n"(n) : "memory")
#define SCHED() __builtin_amdgcn_sched_barrier(0)

__global__ __launch_bounds__(256, 2) void k1(const u16* __restrict__ x, const u16* __restrict__ w_r, const u16* __restrict__ w_i,
                                             char* __restrict__ ws) {
  __shared__ __attribute__((aligned(16))) char sm[61440];
  const int b = blockIdx.x, tid = threadIdx.x, l = tid & 63;
  const int w = __builtin_amdgcn_readfirstlane(tid >> 6);
  if (b < 256) {
    const int xcd = b & 7, idx = b >> 3;
    const int panel = (xcd >> 1) * 8 + (idx & 7), ntg = (xcd & 1) * 4 + (idx >> 3);
    const u32x4* bp = (const u32x4*)&g_RF.b[ntg * 20 + w].v[0] + l;
    const u32x4* ap[4];
#pragma unroll
    for (int mt = 0; mt < 4; ++mt) ap[mt] = (const u32x4*)(x + s_of_rho(panel * 64 + mt * 16 + (l & 15)) * 640 + w * 32 + (l >> 4) * 8);
    u32x4 A[5][4], B[5][5];
#pragma unroll
    for (int i = 0; i < 5; ++i) {
#pragma unroll
      for (int mt = 0; mt < 4; ++mt) A[i][mt] = ap[mt][i * 16];
#pragma unroll
      for (int nt = 0; nt < 5; ++nt) B[i][nt] = bp[i * 4 * 320 + nt * 64];
    }
    f32x4 acc[4][5];
#pragma unroll
    for (int mt = 0; mt < 4; ++mt)
#pragma unroll
      for (int nt = 0; nt < 5; ++nt) acc[mt][nt] = (f32x4){0.f, 0.f, 0.f, 0.f};
#pragma unroll
    for (int i = 0; i < 5; ++i)
#pragma unroll
      for (int mt = 0; mt < 4; ++mt)
#pragma unroll
        for (int nt = 0; nt < 5; ++nt) acc[mt][nt] = MFMA16_BF(A[i][mt], B[i][nt], acc[mt][nt]);
    f32x4* P = (f32x4*)sm;
    f32x4 own[5];
#pragma unroll
    for (int mt = 0; mt < 4; ++mt) {
      if (mt == w) {
#pragma unroll
        for (int nt = 0; nt < 5; ++nt) own[nt] = acc[mt][nt];
      } else {
        const int src = (w - mt - 1) & 3;
#pragma unroll
        for (int nt = 0; nt < 5; ++nt) P[((mt * 3 + src) * 5 + nt) * 64 + l] = acc[mt][nt];
      }
    }
    __syncthreads();
    u16* T1 = (u16*)(ws + WS_T1);
#pragma unroll
    for (int nt = 0; nt < 5; ++nt) {
      f32x4 v = own[nt];
#pragma unroll
      for (int src = 0; src < 3; ++src) v += P[((w * 3 + src) * 5 + nt) * 64 + l];
      const int kk = ntg * 80 + nt * 16 + (l & 15);
      u32x2 o = {pk_bf(v[0], v[1]), pk_bf(v[2], v[3])};
      if (kk != 0) *(u32x2*)(T1 + kk * 2048 + panel * 64 + w * 16 + (l >> 4) * 4) = o;
    }
  } else if (b < 384) {
    const int rho = (b - 256) * 16 + w * 4 + (l >> 4), i = l & 15;
    const u32x4* xp = (const u32x4*)(x + s_of_rho(rho) * 640) + i;
    u32x4 v[5];
#pragma unroll
    for (int c = 0; c < 5; ++c) v[c] = xp[c * 16];
    float f[40];
    float s = 0.f;
#pragma unroll
    for (int c = 0; c < 5; ++c)
#pragma unroll
      for (int j = 0; j < 4; ++j) { f[c * 8 + 2 * j] = bf_lo(v[c][j]); f[c * 8 + 2 * j + 1] = bf_hi(v[c][j]); }
#pragma unroll
    for (int e = 0; e < 40; ++e) s += f[e];
#pragma unroll
    for (int m = 1; m < 16; m <<= 1) s += __shfl_xor(s, m, 16);
    const float mean = s * (1.f / 640.f);
    float q = 0.f;
#pragma unroll
    for (int e = 0; e < 40; ++e) { const float d = f[e] - mean; q += d * d; }
#pragma unroll
    for (int m = 1; m < 16; m <<= 1) q += __shfl_xor(q, m, 16);
    const float mb = rnd_bf(mean), vb = rnd_bf(q * (1.f / 640.f));
    const float sd = rnd_bf(sqrtf(rnd_bf(vb + 1e-5f)));
    const float rs = 1.f / sd;
    float dc = 0.f;
    char* XF = ws + WS_XF;
#pragma unroll
    for (int c = 0; c < 5; ++c) {
      u32x4 o;
#pragma unroll
      for (int j = 0; j < 4; ++j) {
        const unsigned cc = pk_bf(f[c * 8 + 2 * j] - mb, f[c * 8 + 2 * j + 1] - mb);
        const float c0 = bf_lo(cc), c1 = bf_hi(cc);
        dc += c0 + c1;
        o[j] = pk_bf(c0 * rs, c1 * rs);
      }
      const int ch = c * 16 + i;
      *(u32x4*)(XF + ((rho >> 5) * 40 + (ch >> 1)) * 1024 + (ch & 1) * 512 + (rho & 31) * 16) = o;
    }
#pragma unroll
    for (int m = 1; m < 16; m <<= 1) dc += __shfl_xor(dc, m, 16);
    if (i == 0) {
      ((float*)(ws + WS_RS))[rho] = rs;
      ((u16*)(ws + WS_T1))[rho] = (u16)(pk_bf(dc, 0.f) & 0xffffu);
    }
  } else {
    const int blk = b - 384, a = blk & 3, bq = blk >> 2;
    for (int e = tid; e < 2568; e += 256) {
      const int seg = e / 321, o = e - seg * 321, arr = seg >> 2, i = seg & 3;
      const u16* src = (arr ? w_i : w_r) + (32 * (4 * bq + i) + 8 * a) * 321;
      *(u32x4*)(sm + seg * 5136 + o * 16) = ((const u32x4*)src)[o];
    }
    __syncthreads();
    const u16* L = (const u16*)sm;
    unsigned* WT = (unsigned*)(ws + WS_WT) + (bq >> 3) * 1024 + ((bq >> 1) & 3) * 256 + (8 * a + 32 * (bq & 1)) * 4;
    for (int e = tid; e < 2568; e += 256) {
      const int k1l = e & 7, c = e >> 3;
      u32x4 o;
#pragma unroll
      for (int i = 0; i < 4; ++i) o[i] = (unsigned)L[i * 2568 + k1l * 321 + c] | ((unsigned)L[(4 + i) * 2568 + k1l * 321 + c] << 16);
      *(u32x4*)(WT + c * 2048 + k1l * 4) = o;
    }
  }
}

constexpr int STAGE_B = 49152;
constexpr int ZPITCH = 4096 + 64;

DI void pack_h(const f32x16& v, u32x4 (&o)[2]) {
#pragma unroll
  for (int s = 0; s < 2; ++s)
#pragma unroll
    for (int j = 0; j < 4; ++j) o[s][j] = pk_h(v[8 * s + 2 * j], v[8 * s + 2 * j + 1]);
}
DI u32x4 neg_h(u32x4 v) { return v ^ 0x80008000u; }

__global__ __launch_bounds__(512, 2) void k2(const u16* __restrict__ wg, const u16* __restrict__ wu, const u16* __restrict__ wd,
                                             char* __restrict__ ws) {
  __shared__ __attribute__((aligned(1024))) char sm[3 * STAGE_B];
  const int b = blockIdx.x, tid = threadIdx.x, l = tid & 63;
  const int w = __builtin_amdgcn_readfirstlane(tid >> 6);
  const int xcd = b & 7, idx = b >> 3;
  if (idx < 24) {
    const int panel = (xcd >> 2) * 4 + (idx & 3), ft = (xcd & 3) * 6 + (idx >> 2);
    const char* xsrc = ws + WS_XF + (size_t)(panel * 8 + w) * 40 * 1024 + l * 16;
    const int wrow = 8 * w + (l >> 3);
    const int woff = wrow * 1536 + ft * 64 + (((l & 7) ^ (((wrow >> 1) & 1) << 2)) * 8);
    const u16* gsrc = wg + woff;
    const u16* usrc = wu + woff;
#define STAGE(buf, t)                                                                                                                  \
  do {                                                                                                                                 \
    char* sb_ = sm + (buf) * STAGE_B;                                                                                                  \
    _Pragma("unroll") for (int i_ = 0; i_ < 4; ++i_)                                                                                   \
        __builtin_amdgcn_global_load_lds((const unsigned*)(xsrc + ((t) * 4 + i_) * 1024), (LDS_AS unsigned*)(sb_ + (w * 4 + i_) * 1024), 16, 0, 0); \
    __builtin_amdgcn_global_load_lds((const unsigned*)(gsrc + (t) * 64 * 1536), (LDS_AS unsigned*)(sb_ + 32768 + w * 1024), 16, 0, 0);  \
    __builtin_amdgcn_global_load_lds((const unsigned*)(usrc + (t) * 64 * 1536), (LDS_AS unsigned*)(sb_ + 40960 + w * 1024), 16, 0, 0);  \
  } while (0)
    const int wr = w >> 1, fh = w & 1;
    const int h = l >> 5, q = (l & 15) >> 2;
    const int xrd = wr * 8192 + l * 16;
    const int wrd = 32768 + (8 * h + q) * 128 + ((fh * 64 + ((l >> 4) & 1) * 32 + (l & 3) * 8) ^ (((q >> 1) & 1) << 6));
    f32x16 acc[2][2];
#pragma unroll
    for (int g = 0; g < 2; ++g)
#pragma unroll
      for (int r = 0; r < 2; ++r)
#pragma unroll
        for (int e = 0; e < 16; ++e) acc[g][r][e] = 0.f;
    STAGE(0, 0);
    STAGE(1, 1);
#pragma unroll
    for (int t = 0; t < 10; ++t) {
      if (t < 9) WAIT_V(6); else WAIT_V(0);
      __builtin_amdgcn_s_barrier();
      SCHED();
      if (t + 2 < 10) STAGE((t + 2) % 3, t + 2);
      const char* sb = sm + (t % 3) * STAGE_B;
#pragma unroll
      for (int ks = 0; ks < 4; ++ks) {
        u32x4 xf[2], wf[2];
#pragma unroll
        for (int r = 0; r < 2; ++r) xf[r] = *(const u32x4*)(sb + xrd + (r * 4 + ks) * 1024);
#pragma unroll
        for (int g = 0; g < 2; ++g) {
          const u32x2 lo = tr_read(sb + wrd + g * 8192 + ks * 2048), hi = tr_read(sb + wrd + g * 8192 + ks * 2048 + 512);
          wf[g] = (u32x4){lo[0], lo[1], hi[0], hi[1]};
        }
#pragma unroll
        for (int g = 0; g < 2; ++g)
#pragma unroll
          for (int r = 0; r < 2; ++r) acc[g][r] = MFMA32_BF(wf[g], xf[r], acc[g][r]);
      }
      WAIT_L(0);
      SCHED();
    }
#undef STAGE
    char* AF = ws + WS_AF + ((size_t)(panel * 4 + wr) * 68 + ft * 2 + fh) * 4096 + ((l >> 4) & 1) * 1024 + h * 256 + (l & 15) * 16;
#pragma unroll
    for (int r = 0; r < 2; ++r)
#pragma unroll
      for (int s = 0; s < 2; ++s) {
        u32x4 o;
#pragma unroll
        for (int j = 0; j < 4; ++j) {
          const unsigned gg = pk_bf(acc[0][r][8 * s + 2 * j], acc[0][r][8 * s + 2 * j + 1]);
          const unsigned uu = pk_bf(acc[1][r][8 * s + 2 * j], acc[1][r][8 * s + 2 * j + 1]);
          o[j] = pk_bf(bf_lo(gg) * bf_lo(uu), bf_hi(gg) * bf_hi(uu));
        }
        *(u32x4*)(AF + r * 2048 + s * 512) = o;
      }
    return;
  }
  const int aux = (idx - 24) * 8 + xcd;
  if (aux < 41) {
    int c, rre, rim, zrow;
    bool act = true;
    if (aux == 0) {
      if (w == 0) { c = 0; rre = 0; rim = -1; zrow = 0; }
      else if (w == 4) { c = 320; rre = 1; rim = -1; zrow = 1; }
      else { c = w; rre = 2 * w; rim = 2 * w + 1; zrow = 2 * w; act = w < 4; }
    } else if (aux == 40) { c = w; rre = 2 * w; rim = 2 * w + 1; zrow = 2 * w; act = w >= 4; }
    else { c = 8 * aux + w; rre = 2 * c; rim = 2 * c + 1; zrow = 2 * w; }
    {
      const u32x4* tsrc = (const u32x4*)&g_TF;
#pragma unroll
      for (int i = 0; i < 5; ++i) *(u32x4*)(sm + (i * 512 + tid) * 16) = tsrc[i * 512 + tid];
    }
    char* zi = sm + 40960;
    u32x4 xr[4], xi[4], wv[8];
    f32x4 rs[8];
    if (act) {
      const u32x4* pr = (const u32x4*)(ws + WS_T1 + (size_t)rre * 4096) + l;
      const u32x4* pi = (const u32x4*)(ws + WS_T1 + (size_t)(rim < 0 ? 0 : rim) * 4096) + l;
      const f32x4* ps = (const f32x4*)(ws + WS_RS) + l * 2;
      const u32x4* pw = (const u32x4*)(ws + WS_WT + (size_t)c * 8192) + l;
#pragma unroll
      for (int i = 0; i < 4; ++i) { xr[i] = pr[i * 64]; xi[i] = pi[i * 64]; rs[2 * i] = ps[i * 128]; rs[2 * i + 1] = ps[i * 128 + 1]; }
#pragma unroll
      for (int i = 0; i < 8; ++i) wv[i] = pw[i * 64];
    }
    __syncthreads();
    if (act) {
      const u32x4* G1 = (const u32x4*)sm + l;
      const u32x4* F2 = (const u32x4*)(sm + 8192) + l;
      const u32x4* G3 = (const u32x4*)(sm + 24576) + l;
      const f32x4* TW = (const f32x4*)(sm + 32768) + l;
      const float im_on = rim < 0 ? 0.f : 1.f;
      u32x4 ar[4], ai[4];
#pragma unroll
      for (int i = 0; i < 4; ++i)
#pragma unroll
        for (int j = 0; j < 4; ++j) {
          const float s0 = rs[2 * i + (j >> 1)][(j & 1) * 2], s1 = rs[2 * i + (j >> 1)][(j & 1) * 2 + 1];
          ar[i][j] = pk_h(bf_lo(xr[i][j]) * s0, bf_hi(xr[i][j]) * s1);
          ai[i][j] = rim < 0 ? 0u : pk_h(bf_lo(xi[i][j]) * s0, bf_hi(xi[i][j]) * s1);
        }
      (void)im_on;
      f32x16 zero;
#pragma unroll
      for (int e = 0; e < 16; ++e) zero[e] = 0.f;
      f32x4 tw[8];
#pragma unroll
      for (int i = 0; i < 8; ++i) tw[i] = TW[i * 64];
      u32x4 br[2][2], bi[2][2];
#pragma unroll
      for (int t = 0; t < 2; ++t) {
        f32x16 dr = zero, di = zero;
#pragma unroll
        for (int ks = 0; ks < 2; ++ks) {
          const u32x4 C = G1[((t * 2 + ks) * 2) * 64], S = G1[((t * 2 + ks) * 2 + 1) * 64];
          dr = MFMA32_H(ar[t * 2 + ks], C, dr);
          di = MFMA32_H(ai[t * 2 + ks], C, di);
          dr = MFMA32_H(ai[t * 2 + ks], S, dr);
          di = MFMA32_H(neg_h(ar[t * 2 + ks]), S, di);
        }
        f32x16 er, ei;
#pragma unroll
        for (int e = 0; e < 16; ++e) {
          const float cc = tw[e >> 1][(e & 1) * 2], ss = tw[e >> 1][(e & 1) * 2 + 1];
          er[e] = dr[e] * cc + di[e] * ss;
          ei[e] = di[e] * cc - dr[e] * ss;
        }
        pack_h(er, br[t]);
        pack_h(ei, bi[t]);
      }
      f32x16 yr[2], yi[2];
      float mx = 0.f;
#pragma unroll
      for (int t2 = 0; t2 < 2; ++t2) {
        f32x16 xr_ = zero, xi_ = zero;
#pragma unroll
        for (int t = 0; t < 2; ++t)
#pragma unroll
          for (int s2 = 0; s2 < 2; ++s2) {
            const u32x4 C = F2[(((t2 * 2 + t) * 2 + s2) * 2) * 64], S = F2[(((t2 * 2 + t) * 2 + s2) * 2 + 1) * 64];
            xr_ = MFMA32_H(C, br[t][s2], xr_);
            xi_ = MFMA32_H(C, bi[t][s2], xi_);
            xr_ = MFMA32_H(S, bi[t][s2], xr_);
            xi_ = MFMA32_H(S, neg_h(br[t][s2]), xi_);
          }
#pragma unroll
        for (int e = 0; e < 16; ++e) {
          const unsigned u = wv[t2 * 4 + (e >> 2)][e & 3];
          const float wr_ = bf_lo(u), wi_ = bf_hi(u);
          yr[t2][e] = xr_[e] * wr_ - xi_[e] * wi_;
          yi[t2][e] = xr_[e] * wi_ + xi_[e] * wr_;
          mx = fmaxf(mx, fmaxf(fabsf(yr[t2][e]), fabsf(yi[t2][e])));
        }
      }
#pragma unroll
      for (int m = 1; m < 64; m <<= 1) mx = fmaxf(mx, __shfl_xor(mx, m, 64));
      int ex = (int)((__float_as_uint(mx) >> 23) & 0xffu);
      ex = ex < 16 ? 16 : (ex > 240 ? 240 : ex);
      const float up = __uint_as_float((unsigned)(261 - ex) << 23);
      const float down = __uint_as_float((unsigned)(ex - 7) << 23);
      u32x4 pr_[2][2], pi_[2][2];
#pragma unroll
      for (int t2 = 0; t2 < 2; ++t2) { pack_h(yr[t2] * up, pr_[t2]); pack_h(yi[t2] * up, pi_[t2]); }
      const float osc = (rim < 0 ? (8.f / (2048.f * 640.f)) : (16.f / (2048.f * 640.f)));
#pragma unroll
      for (int t = 0; t < 2; ++t) {
        f32x16 dr = zero, di = zero;
#pragma unroll
        for (int t2 = 0; t2 < 2; ++t2)
#pragma unroll
          for (int s2 = 0; s2 < 2; ++s2) {
            const u32x4 C = F2[(((t * 2 + t2) * 2 + s2) * 2) * 64], S = F2[(((t * 2 + t2) * 2 + s2) * 2 + 1) * 64];
            dr = MFMA32_H(pr_[t2][s2], C, dr);
            di = MFMA32_H(pi_[t2][s2], C, di);
            dr = MFMA32_H(neg_h(pi_[t2][s2]), S, dr);
            di = MFMA32_H(pr_[t2][s2], S, di);
          }
        f32x16 er, ei;
#pragma unroll
        for (int e = 0; e < 16; ++e) {
          const float cc = tw[e >> 1][(e & 1) * 2], ss = tw[e >> 1][(e & 1) * 2 + 1];
          er[e] = dr[e] * cc - di[e] * ss;
          ei[e] = di[e] * cc + dr[e] * ss;
        }
        u32x4 qr[2], qi[2];
        pack_h(er, qr);
        pack_h(ei, qi);
        f32x16 zr = zero, zq = zero;
#pragma unroll
        for (int ks = 0; ks < 2; ++ks) {
          const u32x4 C = G3[((t * 2 + ks) * 2) * 64], S = G3[((t * 2 + ks) * 2 + 1) * 64];
          zr = MFMA32_H(C, qr[ks], zr);
          zq = MFMA32_H(C, qi[ks], zq);
          zr = MFMA32_H(S, neg_h(qi[ks]), zr);
          zq = MFMA32_H(S, qr[ks], zq);
        }
#pragma unroll
        for (int ks = 0; ks < 2; ++ks) {
          u32x4 o0, o1;
#pragma unroll
          for (int j = 0; j < 4; ++j) {
            o0[j] = pk_bf(zr[8 * ks + 2 * j] * osc * down, zr[8 * ks + 2 * j + 1] * osc * down);
            o1[j] = pk_bf(zq[8 * ks + 2 * j] * osc * down, zq[8 * ks + 2 * j + 1] * osc * down);
          }
          *(u32x4*)(zi + zrow * ZPITCH + (t * 2 + ks) * 1024 + l * 16) = o0;
          if (rim >= 0) *(u32x4*)(zi + (zrow + 1) * ZPITCH + (t * 2 + ks) * 1024 + l * 16) = o1;
        }
      }
    }
    __syncthreads();
    {
      const int beff = aux == 40 ? 0 : aux;
      const int g = l >> 5, mlo = (l >> 4) & 1, q = (l & 15) >> 2, p = l & 3;
      const bool st = aux == 0 ? (g == 0) : (aux == 40 ? (g == 1) : true);
      const char* rd = zi + (8 * g + q) * ZPITCH + mlo * 32 + p * 8;
      char* AF = ws + WS_AF + (size_t)(48 + (beff >> 1)) * 4096 + mlo * 1024 + (2 * (beff & 1) + g) * 256 + (l & 15) * 16;
#pragma unroll
      for (int i = 0; i < 8; ++i) {
        const int u = w * 8 + i;
        const u32x2 lo = tr_read(rd + u * 64), hi = tr_read(rd + u * 64 + 4 * ZPITCH);
        const u32x4 o = {lo[0], lo[1], hi[0], hi[1]};
        if (st) *(u32x4*)(AF + (size_t)(u >> 1) * 68 * 4096 + (u & 1) * 2048) = o;
      }
    }
    return;
  }
  {
    const int j0 = aux - 41;
    char* BF = ws + WS_BF;
    u16* ct = (u16*)(sm + 65536);
    for (int e = tid; e < 640; e += 512) ct[e] = g_c640.v[e];
    for (int F = j0; F < 48; F += 23) {
      __syncthreads();
      const u32x4* src = (const u32x4*)(wd + (size_t)F * 32 * 640);
#pragma unroll
      for (int i = 0; i < 5; ++i) {
        const int e = i * 512 + tid, row = e / 80, ch = e - row * 80;
        *(u32x4*)(sm + row * 1312 + ch * 16) = src[e];
      }
      __syncthreads();
      const int g4 = l >> 4, q = (l & 15) >> 2, p = l & 3;
      const char* rd = sm + (16 * (g4 >> 1) + 4 * (g4 & 1) + q) * 1312 + p * 8;
#pragma unroll
      for (int i = 0; i < 5; ++i) {
        const int ntile = w * 5 + i;
        const u32x2 lo = tr_read(rd + ntile * 32), hi = tr_read(rd + ntile * 32 + 8 * 1312);
        *(u32x4*)(BF + ((size_t)(w * 68 + F) * 5 + i) * 1024 + l * 16) = (u32x4){lo[0], lo[1], hi[0], hi[1]};
      }
    }
    for (int u = j0 * 8 + w; u < 800; u += 23 * 8) {
      const int ntile = u / 20, Fz = u - ntile * 20, ntg = ntile / 5, nt = ntile - ntg * 5;
      const int d = ntile * 16 + (l & 15), c0 = 16 * Fz + 4 * (l >> 4);
      int ic = (c0 * d) % 640;
      u32x4 o;
#pragma unroll
      for (int j = 0; j < 4; ++j) {
        int is = ic + 160; if (is >= 640) is -= 640;
        if (c0 + j == 0) is = (d & 1) * 320;
        o[j] = (unsigned)ct[ic] | ((unsigned)ct[is] << 16);
        ic += d; if (ic >= 640) ic -= 640;
      }
      *(u32x4*)(BF + ((size_t)(ntg * 68 + 48 + Fz) * 5 + nt) * 1024 + l * 16) = o;
    }
  }
}

__global__ __launch_bounds__(256, 1) void k3(const u16* __restrict__ x, const char* __restrict__ ws, u16* __restrict__ out) {
  __shared__ __attribute__((aligned(16))) char sm[61440];
  const int b = blockIdx.x, tid = threadIdx.x, l = tid & 63;
  const int w = __builtin_amdgcn_readfirstlane(tid >> 6);
  const int xcd = b & 7, idx = b >> 3;
  const int panel = (xcd >> 1) * 8 + (idx & 7), ntg = (xcd & 1) * 4 + (idx >> 3);
  const u32x4* ap = (const u32x4*)(ws + WS_AF + ((size_t)panel * 68 + w) * 4096) + l;
  const u32x4* bp = (const u32x4*)(ws + WS_BF + ((size_t)ntg * 68 + w) * 5120) + l;
  const int orow = s_of_rho(panel * 64 + w * 16 + (l & 15)) * 640 + ntg * 80 + (l >> 4) * 4;
  u32x2 xres[5];
#pragma unroll
  for (int nt = 0; nt < 5; ++nt) xres[nt] = *(const u32x2*)(x + orow + nt * 16);
  f32x4 acc[4][5];
#pragma unroll
  for (int mt = 0; mt < 4; ++mt)
#pragma unroll
    for (int nt = 0; nt < 5; ++nt) acc[mt][nt] = (f32x4){0.f, 0.f, 0.f, 0.f};
  constexpr int D = 5;
  u32x4 A[D][4], B[D][5];
#pragma unroll
  for (int i = 0; i < D; ++i) {
#pragma unroll
    for (int mt = 0; mt < 4; ++mt) A[i][mt] = ap[i * 1024 + mt * 64];
#pragma unroll
    for (int nt = 0; nt < 5; ++nt) B[i][nt] = bp[i * 1280 + nt * 64];
  }
  SCHED();
#pragma unroll
  for (int i = 0; i < 17; ++i) {
#pragma unroll
    for (int mt = 0; mt < 4; ++mt)
#pragma unroll
      for (int nt = 0; nt < 5; ++nt) acc[mt][nt] = MFMA16_BF(B[i % D][nt], A[i % D][mt], acc[mt][nt]);
    SCHED();
    if (i + D < 17) {
#pragma unroll
      for (int mt = 0; mt < 4; ++mt) A[i % D][mt] = ap[(i + D) * 1024 + mt * 64];
#pragma unroll
      for (int nt = 0; nt < 5; ++nt) B[i % D][nt] = bp[(i + D) * 1280 + nt * 64];
    }
    SCHED();
  }
  f32x4* P = (f32x4*)sm;
  f32x4 own[5];
#pragma unroll
  for (int mt = 0; mt < 4; ++mt) {
    if (mt == w) {
#pragma unroll
      for (int nt = 0; nt < 5; ++nt) own[nt] = acc[mt][nt];
    } else {
      const int src = (w - mt - 1) & 3;
#pragma unroll
      for (int nt = 0; nt < 5; ++nt) P[((mt * 3 + src) * 5 + nt) * 64 + l] = acc[mt][nt];
    }
  }
  __syncthreads();
#pragma unroll
  for (int nt = 0; nt < 5; ++nt) {
    f32x4 v = own[nt];
#pragma unroll
    for (int src = 0; src < 3; ++src) v += P[((w * 3 + src) * 5 + nt) * 64 + l];
    u32x2 o = {pk_bf(v[0] + bf_lo(xres[nt][0]), v[1] + bf_hi(xres[nt][0])), pk_bf(v[2] + bf_lo(xres[nt][1]), v[3] + bf_hi(xres[nt][1]))};
    *(u32x2*)(out + orow + nt * 16) = o;
  }
}

extern "C" void kernel_launch(void* const* d_in, const int* in_sizes, int n_in, void* d_out, int out_size, void* d_ws, size_t ws_size,
                              hipStream_t stream) {
  const u16* x = (const u16*)d_in[0];
  const u16* w_r = (const u16*)d_in[1];
  const u16* w_i = (const u16*)d_in[2];
  const u16* wg = (const u16*)d_in[4];
  const u16* wu = (const u16*)d_in[5];
  const u16* wd = (const u16*)d_in[6];
  char* ws = (char*)d_ws;
  k1<<<448, 256, 0, stream>>>(x, w_r, w_i, ws);
  k2<<<256, 512, 0, stream>>>(wg, wu, wd, ws);
  k3<<<256, 256, 0, stream>>>(x, ws, (u16*)d_out);
}
```

```cpp
#include <hip/hip_runtime.h>
#include <hip/hip_bf16.h>

typedef unsigned short u16;
typedef __attribute__((ext_vector_type(8))) __bf16 bf16x8;
typedef __attribute__((ext_vector_type(2))) __bf16 bf16x2;
typedef __attribute__((ext_vector_type(8))) _Float16 f16x8;
typedef __attribute__((ext_vector_type(4))) _Float16 f16x4;
typedef __attribute__((ext_vector_type(2))) _Float16 f16x2;
typedef __attribute__((ext_vector_type(4))) short s16x4;
typedef __attribute__((ext_vector_type(2))) float f32x2;
typedef __attribute__((ext_vector_type(4))) float f32x4;
typedef __attribute__((ext_vector_type(16))) float f32x16;
typedef __attribute__((ext_vector_type(4))) unsigned u32x4;
typedef __attribute__((ext_vector_type(2))) unsigned u32x2;
typedef __amdgpu_buffer_rsrc_t rsrc_t;
#define DI __device__ __forceinline__
#define LDS_AS __attribute__((address_space(3)))

namespace tb {
constexpr double kPi = 3.14159265358979323846264338327950288;
constexpr double sinT(double x) { double x2 = x * x, t = x, s = x; for (int i = 1; i < 12; ++i) { t *= -x2 / ((2 * i) * (2 * i + 1)); s += t; } return s; }
constexpr double cosT(double x) { double x2 = x * x, t = 1, s = 1; for (int i = 1; i < 12; ++i) { t *= -x2 / ((2 * i - 1) * (2 * i)); s += t; } return s; }
constexpr double cos2pi(int q, int den) {
  q %= den; if (2 * q > den) q = den - q;
  double sg = 1; if (4 * q > den) { q = den / 2 - q; sg = -1; }
  if (8 * q > den) return sg * sinT(2 * kPi * (den / 4 - q) / den);
  return sg * cosT(2 * kPi * q / den);
}
constexpr u16 bfbits(double d) { unsigned u = __builtin_bit_cast(unsigned, (float)d); u += 0x7fffu + ((u >> 16) & 1u); return (u16)(u >> 16); }
constexpr u16 hbits(double d) { return __builtin_bit_cast(u16, (_Float16)d); }
constexpr int krow(int s, int h, int j) { return 16 * s + 8 * (j >> 2) + 4 * h + (j & 3); }
template <int N> struct CT { double c[N]; };
template <int N> constexpr CT<N> mkC() { CT<N> c{}; for (int i = 0; i < N; ++i) c.c[i] = cos2pi(i, N); return c; }
constexpr CT<64> c64 = mkC<64>();
constexpr CT<80> c80 = mkC<80>();
constexpr double m80(int o, int d) { return o == 1 ? c80.c[(40 * d) % 80] : c80.c[((o >> 1) * d + ((o & 1) ? 20 : 0)) % 80]; }
struct alignas(16) TabF {
  u16 g1[2][2][3][64][8];
  u16 g3[2][2][3][64][8];
  u16 g8[64][2][4];
};
constexpr TabF mkTabF() {
  TabF T{};
  for (int t = 0; t < 2; ++t) for (int ks = 0; ks < 2; ++ks) for (int l = 0; l < 64; ++l) for (int j = 0; j < 8; ++j) {
    const int r = l & 31, k = krow(ks, l >> 5, j);
    const int q1 = (r * (2 * k + t)) & 63, q3 = (k * (2 * r + t)) & 63;
    T.g1[t][ks][0][l][j] = hbits(c64.c[q1] * 0.25); T.g1[t][ks][1][l][j] = hbits(c64.c[(q1 + 48) & 63] * 0.25); T.g1[t][ks][2][l][j] = hbits(c64.c[(q1 + 16) & 63] * 0.25);
    T.g3[t][ks][0][l][j] = hbits(c64.c[q3] * 0.25); T.g3[t][ks][1][l][j] = hbits(c64.c[(q3 + 48) & 63] * 0.25); T.g3[t][ks][2][l][j] = hbits(c64.c[(q3 + 16) & 63] * 0.25);
  }
  for (int l = 0; l < 64; ++l) for (int e = 0; e < 4; ++e) {
    const int m = l & 15, k = 4 * (l >> 4) + e, q = (8 * (m >> 1) * (k >> 1)) & 63;
    const double C = c64.c[q], S = c64.c[(q + 48) & 63];
    T.g8[l][0][e] = hbits(0.25 * ((m & 1) == (k & 1) ? C : ((m & 1) ? -S : S)));
    T.g8[l][1][e] = bfbits(239. / 256. * ((m & 1) == (k & 1) ? C : ((k & 1) ? -S : S)));
  }
  return T;
}
struct alignas(16) Tab80 {
  u16 f[3][5][64][8];
  u16 pad[512];
  u16 i[8][5][64][4];
};
constexpr Tab80 mkTab80() {
  Tab80 T{};
  for (int n = 0; n < 3; ++n) for (int ks = 0; ks < 5; ++ks) for (int l = 0; l < 64; ++l) for (int j = 0; j < 8; ++j)
    T.f[n][ks][l][j] = 32 * n + (l & 31) < 80 ? hbits(m80(32 * n + (l & 31), 16 * ks + 8 * (l >> 5) + j)) : (u16)0;
  for (int n = 0; n < 8; ++n) for (int ks = 0; ks < 5; ++ks) for (int l = 0; l < 64; ++l) for (int e = 0; e < 4; ++e)
    T.i[n][ks][l][e] = (l & 15) < 10 ? bfbits(m80(16 * ks + 4 * (l >> 4) + e, 10 * n + (l & 15))) : (u16)0;
  return T;
}
}
static __device__ const tb::TabF g_TF = tb::mkTabF();
static __device__ const tb::Tab80 g_T80 = tb::mkTab80();

constexpr unsigned WS_EP = 0;
constexpr unsigned WS_F1 = 256;
constexpr unsigned WS_F2 = WS_F1 + 512;
constexpr unsigned WS_FA = WS_F2 + 512;
constexpr unsigned WS_ST = 4096;
constexpr unsigned RP = 4096 + 256;
constexpr unsigned WS_UB = WS_ST + 2048 * 8;
constexpr unsigned WS_WT = WS_UB + 640 * RP;
constexpr unsigned WS_AF = WS_WT + 41 * 2 * 2048 * 16;
constexpr unsigned WS_BF = WS_AF + 32 * 48 * 4096;
constexpr unsigned WS_VT = WS_BF + 8 * 48 * 5120;
constexpr unsigned WS_END = WS_VT + 640 * RP;

DI int s_of_rho(int rho) {
  const int t = rho >> 10, ks = (rho >> 9) & 1, h = (rho >> 8) & 1, r = (rho >> 3) & 31, jh = (rho >> 2) & 1, jl = rho & 3;
  return 64 * (16 * ks + 8 * jh + 4 * h + jl) + 32 * t + r;
}
DI float bf_lo(unsigned u) { return __uint_as_float(u << 16); }
DI float bf_hi(unsigned u) { return __uint_as_float(u & 0xffff0000u); }
DI unsigned pk_bf(float a, float b) { f32x2 f = {a, b}; return __builtin_bit_cast(unsigned, __builtin_convertvector(f, bf16x2)); }
DI unsigned pk_h(float a, float b) { f32x2 f = {a, b}; return __builtin_bit_cast(unsigned, __builtin_convertvector(f, f16x2)); }
DI float rnd_bf(float a) { return bf_lo(pk_bf(a, 0.f)); }
#define MFMA16_BF(a, b, c) __builtin_amdgcn_mfma_f32_16x16x32_bf16(__builtin_bit_cast(bf16x8, a), __builtin_bit_cast(bf16x8, b), c, 0, 0, 0)
#define MFMA32_BF(a, b, c) __builtin_amdgcn_mfma_f32_32x32x16_bf16(__builtin_bit_cast(bf16x8, a), __builtin_bit_cast(bf16x8, b), c, 0, 0, 0)
#define MFMA32_H(a, b, c) __builtin_amdgcn_mfma_f32_32x32x16_f16(__builtin_bit_cast(f16x8, a), __builtin_bit_cast(f16x8, b), c, 0, 0, 0)
#define MFMA16K16_H(a, b, c) __builtin_amdgcn_mfma_f32_16x16x16f16(__builtin_bit_cast(f16x4, a), __builtin_bit_cast(f16x4, b), c, 0, 0, 0)
#define MFMA16K16_BF(a, b, c) __builtin_amdgcn_mfma_f32_16x16x16bf16_1k(__builtin_bit_cast(s16x4, a), __builtin_bit_cast(s16x4, b), c, 0, 0, 0)
DI u32x2 tr_read(const char* p) {
  s16x4 v = __builtin_amdgcn_ds_read_tr16_b64_v4i16((LDS_AS s16x4*)(p));
  return __builtin_bit_cast(u32x2, v);
}
DI rsrc_t mk_rsrc(const void* p, unsigned bytes) { return __builtin_amdgcn_make_buffer_rsrc((void*)p, 0, bytes, 0x00020000); }
#define ST128(v, r, off) __builtin_amdgcn_raw_buffer_store_b128(v, r, off, 0, 16)
#define ST64(v, r, off) __builtin_amdgcn_raw_buffer_store_b64(v, r, off, 0, 16)
#define WAIT_V(n) asm volatile("s_waitcnt vmcnt(%0)" ::"n"(n) : "memory")
#define WAIT_L(n) asm volatile("s_waitcnt lgkmcnt(%0)" ::"n"(n) : "memory")
#define SCHED() __builtin_amdgcn_sched_barrier(0)
#define BARRIER() do { WAIT_L(0); __builtin_amdgcn_s_barrier(); asm volatile("" ::: "memory"); } while (0)
DI void dma1(unsigned voff, const void* sbase, unsigned lds) {
  unsigned keep;
  asm volatile("s_mov_b32 %0, m0\n\ts_mov_b32 m0, %3\n\ts_nop 2\n\tglobal_load_lds_dwordx4 %1, %2\n\ts_mov_b32 m0, %0"
               : "=&s"(keep) : "v"(voff), "s"(sbase), "s"(lds) : "memory");
}
DI void dma4(unsigned voff, const void* sbase, unsigned lds) {
  unsigned keep;
  asm volatile("s_mov_b32 %0, m0\n\ts_mov_b32 m0, %3\n\ts_nop 2\n\tglobal_load_lds_dwordx4 %1, %2\n\tglobal_load_lds_dwordx4 %1, %2 offset:1024\n\t"
               "global_load_lds_dwordx4 %1, %2 offset:2048\n\tglobal_load_lds_dwordx4 %1, %2 offset:3072\n\ts_mov_b32 m0, %0"
               : "=&s"(keep) : "v"(voff), "s"(sbase), "s"(lds) : "memory");
}
template <int CTRL> DI unsigned dpp_u(unsigned x) { return (unsigned)__builtin_amdgcn_mov_dpp((int)x, CTRL, 0xf, 0xf, true); }
template <int CTRL> DI float dpp_f(float x) { return __uint_as_float(dpp_u<CTRL>(__float_as_uint(x))); }
DI float sum16(float x) {
  x += dpp_f<0xB1>(x); x += dpp_f<0x4E>(x); x += dpp_f<0x141>(x); x += dpp_f<0x128>(x);
  return x;
}
DI float sum_rows(float x) {
  const auto s = __builtin_amdgcn_permlane16_swap(__float_as_uint(x), __float_as_uint(x), false, false);
  x = __uint_as_float(s[0]) + __uint_as_float(s[1]);
  const auto t = __builtin_amdgcn_permlane32_swap(__float_as_uint(x), __float_as_uint(x), false, false);
  return __uint_as_float(t[0]) + __uint_as_float(t[1]);
}
typedef unsigned long long u64;
typedef __attribute__((address_space(1))) u64 gu64;
#define RLX_AGENT __ATOMIC_RELAXED, __HIP_MEMORY_SCOPE_AGENT
DI u64 ld_dev(const char* p) { return __hip_atomic_load((gu64*)p, RLX_AGENT); }
DI void st_dev(char* p, u64 v) { __hip_atomic_store((gu64*)p, v, RLX_AGENT); }
DI u64 next_epoch(u64 e) { return e * 0x9E3779B97F4A7C15ull + 0x632BE59BD9B4E019ull; }
constexpr unsigned SPIN_CAP = 1u << 18;
DI void wait_flags(const char* p, u64 tag) {
#pragma unroll 1
  for (unsigned n = 0; n < SPIN_CAP; ++n) {
    if (__all(ld_dev(p) == tag)) break;
    __builtin_amdgcn_s_sleep(1);
  }
  asm volatile("" ::: "memory");
}
#define LD128_DEV(r, off) __builtin_amdgcn_raw_buffer_load_b128(r, off, 0, 16)
#define LD64_DEV(r, off) __builtin_amdgcn_raw_buffer_load_b64(r, off, 0, 16)
DI unsigned max64(unsigned x) {
  x = max(x, dpp_u<0xB1>(x)); x = max(x, dpp_u<0x4E>(x)); x = max(x, dpp_u<0x141>(x)); x = max(x, dpp_u<0x128>(x));
  const auto s = __builtin_amdgcn_permlane16_swap(x, x, false, false);
  x = max(s[0], s[1]);
  const auto t = __builtin_amdgcn_permlane32_swap(x, x, false, false);
  return max(t[0], t[1]);
}
DI unsigned lds_addr(const char* p) { return (unsigned)(size_t)(LDS_AS const char*)p; }

#define TS_DECL
#define TS_MARK()
#define TS_END(k, role)

constexpr int STAGE_B = 49152;
constexpr int G_CS = 3 * STAGE_B;
constexpr int G_TAG = G_CS + 8192;
constexpr int A_T80 = 0;
constexpr int A_XH = 16384;
constexpr int A_UT = A_XH + 32 * 1344;
constexpr int A_W = A_UT + 4 * 10304;
constexpr int A_MX = A_W + 8 * 5152;
constexpr int A_TF = 0;
constexpr int A_E = 24576;
constexpr int KA_LDS = G_TAG + 64;
static_assert(A_MX + 64 <= KA_LDS, "lds");

DI void pack_h(const f32x16& v, u32x4 (&o)[2]) {
#pragma unroll
  for (int s = 0; s < 2; ++s)
#pragma unroll
    for (int j = 0; j < 4; ++j) o[s][j] = pk_h(v[8 * s + 2 * j], v[8 * s + 2 * j + 1]);
}
DI f16x2 as_h2(unsigned u) { return __builtin_bit_cast(f16x2, u); }
DI unsigned as_u(f16x2 v) { return __builtin_bit_cast(unsigned, v); }
template <bool INV> DI void bfly_tw(const f32x16 (&dr)[2], const f32x16 (&di)[2], const f16x2 (&tc)[8], const f16x2 (&ts)[8],
                                    u32x4 (&er)[2][2], u32x4 (&ei)[2][2]) {
#pragma unroll
  for (int e = 0; e < 8; ++e) {
    const f16x2 r0 = as_h2(pk_h(dr[0][2 * e], dr[0][2 * e + 1])), r1 = as_h2(pk_h(dr[1][2 * e], dr[1][2 * e + 1]));
    const f16x2 i0 = as_h2(pk_h(di[0][2 * e], di[0][2 * e + 1])), i1 = as_h2(pk_h(di[1][2 * e], di[1][2 * e + 1]));
#pragma unroll
    for (int t = 0; t < 2; ++t) {
      const f16x2 sr = t ? r0 - r1 : r0 + r1, si = t ? i0 - i1 : i0 + i1;
      const f16x2 a = sr * tc[e], c = si * tc[e];
      er[t][e >> 2][e & 3] = as_u(__builtin_elementwise_fma(INV ? -si : si, ts[e], a));
      ei[t][e >> 2][e & 3] = as_u(__builtin_elementwise_fma(INV ? sr : -sr, ts[e], c));
    }
  }
}
DI u32x4 swap_halves(u32x4 v) { return (u32x4){v[2], v[3], v[0], v[1]}; }
DI int e_key(int d2) { return ((d2 & 2) << 2) | ((d2 & 1) << 2) | ((d2 & 4) >> 1) | ((d2 & 2) >> 1); }
DI float dot_ones(u32x4 v, float acc) {
#pragma unroll
  for (int j = 0; j < 4; ++j) {
    const unsigned u = v[j];
    acc = __builtin_amdgcn_fdot2_f32_bf16(__builtin_bit_cast(bf16x2, u), __builtin_bit_cast(bf16x2, 0x3f803f80u), acc, false);
  }
  return acc;
}

__global__ __launch_bounds__(512, 2) void ka(const u16* __restrict__ x, const u16* __restrict__ w_r, const u16* __restrict__ w_i,
                                             const u16* __restrict__ wg, const u16* __restrict__ wu, const u16* __restrict__ wd,
                                             char* __restrict__ ws) {
  __shared__ __attribute__((aligned(1024))) char sm[KA_LDS];
  const int b = blockIdx.x, tid = threadIdx.x, l = tid & 63;
  const int w = __builtin_amdgcn_readfirstlane(tid >> 6);
  const int xcd = b & 7, idx = b >> 3;
  const unsigned smb = lds_addr(sm);
  const rsrc_t rw = mk_rsrc(ws, WS_END);
  const int g = l >> 4;
  TS_DECL TS_MARK();
  if (idx >= 8) {
    const int gi = idx - 8;
    const int panel = (xcd >> 2) * 4 + (gi & 3), ft = (xcd & 3) * 6 + (gi >> 2);
    u64 ep;
    asm volatile("global_load_dwordx2 %0, %1, %2 sc1" : "=v"(ep) : "v"(WS_EP), "s"(ws) : "memory");
    const int r8 = l >> 3, c8 = l & 7;
    const unsigned xo = (s_of_rho(panel * 256 + w) + r8) * 1280 + ((c8 ^ w) << 4);
    const unsigned woff = ((8 * w + r8) * 1536 + ft * 64) * 2 + ((c8 ^ ((((r8 >> 1) & 1) | ((w & 1) << 1)) << 1)) << 4);
#define STAGE_X(t)                                                                                                   \
  do {                                                                                                               \
    _Pragma("unroll") for (int i_ = 0; i_ < 4; ++i_)                                                                 \
        dma1(xo, (const char*)x + (t) * 128 + i_ * 10240, smb + ((t) % 3) * STAGE_B + (w * 4 + i_) * 1024); \
  } while (0)
#define STAGE_W(t)                                                                    \
  do {                                                                                \
    dma1(woff, wg + (t) * 64 * 1536, smb + ((t) % 3) * STAGE_B + 32768 + w * 1024);   \
    dma1(woff, wu + (t) * 64 * 1536, smb + ((t) % 3) * STAGE_B + 40960 + w * 1024);   \
  } while (0)
    const int wr = w >> 1, fh = w & 1;
    const int q = (l & 15) >> 2;
    const int xrd0 = (l & 7) * 4096 + (wr * 8 + ((l >> 3) & 1)) * 128 + ((g ^ (l & 7)) << 4), xrd1 = xrd0 ^ 64;
    const int wsw = ((q >> 1) & 1) | ((g & 1) << 1);
    const int wrd0 = 32768 + (8 * g + q) * 128 + (((fh * 2) ^ wsw) << 5) + (l & 3) * 8, wrd1 = wrd0 ^ 32;
#define READ_FRAGS(t, ks, pb)                                                                                        \
  do {                                                                                                               \
    const char* sb_ = sm + ((t) % 3) * STAGE_B;                                                                      \
    _Pragma("unroll") for (int i_ = 0; i_ < 4; ++i_) {                                                               \
      const char* wp_ = sb_ + ((i_ & 1) ? wrd1 : wrd0) + (i_ >> 1) * 8192 + (ks) * 4096;                             \
      const u32x2 lo_ = tr_read(wp_), hi_ = tr_read(wp_ + 512);                                                      \
      wf[pb][i_] = (u32x4){lo_[0], lo_[1], hi_[0], hi_[1]};                                                          \
      xf[pb][i_] = *(const u32x4*)(sb_ + ((ks) ? xrd1 : xrd0) + i_ * 256);                                           \
    }                                                                                                                \
  } while (0)
    f32x4 acc[4][4];
#pragma unroll
    for (int i = 0; i < 4; ++i)
#pragma unroll
      for (int r = 0; r < 4; ++r) acc[i][r] = (f32x4){0.f, 0.f, 0.f, 0.f};
    float cs[4] = {0.f, 0.f, 0.f, 0.f};
    STAGE_X(0); STAGE_W(0);
    STAGE_X(1); STAGE_W(1);
    STAGE_X(2); STAGE_W(2);
    u32x4 xf[2][4], wf[2][4];
    TS_MARK();
    asm volatile("s_waitcnt vmcnt(12)" : "+v"(ep)::"memory");
    if (w == 0) {
      const u64 tag = next_epoch(ep);
      if (l == 0) { *(u64*)(sm + G_TAG) = tag; st_dev(ws + WS_FA + (gi * 8 + xcd) * 8, tag); }
    }
    BARRIER();
    TS_MARK();
    READ_FRAGS(0, 0, 0);
    SCHED();
    u32x2 stv[4];
    const unsigned sto = WS_ST + (panel * 256 + wr * 64 + (l & 15)) * 8;
#pragma unroll
    for (int t = 0; t < 10; ++t) {
#pragma unroll
      for (int ks = 0; ks < 2; ++ks) {
        if (ks == 0) {
          READ_FRAGS(t, 1, 1);
        } else if (t < 9) {
          if (t < 8) WAIT_V(6); else WAIT_V(0);
          BARRIER();
          READ_FRAGS(t + 1, 0, 0);
          if (t == 8) {
#pragma unroll
            for (int r = 0; r < 4; ++r) stv[r] = LD64_DEV(rw, sto + r * 128);
          }
        }
        SCHED();
#pragma unroll
        for (int i = 0; i < 2; ++i)
#pragma unroll
          for (int r = 0; r < 4; ++r) acc[i][r] = MFMA16_BF(wf[ks][i], xf[ks][r], acc[i][r]);
        SCHED();
        if (ks == 1 && t + 3 < 10) STAGE_X(t + 3);
        if (ks == 0 && t >= 1 && t + 2 < 10) STAGE_W(t + 2);
        SCHED();
#pragma unroll
        for (int i = 2; i < 4; ++i)
#pragma unroll
          for (int r = 0; r < 4; ++r) acc[i][r] = MFMA16_BF(wf[ks][i], xf[ks][r], acc[i][r]);
        if (t < 9 || ks == 0) {
          if (((2 * t + ks) & 3) == wr) {
#pragma unroll
            for (int i = 0; i < 4; ++i) cs[i] = dot_ones(wf[ks][i], cs[i]);
          }
        }
        if (t == 9 && ks == 0) {
          if (wr == 3) {
#pragma unroll
            for (int i = 0; i < 4; ++i) cs[i] = dot_ones(wf[1][i], cs[i]);
          }
#pragma unroll
          for (int i = 0; i < 4; ++i) {
            const float hi = rnd_bf(cs[i]);
            *(unsigned*)(sm + G_CS + ((fh * 4 + i) * 64 + l) * 16 + wr * 4) = pk_bf(hi, cs[i] - hi);
          }
          BARRIER();
#pragma unroll
          for (int i = 0; i < 4; ++i) wf[0][i] = *(const u32x4*)(sm + G_CS + ((fh * 4 + i) * 64 + l) * 16);
        }
        SCHED();
      }
      if (t == 1 || t == 4 || t == 7) TS_MARK();
    }
#undef STAGE_X
#undef STAGE_W
#undef READ_FRAGS
    TS_MARK();
    const unsigned tag32 = *(const unsigned*)(sm + G_TAG);
#pragma unroll 1
    for (unsigned n = 0; n < SPIN_CAP; ++n) {
      if (__all(stv[0][1] == tag32 && stv[1][1] == tag32 && stv[2][1] == tag32 && stv[3][1] == tag32)) break;
      __builtin_amdgcn_s_sleep(1);
      asm volatile("" ::: "memory");
#pragma unroll
      for (int r = 0; r < 4; ++r) stv[r] = LD64_DEV(rw, sto + r * 128);
    }
    const unsigned ao = WS_AF + ((panel * 4 + wr) * 48 + ft * 2 + fh) * 4096 + l * 16;
#pragma unroll
    for (int r = 0; r < 4; ++r) {
      const unsigned nm = ((stv[r][0] & 0xffffu) ^ 0x8000u) * 0x10001u;
      const u32x4 bm = {nm, nm, nm, nm};
#pragma unroll
      for (int i = 0; i < 4; ++i) acc[i][r] = MFMA16_BF(wf[0][i], bm, acc[i][r]);
    }
#pragma unroll
    for (int r = 0; r < 4; ++r) {
      const float rs = __builtin_amdgcn_rcpf(bf_hi(stv[r][0])), r2 = rs * rs;
      u32x4 o;
#pragma unroll
      for (int i = 0; i < 2; ++i) {
        const f32x4 hv = (acc[i][r] * r2) * acc[2 + i][r];
        o[2 * i] = pk_bf(hv[0], hv[1]); o[2 * i + 1] = pk_bf(hv[2], hv[3]);
      }
      ST128(o, rw, ao + r * 1024);
    }
    TS_MARK();
    TS_END(0, 0);
    return;
  }
  const int aux = idx * 8 + xcd;
  u64 tag;
  {
    const int row = 4 * w + g, i16 = l & 15;
    const u32x4* xp = (const u32x4*)(x + s_of_rho(aux * 32 + row) * 640) + i16;
    u32x4 xv[5];
#pragma unroll
    for (int c = 0; c < 5; ++c) xv[c] = xp[c * 16];
    const u64 ep = ld_dev(ws + WS_EP);
    SCHED();
    dma1(l * 16, (const char*)&g_T80 + w * 2048, smb + A_T80 + w * 2048);
    dma1(l * 16, (const char*)&g_T80 + w * 2048 + 1024, smb + A_T80 + w * 2048 + 1024);
    SCHED();
    u32x4 wv[6];
    {
      const rsrc_t rr = mk_rsrc(w < 4 ? w_r : w_i, 2048 * 321 * 2);
      const unsigned so = (64 * (4 * (aux >> 3) + (w & 3)) + 8 * (aux & 7)) * 642;
#pragma unroll
      for (int i = 0; i < 5; ++i) wv[i] = __builtin_amdgcn_raw_buffer_load_b128(rr, so + i * 1024 + l * 16, 0, 2);
      wv[5] = __builtin_amdgcn_raw_buffer_load_b128(rr, so + 5120, 0, 2);
    }
    SCHED();
    TS_MARK();
    float f[40];
#pragma unroll
    for (int c = 0; c < 5; ++c)
#pragma unroll
      for (int j = 0; j < 4; ++j) { f[c * 8 + 2 * j] = bf_lo(xv[c][j]); f[c * 8 + 2 * j + 1] = bf_hi(xv[c][j]); }
    float s0 = 0.f, s1 = 0.f;
#pragma unroll
    for (int c = 0; c < 5; ++c) { if (c & 1) s1 = dot_ones(xv[c], s1); else s0 = dot_ones(xv[c], s0); }
    const float mean = sum16(s0 + s1) * (1.f / 640.f);
    float q4[4] = {0.f, 0.f, 0.f, 0.f};
#pragma unroll
    for (int e = 0; e < 40; ++e) { f[e] -= mean; q4[e & 3] += f[e] * f[e]; }
    const float mb = rnd_bf(mean), vb = rnd_bf(sum16((q4[0] + q4[1]) + (q4[2] + q4[3])) * (1.f / 640.f));
    const float sd = rnd_bf(__builtin_amdgcn_sqrtf(rnd_bf(vb + 1e-5f)));
    const float rs = __builtin_amdgcn_rcpf(sd), off = (mean - mb) * rs;
    tag = next_epoch(ep);
    if (i16 == 0) st_dev(ws + WS_ST + (aux * 32 + row) * 8, (tag << 32) | (__float_as_uint(mb) >> 16) | __float_as_uint(sd));
#pragma unroll
    for (int c = 0; c < 5; ++c) {
      u32x4 oh;
#pragma unroll
      for (int j = 0; j < 4; ++j) oh[j] = pk_h(f[c * 8 + 2 * j] * rs + off, f[c * 8 + 2 * j + 1] * rs + off);
      *(u32x4*)(sm + A_XH + row * 1344 + (c * 16 + i16) * 16) = oh;
    }
    TS_MARK();
    WAIT_V(7);
    BARRIER();
    TS_MARK();
    {
      const int h = l >> 5, p = l & 3;
      const char* rd = sm + A_XH + (4 * w + 2 * (g & 1) + (p >> 1)) * 1344 + (8 * h + ((l & 15) >> 2)) * 16 + (p & 1) * 8;
      u32x4 ax[5];
#pragma unroll
      for (int ks = 0; ks < 5; ++ks) {
        const u32x2 lo = tr_read(rd + ks * 256), hi = tr_read(rd + ks * 256 + 64);
        ax[ks] = (u32x4){lo[0], lo[1], hi[0], hi[1]};
      }
      const int ol = l & 31;
      char* ut = sm + A_UT + (w >> 1) * 10304 + (ol >> 1) * 256 + (w & 1) * 8;
      const int ub = (8 * h + (ol & 1)) ^ (ol & 14);
#pragma unroll
      for (int nt = 0; nt < 3; ++nt) {
        f32x16 acc;
#pragma unroll
        for (int e = 0; e < 16; ++e) acc[e] = 0.f;
#pragma unroll
        for (int ks = 0; ks < 5; ++ks) acc = MFMA32_H(ax[ks], *(const u32x4*)(sm + A_T80 + (nt * 5 + ks) * 1024 + l * 16), acc);
        if (nt < 2 || ol < 16) {
#pragma unroll
          for (int dl = 0; dl < 4; ++dl)
            *(u32x2*)(ut + nt * 4096 + ((ub ^ (2 * dl)) << 4)) = (u32x2){pk_h(acc[dl], acc[4 + dl]), pk_h(acc[8 + dl], acc[12 + dl])};
        }
      }
    }
    {
      char* wl = sm + A_W + w * 5152;
#pragma unroll
      for (int i = 0; i < 5; ++i) *(u32x4*)(wl + i * 1024 + l * 16) = wv[i];
      if (l == 0) *(u32x4*)(wl + 5120) = wv[5];
    }
    BARRIER();
    TS_MARK();
#pragma unroll
    for (int i = 0; i < 5; ++i) {
      const int e = i * 512 + tid, seg = e >> 2, grp = e & 3;
      ST128(*(const u32x4*)(sm + A_UT + grp * 10304 + ((seg ^ ((seg >> 3) & 14)) << 4)), rw, WS_UB + seg * RP + aux * 64 + grp * 16);
    }
    SCHED();
#pragma unroll
    for (int i = 0; i < 6; ++i) {
      const int e = i * 512 + tid;
      if (i < 5 || w == 0) {
        const int eq = e & 3, kb = (e >> 2) & 7, half = (e >> 5) & 1, c1 = e >> 6;
        const u16* L = (const u16*)(sm + A_W) + eq * 2576 + kb * 321 + (half ? 320 - c1 : c1);
        const int st = half ? -80 : 80;
        u32x4 o;
#pragma unroll
        for (int j = 0; j < 4; ++j) o[j] = ((unsigned)L[j * st] | ((unsigned)L[10304 + j * st] << 16)) ^ (half ? 0x80000000u : 0u);
        int f = 64 * (4 * (aux >> 3) + eq) + 8 * (aux & 7) + kb;
        if (half) f = (2048 - f) & 2047;
        ST128(o, rw, WS_WT + ((c1 * 2 + half) * 2048 + (f >> 8) * 256 + (f & 63) * 4 + ((f >> 6) & 3)) * 16);
      }
    }
    {
      typedef __attribute__((ext_vector_type(2))) u16 u16x2;
      u16x2 m2 = {0, 0};
#pragma unroll
      for (int i = 0; i < 6; ++i)
#pragma unroll
        for (int j = 0; j < 4; ++j) { const unsigned u = wv[i][j] & 0x7fff7fffu; m2 = __builtin_elementwise_max(m2, __builtin_bit_cast(u16x2, u)); }
      const unsigned mx = max64(max((unsigned)m2[0], (unsigned)m2[1]));
      if (l == 0) ((unsigned*)(sm + A_MX))[w] = mx;
    }
    SCHED();
    TS_MARK();
    WAIT_V(5);
    BARRIER();
    if (tid == 0) st_dev(ws + WS_F1 + aux * 8, tag);
    TS_MARK();
  }
#define PUBLISH_F2()                                                                                                  \
  do {                                                                                                                \
    WAIT_V(0);                                                                                                        \
    BARRIER();                                                                                                        \
    if (tid == 0) {                                                                                                   \
      const u32x4 m0_ = *(const u32x4*)(sm + A_MX), m1_ = *(const u32x4*)(sm + A_MX + 16);                            \
      const unsigned mx_ = max(max(max(m0_[0], m0_[1]), max(m0_[2], m0_[3])), max(max(m1_[0], m1_[1]), max(m1_[2], m1_[3]))); \
      st_dev(ws + WS_F2 + aux * 8, (tag & ~0xffffull) | mx_);                                                         \
    }                                                                                                                 \
  } while (0)
  if (aux < 41) {
    const int c1 = aux, c1s = c1 == 40 ? 0 : c1;
    const bool real_cls = c1s == 0;
    char* eb = sm + A_E;
#pragma unroll
    for (int i = 0; i < 3; ++i) dma1(l * 16, (const char*)&g_TF + (w * 3 + i) * 1024, smb + A_TF + (w * 3 + i) * 1024);
    const u32x4 g8 = *(const u32x4*)&g_TF.g8[l][0][0];
    f16x2 tc[8], tsn[8];
    {
      const float a0 = (float)(c1 * w) * (1.f / 640.f), a1 = (float)(l & 31) * (1.f / 2048.f);
#pragma unroll
      for (int e = 0; e < 8; ++e) {
        const float a = a0 + a1 * (float)(2 * (e & 1) + 8 * (e >> 1) + 4 * (l >> 5)), a2 = a + a1;
        tc[e] = as_h2(pk_h(__builtin_amdgcn_cosf(a), __builtin_amdgcn_cosf(a2)));
        tsn[e] = as_h2(pk_h(__builtin_amdgcn_sinf(a), __builtin_amdgcn_sinf(a2)));
        asm volatile("" : "+v"(tc[e]), "+v"(tsn[e]));
      }
    }
    SCHED();
    PUBLISH_F2();
    wait_flags(ws + WS_F1 + l * 8, tag);
    TS_MARK();
    u32x4 ar[4], ai[4];
    {
      const unsigned uo = WS_UB + (c1s * 16 + 2 * w) * RP + l * 16;
#pragma unroll
      for (int i = 0; i < 4; ++i) {
        ar[i] = LD128_DEV(rw, uo + (c1 == 40 ? RP : 0) + i * 1024);
        ai[i] = LD128_DEV(rw, uo + RP + i * 1024);
      }
    }
    u64 f2 = ld_dev(ws + WS_F2 + l * 8);
    SCHED();
    if (real_cls) {
#pragma unroll
      for (int i = 0; i < 4; ++i) ai[i] = (u32x4){0u, 0u, 0u, 0u};
    }
    const u32x4* G1 = (const u32x4*)(sm + A_TF) + l;
    const u32x4* G3 = (const u32x4*)(sm + A_TF + 12288) + l;
    char* erow = eb + (2 * w) * 4096 + ((l ^ e_key(w)) << 4);
    f32x16 zero;
#pragma unroll
    for (int e = 0; e < 16; ++e) zero[e] = 0.f;
    u32x2 wv[16];
    {
      f32x16 dr[2], di[2];
#pragma unroll
      for (int t = 0; t < 2; ++t) {
        dr[t] = zero; di[t] = zero;
#pragma unroll
        for (int ks = 0; ks < 2; ++ks) {
          const u32x4 C = G1[((t * 2 + ks) * 3) * 64], S = G1[((t * 2 + ks) * 3 + 1) * 64], N = G1[((t * 2 + ks) * 3 + 2) * 64];
          dr[t] = MFMA32_H(ar[t * 2 + ks], C, dr[t]);
          di[t] = MFMA32_H(ai[t * 2 + ks], C, di[t]);
          dr[t] = MFMA32_H(ai[t * 2 + ks], S, dr[t]);
          di[t] = MFMA32_H(ar[t * 2 + ks], N, di[t]);
        }
      }
      SCHED();
      TS_MARK();
#pragma unroll 1
      for (unsigned n = 0; n < SPIN_CAP; ++n) {
        if (__all((f2 >> 16) == (tag >> 16))) break;
        __builtin_amdgcn_s_sleep(1);
        f2 = ld_dev(ws + WS_F2 + l * 8);
      }
      asm volatile("" ::: "memory");
      {
        const unsigned base = WS_WT + (c1 * 2 + (g >> 1)) * 32768 + (g & 1) * 8 + (l & 15) * 16 +
                              ((4 * ((w >> 1) & 1) + (w & 1)) * 64 + 32 * (w >> 2)) * 64;
#pragma unroll
        for (int i = 0; i < 16; ++i) wv[i] = LD64_DEV(rw, base + (i & 1) * 8192 + (i >> 1) * 256);
      }
      SCHED();
      u32x4 br[2][2], bi[2][2];
      bfly_tw<false>(dr, di, tc, tsn, br, bi);
#pragma unroll
      for (int t2 = 0; t2 < 2; ++t2) {
        f32x16 xr_ = zero, xi_ = zero;
#pragma unroll
        for (int ks = 0; ks < 2; ++ks) {
          const u32x4 C = G3[((t2 * 2 + ks) * 3) * 64], S = G3[((t2 * 2 + ks) * 3 + 1) * 64], N = G3[((t2 * 2 + ks) * 3 + 2) * 64];
          xr_ = MFMA32_H(C, br[t2][ks], xr_);
          xi_ = MFMA32_H(C, bi[t2][ks], xi_);
          xr_ = MFMA32_H(S, bi[t2][ks], xr_);
          xi_ = MFMA32_H(N, br[t2][ks], xi_);
        }
        u32x4 o0[2], o1[2];
        pack_h(xr_, o0);
        pack_h(xi_, o1);
#pragma unroll
        for (int s2 = 0; s2 < 2; ++s2) {
          *(u32x4*)(erow + (t2 * 2 + s2) * 1024) = o0[s2];
          *(u32x4*)(erow + 4096 + (t2 * 2 + s2) * 1024) = swap_halves(o1[s2]);
        }
      }
    }
    float osc;
    u32x2 gi;
    {
      int ex = (int)(max64((unsigned)f2 & 0xffffu) >> 7);
      ex = ex < 8 ? 8 : (ex > 240 ? 240 : ex);
      const float up = __uint_as_float((unsigned)(250 - ex) << 23);
      gi = (u32x2){pk_bf(bf_lo(g8[2]) * up, bf_hi(g8[2]) * up), pk_bf(bf_lo(g8[3]) * up, bf_hi(g8[3]) * up)};
      osc = __uint_as_float((unsigned)(ex + 2) << 23) * ((real_cls ? 2.f : 4.f) * 256.f / (239.f * 640.f));
    }
    BARRIER();
    TS_MARK();
    {
      const int rr = 4 * g + ((l & 15) >> 2), wrw = l & 15;
      const int rdo = rr * 4096 + w * 512 + (((l & 3) ^ e_key(rr >> 1)) << 4) + (rr & 1) * 8;
      const int wro = wrw * 4096 + w * 512 + ((g ^ e_key(wrw >> 1)) << 4) + (wrw & 1) * 8;
      const u32x2 gf = {g8[0], g8[1]};
      const f32x4 z4 = {0.f, 0.f, 0.f, 0.f};
      u32x2 ev[16];
#pragma unroll
      for (int i = 0; i < 16; ++i) ev[i] = tr_read(eb + (rdo ^ (((i >> 1) & 3) * 64 + (i & 1) * 8)) + (i >> 3) * 256);
      f32x4 xx[16];
#pragma unroll
      for (int i = 0; i < 16; ++i) xx[i] = MFMA16K16_H(gf, ev[i], z4);
#pragma unroll
      for (int i = 0; i < 16; ++i) {
        const f32x4 xs = xx[i];
        const float r0 = bf_lo(wv[i][0]), i0 = bf_hi(wv[i][0]), r1 = bf_lo(wv[i][1]), i1 = bf_hi(wv[i][1]);
        const u32x2 y = {pk_bf(xs[0] * r0 - xs[1] * i0, xs[0] * i0 + xs[1] * r0), pk_bf(xs[2] * r1 - xs[3] * i1, xs[2] * i1 + xs[3] * r1)};
        xx[i] = MFMA16K16_BF(y, gi, z4);
      }
#pragma unroll
      for (int i = 0; i < 16; ++i)
        *(u32x2*)(eb + (wro ^ (((i >> 1) & 3) * 64 + (i & 1) * 8)) + (i >> 3) * 256) = (u32x2){pk_h(xx[i][0], xx[i][1]), pk_h(xx[i][2], xx[i][3])};
    }
    BARRIER();
    TS_MARK();
    {
      f32x16 dr[2], di[2];
#pragma unroll
      for (int t2 = 0; t2 < 2; ++t2) {
        dr[t2] = zero; di[t2] = zero;
#pragma unroll
        for (int s2 = 0; s2 < 2; ++s2) {
          const u32x4 pr_ = *(const u32x4*)(erow + (t2 * 2 + s2) * 1024);
          const u32x4 pi_ = swap_halves(*(const u32x4*)(erow + 4096 + (t2 * 2 + s2) * 1024));
          const u32x4 C = G1[((t2 * 2 + s2) * 3) * 64], S = G1[((t2 * 2 + s2) * 3 + 1) * 64], N = G1[((t2 * 2 + s2) * 3 + 2) * 64];
          dr[t2] = MFMA32_H(pr_, C, dr[t2]);
          di[t2] = MFMA32_H(pi_, C, di[t2]);
          dr[t2] = MFMA32_H(pi_, N, dr[t2]);
          di[t2] = MFMA32_H(pr_, S, di[t2]);
        }
      }
      u32x4 qr[2][2], qi[2][2];
      bfly_tw<true>(dr, di, tc, tsn, qr, qi);
      const unsigned vo = WS_VT + (w * 80 + (c1 == 40 ? 1 : 2 * c1)) * RP + l * 16;
#pragma unroll
      for (int t = 0; t < 2; ++t) {
        f32x16 zr = zero, zq = zero;
#pragma unroll
        for (int ks = 0; ks < 2; ++ks) {
          const u32x4 C = G3[((t * 2 + ks) * 3) * 64], S = G3[((t * 2 + ks) * 3 + 1) * 64], N = G3[((t * 2 + ks) * 3 + 2) * 64];
          zr = MFMA32_H(C, qr[t][ks], zr);
          zq = MFMA32_H(C, qi[t][ks], zq);
          zr = MFMA32_H(N, qi[t][ks], zr);
          zq = MFMA32_H(S, qr[t][ks], zq);
        }
#pragma unroll
        for (int ks = 0; ks < 2; ++ks) {
          u32x4 o0, o1;
#pragma unroll
          for (int j = 0; j < 4; ++j) {
            o0[j] = pk_bf(zr[8 * ks + 2 * j] * osc, zr[8 * ks + 2 * j + 1] * osc);
            o1[j] = pk_bf(zq[8 * ks + 2 * j] * osc, zq[8 * ks + 2 * j + 1] * osc);
          }
          ST128(o0, rw, vo + (t * 2 + ks) * 1024);
          if (!real_cls) ST128(o1, rw, vo + RP + (t * 2 + ks) * 1024);
        }
      }
    }
    TS_MARK();
    TS_END(0, 1);
    return;
  }
  {
    PUBLISH_F2();
    const int j0 = aux - 41;
    const rsrc_t rd_ = mk_rsrc(wd, 1536 * 640 * 2);
    u32x4 v[3][5];
#pragma unroll
    for (int it = 0; it < 3; ++it)
#pragma unroll
      for (int i = 0; i < 5; ++i) v[it][i] = __builtin_amdgcn_raw_buffer_load_b128(rd_, (i * 512 + tid) * 16 + (j0 + it * 23) * 40960, 0, 2);
    SCHED();
#pragma unroll
    for (int it = 0; it < 3; ++it)
#pragma unroll
      for (int i = 0; i < 5; ++i) {
        const int e = i * 512 + tid, row = e / 80, ch = e - row * 80;
        *(u32x4*)(sm + it * 41984 + row * 1312 + ch * 16) = v[it][i];
      }
    BARRIER();
    const char* rd = sm + (4 * g + ((l & 15) >> 2)) * 1312 + (l & 3) * 8 + w * 160;
#pragma unroll
    for (int it = 0; it < 3; ++it) {
      const int F = j0 + it * 23;
      if (F < 48) {
#pragma unroll
        for (int i = 0; i < 5; ++i) {
          const u32x2 lo = tr_read(rd + it * 41984 + i * 32), hi = tr_read(rd + it * 41984 + i * 32 + 16 * 1312);
          ST128(((u32x4){lo[0], lo[1], hi[0], hi[1]}), rw, WS_BF + ((w * 48 + F) * 5 + i) * 1024 + l * 16);
        }
      }
    }
    if (aux == 63 && w == 0) {
      wait_flags(ws + WS_F1 + l * 8, tag);
      wait_flags(ws + WS_FA + l * 8, tag);
      wait_flags(ws + WS_FA + 512 + l * 8, tag);
      wait_flags(ws + WS_FA + 1024 + l * 8, tag);
      if (l == 0) st_dev(ws + WS_EP, tag);
    }
    TS_END(0, 2);
  }
}

constexpr int K3_Z = 4 * 20480;
__global__ __launch_bounds__(256, 1) void kb(const u16* __restrict__ x, const char* __restrict__ ws, u16* __restrict__ out) {
  __shared__ __attribute__((aligned(1024))) char sm[K3_Z + 64 * 336];
  const int b = blockIdx.x, tid = threadIdx.x, l = tid & 63;
  const int w = __builtin_amdgcn_readfirstlane(tid >> 6);
  const int xcd = b & 7, idx = b >> 3;
  const int panel = (xcd >> 1) * 8 + (idx & 7), ntg = (xcd & 1) * 4 + (idx >> 3);
  TS_DECL TS_MARK();
  {
    const char* vsrc = ws + WS_VT + (size_t)w * 160 * RP + panel * 128;
    const int r8 = l >> 3, c8 = l & 7;
    const unsigned vo = r8 * RP + ((c8 ^ (((r8 >> 1) & 3) << 1)) << 4);
    const unsigned dst = lds_addr(sm) + w * 20480;
#pragma unroll
    for (int i = 0; i < 20; ++i) dma1(vo, vsrc + i * 8 * RP, dst + i * 1024);
  }
  u32x2 mi[5];
#pragma unroll
  for (int ks = 0; ks < 5; ++ks) mi[ks] = *(const u32x2*)&g_T80.i[ntg][ks][l][0];
  const u32x4* ap = (const u32x4*)(ws + WS_AF + ((size_t)panel * 48 + w) * 4096) + l;
  const u32x4* bp = (const u32x4*)(ws + WS_BF + ((size_t)ntg * 48 + w) * 5120) + l;
  constexpr int D = 4;
  u32x4 A[D][4], B[D][5];
#pragma unroll
  for (int i = 0; i < D; ++i) {
#pragma unroll
    for (int mt = 0; mt < 4; ++mt) A[i][mt] = ap[i * 1024 + mt * 64];
#pragma unroll
    for (int nt = 0; nt < 5; ++nt) B[i][nt] = bp[i * 1280 + nt * 64];
  }
  SCHED();
  TS_MARK();
  const int g = l >> 4;
  const int zq_ = (l & 15) >> 2, zrr = 4 * g + zq_, zsw = (zrr >> 1) & 3;
  const char* zrd = sm + w * 20480 + zrr * 128 + (l & 3) * 8;
  float* zb = (float*)(sm + K3_Z) + (l & 15) * 84 + 32 * g + 2 * w;
  const int orow = s_of_rho(panel * 64 + w * 16 + (l & 15)) * 640 + ntg * 80;
  const int ocol = (g & 1) * 16 + (g >> 1) * 8;
  u32x4 xres[2];
  u32x2 xres4;
  f32x4 acc[4][5];
#pragma unroll
  for (int mt = 0; mt < 4; ++mt)
#pragma unroll
    for (int nt = 0; nt < 5; ++nt) acc[mt][nt] = (f32x4){0.f, 0.f, 0.f, 0.f};
#pragma unroll
  for (int i = 0; i < 12; ++i) {
#pragma unroll
    for (int mt = 0; mt < 4; ++mt)
#pragma unroll
      for (int nt = 0; nt < 5; ++nt) acc[mt][nt] = MFMA16_BF(B[i % D][nt], A[i % D][mt], acc[mt][nt]);
    SCHED();
    if (i == 5) TS_MARK();
    if (i + D < 12) {
#pragma unroll
      for (int mt = 0; mt < 4; ++mt) A[i % D][mt] = ap[(i + D) * 1024 + mt * 64];
#pragma unroll
      for (int nt = 0; nt < 5; ++nt) B[i % D][nt] = bp[(i + D) * 1280 + nt * 64];
    }
    if (i + D == 12) {
      xres[0] = *(const u32x4*)(x + orow + ocol);
      xres[1] = *(const u32x4*)(x + orow + 32 + ocol);
      xres4 = *(const u32x2*)(x + orow + 64 + g * 4);
    }
    SCHED();
    if (i >= 4 && i < 8) {
      const int mt = i - 4;
      u32x2 v0[5], v1[5];
#pragma unroll
      for (int ks = 0; ks < 5; ++ks) { v0[ks] = tr_read(zrd + ks * 2048 + ((mt ^ zsw) << 5)); v1[ks] = tr_read(zrd + 10240 + ks * 2048 + ((mt ^ zsw) << 5)); }
      f32x4 z0 = {0.f, 0.f, 0.f, 0.f}, z1 = z0;
#pragma unroll
      for (int ks = 0; ks < 5; ++ks) { z0 = MFMA16K16_BF(mi[ks], v0[ks], z0); z1 = MFMA16K16_BF(mi[ks], v1[ks], z1); }
#pragma unroll
      for (int e = 0; e < 4; ++e)
        if (4 * g + e < 10) *(f32x2*)(zb + mt * 16 * 84 + 8 * e) = (f32x2){z0[e], z1[e]};
      SCHED();
    }
  }
  TS_MARK();
  f32x4 own[5];
#pragma unroll
  for (int mt = 0; mt < 4; ++mt) {
    if (mt == w) {
#pragma unroll
      for (int nt = 0; nt < 5; ++nt) own[nt] = acc[mt][nt];
    } else {
      f32x4* P = (f32x4*)(sm + w * 20480 + mt * 5120) + l;
#pragma unroll
      for (int nt = 0; nt < 5; ++nt) P[nt * 64] = acc[mt][nt];
    }
  }
  BARRIER();
  TS_MARK();
  const rsrc_t ro = mk_rsrc(out, 2048 * 640 * 2);
#pragma unroll
  for (int nt = 0; nt < 5; ++nt) {
    own[nt] += *(const f32x4*)(sm + K3_Z + (w * 16 + (l & 15)) * 336 + nt * 64 + g * 16);
#pragma unroll
    for (int sw = 1; sw < 4; ++sw) own[nt] += *((const f32x4*)(sm + ((w + sw) & 3) * 20480 + w * 5120) + nt * 64 + l);
  }
#pragma unroll
  for (int i = 0; i < 2; ++i) {
    float v[8];
#pragma unroll
    for (int e = 0; e < 4; ++e) {
      const auto sw = __builtin_amdgcn_permlane16_swap(__float_as_uint(own[2 * i][e]), __float_as_uint(own[2 * i + 1][e]), false, false);
      v[e] = __uint_as_float(sw[0]); v[4 + e] = __uint_as_float(sw[1]);
    }
    u32x4 o;
#pragma unroll
    for (int e = 0; e < 4; ++e) o[e] = pk_bf(v[2 * e] + bf_lo(xres[i][e]), v[2 * e + 1] + bf_hi(xres[i][e]));
    ST128(o, ro, (orow + i * 32 + ocol) * 2);
  }
  {
    const f32x4 v = own[4];
    u32x2 o = {pk_bf(v[0] + bf_lo(xres4[0]), v[1] + bf_hi(xres4[0])), pk_bf(v[2] + bf_lo(xres4[1]), v[3] + bf_hi(xres4[1]))};
    ST64(o, ro, (orow + 64 + g * 4) * 2);
  }
  TS_END(1, 0);
}

extern "C" void kernel_launch(void* const* d_in, const int* in_sizes, int n_in, void* d_out, int out_size, void* d_ws, size_t ws_size,
                              hipStream_t stream) {
  const u16* x = (const u16*)d_in[0];
  const u16* w_r = (const u16*)d_in[1];
  const u16* w_i = (const u16*)d_in[2];
  const u16* wg = (const u16*)d_in[4];
  const u16* wu = (const u16*)d_in[5];
  const u16* wd = (const u16*)d_in[6];
  char* ws = (char*)d_ws;
  ka<<<256, 512, 0, stream>>>(x, w_r, w_i, wg, wu, wd, ws);
  kb<<<256, 256, 0, stream>>>(x, ws, (u16*)d_out);
}
```

```cpp
#include <hip/hip_runtime.h>
#include <hip/hip_bf16.h>

typedef unsigned short u16;
typedef __attribute__((ext_vector_type(8))) __bf16 bf16x8;
typedef __attribute__((ext_vector_type(2))) __bf16 bf16x2;
typedef __attribute__((ext_vector_type(8))) _Float16 f16x8;
typedef __attribute__((ext_vector_type(4))) _Float16 f16x4;
typedef __attribute__((ext_vector_type(2))) _Float16 f16x2;
typedef __attribute__((ext_vector_type(4))) short s16x4;
typedef __attribute__((ext_vector_type(2))) float f32x2;
typedef __attribute__((ext_vector_type(4))) float f32x4;
typedef __attribute__((ext_vector_type(16))) float f32x16;
typedef __attribute__((ext_vector_type(4))) unsigned u32x4;
typedef __attribute__((ext_vector_type(2))) unsigned u32x2;
typedef __attribute__((ext_vector_type(2))) u16 u16x2;
typedef __amdgpu_buffer_rsrc_t rsrc_t;
#define DI __device__ __forceinline__
#define LDS_AS __attribute__((address_space(3)))

namespace tb {
constexpr double kPi = 3.14159265358979323846264338327950288;
constexpr double sinT(double x) { double x2 = x * x, t = x, s = x; for (int i = 1; i < 12; ++i) { t *= -x2 / ((2 * i) * (2 * i + 1)); s += t; } return s; }
constexpr double cosT(double x) { double x2 = x * x, t = 1, s = 1; for (int i = 1; i < 12; ++i) { t *= -x2 / ((2 * i - 1) * (2 * i)); s += t; } return s; }
constexpr double cos2pi(int q, int den) {
  q %= den; if (2 * q > den) q = den - q;
  double sg = 1; if (4 * q > den) { q = den / 2 - q; sg = -1; }
  if (8 * q > den) return sg * sinT(2 * kPi * (den / 4 - q) / den);
  return sg * cosT(2 * kPi * q / den);
}
constexpr u16 bfbits(double d) { unsigned u = __builtin_bit_cast(unsigned, (float)d); u += 0x7fffu + ((u >> 16) & 1u); return (u16)(u >> 16); }
constexpr u16 hbits(double d) { return __builtin_bit_cast(u16, (_Float16)d); }
constexpr int krow(int s, int h, int j) { return 16 * s + 8 * (j >> 2) + 4 * h + (j & 3); }
template <int N> struct CT { double c[N]; };
template <int N> constexpr CT<N> mkC() { CT<N> c{}; for (int i = 0; i < N; ++i) c.c[i] = cos2pi(i, N); return c; }
constexpr CT<64> c64 = mkC<64>();
constexpr CT<80> c80 = mkC<80>();
constexpr double m80(int o, int d) { return o == 1 ? c80.c[(40 * d) % 80] : c80.c[((o >> 1) * d + ((o & 1) ? 20 : 0)) % 80]; }
struct alignas(16) TabF {
  u16 g1[2][2][2][64][8];
  u16 g3[2][2][2][64][8];
  u16 g8[64][2][4];
};
constexpr TabF mkTabF() {
  TabF T{};
  for (int t = 0; t < 2; ++t) for (int ks = 0; ks < 2; ++ks) for (int l = 0; l < 64; ++l) for (int j = 0; j < 8; ++j) {
    const int r = l & 31, k = krow(ks, l >> 5, j);
    const int q1 = (r * (2 * k + t)) & 63, q3 = (k * (2 * r + t)) & 63;
    T.g1[t][ks][0][l][j] = hbits(c64.c[q1] * 0.25); T.g1[t][ks][1][l][j] = hbits(c64.c[(q1 + 48) & 63] * 0.25);
    T.g3[t][ks][0][l][j] = hbits(c64.c[q3] * 0.25); T.g3[t][ks][1][l][j] = hbits(c64.c[(q3 + 48) & 63] * 0.25);
  }
  for (int l = 0; l < 64; ++l) for (int e = 0; e < 4; ++e) {
    const int m = l & 15, k = 4 * (l >> 4) + e, q = (8 * (m >> 1) * (k >> 1)) & 63;
    const double C = c64.c[q], S = c64.c[(q + 48) & 63];
    T.g8[l][0][e] = hbits(0.25 * ((m & 1) == (k & 1) ? C : ((m & 1) ? -S : S)));
    T.g8[l][1][e] = bfbits(239. / 256. * ((m & 1) == (k & 1) ? C : ((k & 1) ? -S : S)));
  }
  return T;
}
struct alignas(16) Tab80 {
  u16 f[3][5][64][8];
  u16 pad[512];
  u16 i[6][5][64][4];
};
constexpr Tab80 mkTab80() {
  Tab80 T{};
  for (int n = 0; n < 3; ++n) for (int ks = 0; ks < 5; ++ks) for (int l = 0; l < 64; ++l) for (int j = 0; j < 8; ++j)
    T.f[n][ks][l][j] = 32 * n + (l & 31) < 80 ? hbits(m80(32 * n + (l & 31), 16 * ks + 8 * (l >> 5) + j)) : (u16)0;
  for (int n = 0; n < 6; ++n) for (int ks = 0; ks < 5; ++ks) for (int l = 0; l < 64; ++l) for (int e = 0; e < 4; ++e)
    T.i[n][ks][l][e] = bfbits(m80(16 * ks + 4 * (l >> 4) + e, 13 * n + (n < 2 ? n : 2) + (l & 15)));
  return T;
}
}
static __device__ const tb::TabF g_TF = tb::mkTabF();
static __device__ const tb::Tab80 g_T80 = tb::mkTab80();

constexpr unsigned WS_EP = 0;
constexpr unsigned WS_F1 = 256;
constexpr unsigned WS_F2 = WS_F1 + 512;
constexpr unsigned WS_FV = WS_F2 + 512;
constexpr unsigned WS_FH = WS_FV + 512;
constexpr unsigned WS_FA = 4096;
constexpr unsigned WS_ST = 8192;
constexpr unsigned CP = 65536 + 256;
constexpr unsigned WS_UB = WS_ST + 2048 * 8;
constexpr unsigned WS_WT = WS_UB + 40 * CP;
constexpr unsigned WS_AF = WS_WT + 41 * CP;
constexpr unsigned WS_BF = WS_AF + 32 * 48 * 4096;
constexpr unsigned VP = 8192 + 256;
constexpr unsigned WS_VT = WS_BF + 6 * 48 * 7168;
constexpr unsigned WS_END = WS_VT + 328 * VP;

#if defined(__HIP_DEVICE_COMPILE__)
#pragma clang attribute push(__attribute__((target("no-packed-fp32-ops"))), apply_to = function)
#endif
DI float u2f(unsigned u) { return __builtin_bit_cast(float, u); }
DI unsigned f2u(float f) { return __builtin_bit_cast(unsigned, f); }
DI bool all_lanes(bool p) { return __builtin_amdgcn_ballot_w64(!p) == 0; }
DI int s_of_rho(int rho) {
  const int t = rho >> 10, ks = (rho >> 9) & 1, h = (rho >> 8) & 1, r = (rho >> 3) & 31, jh = (rho >> 2) & 1, jl = rho & 3;
  return 64 * (16 * ks + 8 * jh + 4 * h + jl) + 32 * t + r;
}
DI float bf_lo(unsigned u) { return u2f(u << 16); }
DI float bf_hi(unsigned u) { return u2f(u & 0xffff0000u); }
DI unsigned pk_bf(float a, float b) { f32x2 f = {a, b}; return __builtin_bit_cast(unsigned, __builtin_convertvector(f, bf16x2)); }
DI unsigned pk_h(float a, float b) { f32x2 f = {a, b}; return __builtin_bit_cast(unsigned, __builtin_convertvector(f, f16x2)); }
DI float rnd_bf(float a) { return bf_lo(pk_bf(a, 0.f)); }
#define MFMA16_BF(a, b, c) __builtin_amdgcn_mfma_f32_16x16x32_bf16(__builtin_bit_cast(bf16x8, a), __builtin_bit_cast(bf16x8, b), c, 0, 0, 0)
#define MFMA32_BF(a, b, c) __builtin_amdgcn_mfma_f32_32x32x16_bf16(__builtin_bit_cast(bf16x8, a), __builtin_bit_cast(bf16x8, b), c, 0, 0, 0)
#define MFMA32_H(a, b, c) __builtin_amdgcn_mfma_f32_32x32x16_f16(__builtin_bit_cast(f16x8, a), __builtin_bit_cast(f16x8, b), c, 0, 0, 0)
#define MFMA16K16_H(a, b, c) __builtin_amdgcn_mfma_f32_16x16x16f16(__builtin_bit_cast(f16x4, a), __builtin_bit_cast(f16x4, b), c, 0, 0, 0)
#define MFMA16K16_BF(a, b, c) __builtin_amdgcn_mfma_f32_16x16x16bf16_1k(__builtin_bit_cast(s16x4, a), __builtin_bit_cast(s16x4, b), c, 0, 0, 0)
DI u32x2 tr_read(const char* p) {
  s16x4 v = __builtin_amdgcn_ds_read_tr16_b64_v4i16((LDS_AS s16x4*)(p));
  return __builtin_bit_cast(u32x2, v);
}
DI rsrc_t mk_rsrc(const void* p, unsigned bytes) { return __builtin_amdgcn_make_buffer_rsrc((void*)p, 0, bytes, 0x00020000); }
#define ST128(v, r, off) __builtin_amdgcn_raw_buffer_store_b128(v, r, off, 0, 16)
#define ST64(v, r, off) __builtin_amdgcn_raw_buffer_store_b64(v, r, off, 0, 16)
#define WAIT_V(n) asm volatile("s_waitcnt vmcnt(%0)" ::"n"(n) : "memory")
#define WAIT_L(n) asm volatile("s_waitcnt lgkmcnt(%0)" ::"n"(n) : "memory")
#define SCHED() __builtin_amdgcn_sched_barrier(0)
#define BARRIER() do { WAIT_L(0); __builtin_amdgcn_s_barrier(); asm volatile("" ::: "memory"); } while (0)
DI void dma1(unsigned voff, const void* sbase, unsigned lds) {
  asm volatile("s_mov_b32 m0, %2\n\ts_nop 3\n\tglobal_load_lds_dwordx4 %0, %1" ::"v"(voff), "s"(sbase), "s"(lds) : "memory");
}
DI void dma1nt(unsigned voff, const void* sbase, unsigned lds) {
  asm volatile("s_mov_b32 m0, %2\n\ts_nop 3\n\tglobal_load_lds_dwordx4 %0, %1 nt" ::"v"(voff), "s"(sbase), "s"(lds) : "memory");
}
template <int OFF> DI void dma1q(unsigned voff, const void* sbase, unsigned lds) {
  asm volatile("s_add_u32 m0, %2, %3\n\ts_nop 0\n\tglobal_load_lds_dwordx4 %0, %1" ::"v"(voff), "s"(sbase), "s"(lds), "n"(OFF) : "memory", "scc");
}
template <int CTRL> DI unsigned dpp_u(unsigned x) { return (unsigned)__builtin_amdgcn_mov_dpp((int)x, CTRL, 0xf, 0xf, true); }
template <int CTRL> DI float dpp_f(float x) { return u2f(dpp_u<CTRL>(f2u(x))); }
DI float sum16(float x) {
  x += dpp_f<0xB1>(x); x += dpp_f<0x4E>(x); x += dpp_f<0x141>(x); x += dpp_f<0x128>(x);
  return x;
}
DI float sum_rows(float x) {
  const auto s = __builtin_amdgcn_permlane16_swap(f2u(x), f2u(x), false, false);
  x = u2f(s[0]) + u2f(s[1]);
  const auto t = __builtin_amdgcn_permlane32_swap(f2u(x), f2u(x), false, false);
  return u2f(t[0]) + u2f(t[1]);
}
typedef unsigned long long u64;
typedef __attribute__((address_space(1))) u64 gu64;
#define RLX_AGENT __ATOMIC_RELAXED, __HIP_MEMORY_SCOPE_AGENT
DI u64 ld_dev(const char* p) { return __hip_atomic_load((gu64*)p, RLX_AGENT); }
DI void st_dev(char* p, u64 v) { __hip_atomic_store((gu64*)p, v, RLX_AGENT); }
DI u64 next_epoch(u64 e) { return e * 0x9E3779B97F4A7C15ull + 0x632BE59BD9B4E019ull; }
constexpr unsigned SPIN_CAP = 1u << 18;
DI u64 wait_flags(const char* p, u64 tag) {
  u64 v = 0;
#pragma unroll 1
  for (unsigned n = 0; n < SPIN_CAP; ++n) {
    v = ld_dev(p);
    if (all_lanes((v >> 16) == (tag >> 16))) break;
    __builtin_amdgcn_s_sleep(1);
  }
  asm volatile("" ::: "memory");
  return v;
}
#define LD128_DEV(r, off) __builtin_amdgcn_raw_buffer_load_b128(r, off, 0, 16)
#define LD128_DEV2(r, off, soff) __builtin_amdgcn_raw_buffer_load_b128(r, off, soff, 16)
#define LD64_DEV(r, off) __builtin_amdgcn_raw_buffer_load_b64(r, off, 0, 16)
DI unsigned max64(unsigned x) {
  x = max(x, dpp_u<0xB1>(x)); x = max(x, dpp_u<0x4E>(x)); x = max(x, dpp_u<0x141>(x)); x = max(x, dpp_u<0x128>(x));
  const auto s = __builtin_amdgcn_permlane16_swap(x, x, false, false);
  x = max(s[0], s[1]);
  const auto t = __builtin_amdgcn_permlane32_swap(x, x, false, false);
  return max(t[0], t[1]);
}
DI unsigned lds_addr(const char* p) { return (unsigned)(size_t)(LDS_AS const char*)p; }

#define TS_DECL
#define TS_SET(n)
#define TS_MARK()
#define TS_END(k, role)

#define MAPV 0
#define PFPOL "nt"
constexpr int CODE_LINES = 224;
constexpr int STAGE_B = 49152;
constexpr int G_CS = 3 * STAGE_B;
constexpr int G_ST = G_CS + 512;
constexpr int G_PF = G_ST + 2048;
constexpr int G_TAG = G_PF + 1024;
constexpr int A_TF = 0;
constexpr int A_T80 = 16384;
constexpr int A_XH = 32768;
constexpr int A_UT = A_XH + 32 * 1344;
constexpr int A_W = A_UT + 4 * 10304;
constexpr int A_MX = A_W + 8 * 5152;
constexpr int A_E = 16384;
constexpr int KA_LDS = A_MX + 64;
static_assert(G_TAG + 1024 <= KA_LDS && KA_LDS <= 163840, "lds");

DI void pack_h(const f32x16& v, u32x4 (&o)[2]) {
#pragma unroll
  for (int s = 0; s < 2; ++s)
#pragma unroll
    for (int j = 0; j < 4; ++j) o[s][j] = pk_h(v[8 * s + 2 * j], v[8 * s + 2 * j + 1]);
}
DI f16x2 as_h2(unsigned u) { return __builtin_bit_cast(f16x2, u); }
DI unsigned as_u(f16x2 v) { return __builtin_bit_cast(unsigned, v); }
template <bool INV> DI void bfly_tw(const f32x16 (&dr)[2], const f32x16 (&di)[2], const f16x2 (&tc)[8], const f16x2 (&ts)[8],
                                    u32x4 (&er)[2][2], u32x4 (&ei)[2][2]) {
#pragma unroll
  for (int e = 0; e < 8; ++e) {
    const f16x2 r0 = as_h2(pk_h(dr[0][2 * e], dr[0][2 * e + 1])), r1 = as_h2(pk_h(dr[1][2 * e], dr[1][2 * e + 1]));
    const f16x2 i0 = as_h2(pk_h(di[0][2 * e], di[0][2 * e + 1])), i1 = as_h2(pk_h(di[1][2 * e], di[1][2 * e + 1]));
#pragma unroll
    for (int t = 0; t < 2; ++t) {
      const f16x2 sr = t ? r0 - r1 : r0 + r1, si = t ? i0 - i1 : i0 + i1;
      const f16x2 a = sr * tc[e], c = si * tc[e];
      er[t][e >> 2][e & 3] = as_u(__builtin_elementwise_fma(INV ? -si : si, ts[e], a));
      ei[t][e >> 2][e & 3] = as_u(__builtin_elementwise_fma(INV ? sr : -sr, ts[e], c));
    }
  }
}
DI u32x4 neg_h(u32x4 v) { return v ^ 0x80008000u; }
DI u32x4 swap_halves(u32x4 v) { return (u32x4){v[2], v[3], v[0], v[1]}; }
DI int e_key(int d2) { return ((d2 & 2) << 2) | ((d2 & 1) << 2) | ((d2 & 4) >> 1) | ((d2 & 2) >> 1); }

__global__ __launch_bounds__(512, 2) void ka(const u16* __restrict__ x, const u16* __restrict__ w_r, const u16* __restrict__ w_i,
                                             const u16* __restrict__ wg, const u16* __restrict__ wu, const u16* __restrict__ wd,
                                             char* __restrict__ ws, u16* __restrict__ out) {
  __shared__ __attribute__((aligned(1024))) char sm[KA_LDS];
  u64 pc0;
  asm volatile("s_getpc_b64 %0" : "=s"(pc0));
  pc0 &= ~255ull;
  const int b = blockIdx.x, tid = threadIdx.x, l = tid & 63;
  const int w = __builtin_amdgcn_readfirstlane(tid >> 6);
  const int xcd = b & 7, idx = b >> 3;
  const unsigned smb = lds_addr(sm);
  const rsrc_t rw = mk_rsrc(ws, WS_END);
  const int g = l >> 4;
  TS_DECL TS_MARK();
  if (idx >= 8) {
    const int gi = idx - 8;
    const int panel = (xcd >> 2) * 4 + (gi & 3), ft = (xcd & 3) * 6 + (gi >> 2);
    const int gwg = gi * 8 + xcd;
    const int p3 = xcd * 4 + (gi & 3), ct = gi >> 2, d1s = 13 * ct + (ct < 2 ? ct : 2), nd1 = ct < 2 ? 14 : 13;
    const int i16 = l & 15, kh = w >> 2, c0 = 2 * (w & 3) + kh;
    constexpr int NB = 20;
    u32x4 B[NB][2];
    const unsigned bo = WS_BF + (ct * 48 + kh) * 7168 + g * 224 + min(i16, 13) * 16;
#define LOADB(j) do { B[(j) % NB][0] = LD128_DEV2(rw, bo + c0 * 896, (j) * 14336); B[(j) % NB][1] = LD128_DEV2(rw, bo + (c0 ^ 1) * 896, (j) * 14336); } while (0)
    if (w >= 4) {
      const int lw = w - 4, r8 = l >> 3, c8 = l & 7;
      asm volatile("s_mov_b32 m0, %2\n\ts_nop 3\n\tglobal_load_lds_dword %0, %1 sc1" ::"v"(WS_EP + (l & 1) * 4), "s"(ws), "s"(smb + G_TAG + lw * 256) : "memory");
      unsigned xo[2], woff[2];
#pragma unroll
      for (int v = 0; v < 2; ++v) {
        xo[v] = (s_of_rho(panel * 256 + 2 * lw + v) + r8) * 1280 + ((c8 ^ ((r8 >> 1) | ((lw >> 1) << 2))) << 4);
        woff[v] = ((16 * lw + 8 * v + r8) * 1536 + ft * 64) * 2 + ((c8 ^ ((((r8 >> 1) & 1) | (v << 1)) << 1)) << 4);
      }
      const char *xs = (const char*)x, *gs = (const char*)wg, *us = (const char*)wu;
      const unsigned dx = smb + lw * 8192, dw = smb + 32768 + lw * 2048;
#define STAGE(off)                                                                                   \
  do {                                                                                               \
    _Pragma("unroll") for (int v_ = 0; v_ < 2; ++v_) {                                               \
      dma1(woff[v_], gs, dw + (off) + v_ * 1024);                                                    \
      dma1(woff[v_], us, dw + (off) + 8192 + v_ * 1024);                                             \
    }                                                                                                \
    _Pragma("unroll") for (int i_ = 0; i_ < 8; ++i_) dma1(xo[i_ >> 2], xs + (i_ & 3) * 10240, dx + (off) + i_ * 1024); \
    xs += 128; gs += 64 * 3072; us += 64 * 3072;                                                     \
  } while (0)
#pragma unroll 1
      for (int t = 0; t < 3; ++t) STAGE(t * STAGE_B);
      WAIT_V(24);
      __builtin_amdgcn_s_barrier();
      {
        const int t2 = tid - 256, n = (t2 < 224 ? t2 : 0) * 4 + (gi & 3);
        asm volatile("s_mov_b32 m0, %2\n\ts_nop 3\n\tglobal_load_lds_dword %0, %1" ::"v"((unsigned)((192 + (n >> 1)) * 3072 + ft * 128)),
                     "s"((gi & 1) ? wu : wg), "s"(smb + G_PF + lw * 256) : "memory");
      }
      const u64 tag = next_epoch(*(const u64*)(sm + G_TAG + lw * 256));
      int cur = 0;
      WAIT_V(MAPV == 0 ? 13 : 12);
#pragma unroll 1
      for (int u = 0; u < 7; ++u) {
        __builtin_amdgcn_s_barrier();
        if (u == 0 && tid == 256) st_dev(ws + WS_FA + gwg * 8, tag);
        STAGE(cur);
        cur = cur == 2 * STAGE_B ? 0 : cur + STAGE_B;
        WAIT_V(12);
      }
#undef STAGE
      __builtin_amdgcn_s_barrier();
      {
        u64 st = 0;
#pragma unroll 1
        for (unsigned n = 0; n < SPIN_CAP; ++n) {
          st = ld_dev(ws + WS_ST + (panel * 256 + tid - 256) * 8);
          const u64 f1 = ld_dev(ws + WS_F1 + l * 8);
          if (all_lanes((unsigned)(st >> 32) == (unsigned)tag && f1 == tag)) break;
          __builtin_amdgcn_s_sleep(8);
        }
        WAIT_V(0);
        const float rs = __builtin_amdgcn_rcpf(bf_hi((unsigned)st));
        *(f32x2*)(sm + G_ST + (tid - 256) * 8) = (f32x2){bf_lo((unsigned)st), rs * rs};
      }
      const char* bsrc = ws + WS_BF + ct * 48 * 7168 + 2 * lw * 896;
      const unsigned bl = g * 224 + min(i16, 13) * 16;
#define STAGEB(j0)                                                                                                    \
  do {                                                                                                                \
    _Pragma("unroll 1") for (int i_ = 2 * (j0); i_ < 2 * (j0) + 12; ++i_)                                             \
      asm volatile("s_mov_b32 m0, %2\n\ts_nop 3\n\tglobal_load_lds_dwordx4 %0, %1 sc1" ::"v"(bl),                    \
                   "s"(bsrc + (i_ >> 1) * 14336 + (i_ & 1) * 896), "s"(smb + (i_ >> 1) * 8192 + (2 * lw + (i_ & 1)) * 1024) : "memory"); \
  } while (0)
      STAGEB(6);
      BARRIER();
      STAGEB(12);
#pragma unroll
      for (int j = 0; j < 3; ++j) LOADB(j);
      SCHED();
      __builtin_amdgcn_s_barrier();
      STAGEB(0);
#undef STAGEB
#pragma unroll
      for (int j = 3; j < 10; ++j) LOADB(j);
      SCHED();
      WAIT_V(14);
      __builtin_amdgcn_s_barrier();
#pragma unroll
      for (int j = 10; j < NB; ++j) LOADB(j);
    } else {
    if (w == 3) {
      asm volatile("s_mov_b32 m0, %2\n\ts_nop 3\n\tglobal_load_lds_dword %0, %1" ::"v"(min((gi & 3) * 64 + l, CODE_LINES - 1) * 128), "s"(pc0),
                   "s"(smb + G_PF + 768) : "memory");
    }
    const int wr = w >> 1, fh = w & 1;
    {
    const int q = (l & 15) >> 2;
    const int xb0 = (i16 >> 3) * 16384 + (wr * 16 + (l & 7)) * 128 + ((g ^ (((l & 7) >> 1) | ((i16 >> 3) << 2))) << 4);
    const int wb0 = (32768 + (8 * g + q) * 128 + (((fh * 2) ^ (((q >> 1) & 1) | ((g & 1) << 1))) << 5) + (l & 3) * 8) ^ (wr << 13);
    const char* xr[2];
    const char* wp[4];
#define SET_BUF(off)                                                                                       \
  do {                                                                                                     \
    xr[0] = sm + (off) + xb0; xr[1] = sm + (off) + (xb0 ^ 64);                                             \
    _Pragma("unroll") for (int i_ = 0; i_ < 4; ++i_) wp[i_] = sm + (off) + (wb0 ^ ((i_ & 1) << 5) ^ ((i_ >> 1) << 13)); \
  } while (0)
    f32x4 acc[4][8];
#pragma unroll
    for (int i = 0; i < 4; ++i)
#pragma unroll
      for (int r = 0; r < 8; ++r) acc[i][r] = (f32x4){0.f, 0.f, 0.f, 0.f};
    f32x4 cs[2] = {{0.f, 0.f, 0.f, 0.f}, {0.f, 0.f, 0.f, 0.f}};
    u32x4 ones = {0x3f803f80u, 0x3f803f80u, 0x3f803f80u, 0x3f803f80u};
    asm volatile("" : "+v"(ones));
    u32x4 xf[2][8];
    u32x2 wf[2][8];
#define RD(ks, n)                                                                                          \
  do {                                                                                                     \
    if ((n) >= 2 && (n) < 10) xf[ks][(n) - 2] = *(const u32x4*)(xr[ks] + (((n) - 2) & 3) * 4096 + (((n) - 2) >> 2) * 1024); \
    else { const int m_ = (n) < 2 ? (n) : (n) - 8; wf[ks][m_] = tr_read(wp[m_ >> 1] + (ks) * 4096 + (m_ & 1) * 512); } \
  } while (0)
#define WF(ks, i) ((u32x4){wf[ks][2 * (i)][0], wf[ks][2 * (i)][1], wf[ks][2 * (i) + 1][0], wf[ks][2 * (i) + 1][1]})
#define KSTEP(ks, READS, CSUM)                                                                             \
  do {                                                                                                     \
    _Pragma("unroll") for (int n_ = 0; n_ < 32; ++n_) {                                                    \
      acc[n_ >> 3][n_ & 7] = MFMA16_BF(WF(ks, n_ >> 3), xf[ks][n_ & 7], acc[n_ >> 3][n_ & 7]);             \
      if (READS && n_ < 16) RD((ks) ^ 1, n_);                                                              \
      if (CSUM && n_ >= 30) cs[n_ & 1] = MFMA16_BF(WF(ks, n_ & 1), ones, cs[n_ & 1]);                      \
      SCHED();                                                                                             \
    }                                                                                                      \
  } while (0)
    SET_BUF(0);
    TS_MARK();
    BARRIER();
    TS_MARK();
#pragma unroll
    for (int n = 0; n < 16; ++n) RD(0, n);
    SCHED();
    KSTEP(0, true, 1);
    int nxt = STAGE_B;
#pragma unroll 1
    for (int u = 0; u < 9; ++u) {
      BARRIER();
      SET_BUF(nxt);
      SCHED();
      KSTEP(1, true, 1);
      KSTEP(0, true, 1);
      nxt = nxt == 2 * STAGE_B ? 0 : nxt + STAGE_B;
    }
    TS_MARK();
#pragma unroll
    for (int i = 0; i < 2; ++i) {
      cs[i] = MFMA16_BF(WF(1, i), ones, cs[i]);
      if ((l & 15) == 0) *(f32x4*)(sm + G_CS + w * 128 + i * 64 + g * 16) = cs[i];
    }
    BARRIER();
    f32x4 co[2];
    f32x4 st[4];
#pragma unroll
    for (int i = 0; i < 2; ++i) co[i] = *(const f32x4*)(sm + G_CS + (w ^ 2) * 128 + i * 64 + g * 16);
#pragma unroll
    for (int r = 0; r < 4; ++r) st[r] = *(const f32x4*)(sm + G_ST + (wr * 128 + (r >> 1) * 64 + 8 * (l & 7) + 4 * (i16 >> 3) + (r & 1) * 2) * 8);
    SCHED();
    KSTEP(1, false, 0);
#undef SET_BUF
#undef RD
#undef WF
#undef KSTEP
    TS_MARK();
    const unsigned ao = WS_AF + ((panel * 4 + wr * 2) * 48 + ft * 2 + fh) * 4096 + l * 16;
#pragma unroll
    for (int r = 0; r < 8; ++r) {
      const float mb = st[r >> 1][(r & 1) * 2], r2 = st[r >> 1][(r & 1) * 2 + 1];
      u32x4 o;
#pragma unroll
      for (int i = 0; i < 2; ++i) {
        const f32x4 hv = ((acc[i][r] - mb * cs[i]) * r2) * (acc[2 + i][r] - mb * co[i]);
        o[2 * i] = pk_bf(hv[0], hv[1]); o[2 * i + 1] = pk_bf(hv[2], hv[3]);
      }
      ST128(o, rw, ao + (r >> 2) * 48 * 4096 + (r & 3) * 1024);
    }
    }
    SCHED();
    TS_MARK();
    WAIT_V(0);
    __builtin_amdgcn_s_barrier();
    asm volatile("" ::: "memory");
    TS_MARK();
    const u64 tag = next_epoch(*(const u64*)(sm + G_TAG));
    if (tid == 0) st_dev(ws + WS_FH + (panel * 32 + ft) * 8, tag);
#pragma unroll
    for (int j = 0; j < 18; ++j)
#pragma unroll
      for (int i = 0; i < 2; ++i) B[j][i] = *(const u32x4*)(sm + j * 8192 + (2 * w + i) * 1024 + l * 16);
    if (w == 0) wait_flags(ws + WS_FH + (xcd * 32 + (l < 24 ? l : 0)) * 8, tag);
    LOADB(18); LOADB(19);
    }
    SCHED();
    TS_SET(7);
    BARRIER();
    asm volatile("" ::: "memory");
    TS_MARK();
    const char* asrc = ws + WS_AF + p3 * 48 * 4096 + w * 4096;
#define LOADA(ch)                                                                                                     \
  asm volatile("s_mov_b32 m0, %2\n\ts_nop 3\n\tglobal_load_lds_dwordx4 %0, %1 sc1\n\tglobal_load_lds_dwordx4 %0, %1 offset:1024 sc1\n\t" \
               "global_load_lds_dwordx4 %0, %1 offset:2048 sc1\n\tglobal_load_lds_dwordx4 %0, %1 offset:3072 sc1" ::"v"(l * 16), \
               "s"(asrc + (ch) * 32768), "s"(smb + ((ch) & 3) * 32768 + w * 4096) : "memory")
    LOADA(0); LOADA(1); LOADA(2); LOADA(3);
    const u64 tag = next_epoch(*(const u64*)(sm + G_TAG));
    f32x4 acc[4][2];
#pragma unroll
    for (int m = 0; m < 4; ++m) { acc[m][0] = (f32x4){0.f, 0.f, 0.f, 0.f}; acc[m][1] = acc[m][0]; }
    const LDS_AS char* ap[2];
#pragma unroll
    for (int i = 0; i < 2; ++i) { ap[i] = (const LDS_AS char*)sm + i * 65536 + kh * 4096 + l * 16; asm volatile("" : "+v"(ap[i])); }
    u32x4 A[2][4], V[11], xres[2];
    u32x2 mi[5];
    unsigned orow[2];
    u64 fv = 0;
    const unsigned vo = WS_VT + (c0 * 41 + 2 * g) * VP + (p3 >> 3) * 2048 + ((l >> 3) & 1) * 1024 + ((p3 & 7) * 8 + (l & 7)) * 16;
#define LOADV()                                                                                                       \
  do {                                                                                                                \
    _Pragma("unroll") for (int u = 0; u < 10; ++u) V[u] = LD128_DEV2(rw, vo, ((u >> 1) * 8 + (u & 1)) * VP);         \
    V[10] = LD128_DEV2(rw, vo - 2 * g * VP, 40 * VP);                                                                 \
  } while (0)
    bool early = false;
    WAIT_V(12);
    BARRIER();
    TS_MARK();
#pragma unroll
    for (int m = 0; m < 4; ++m) A[0][m] = *(const LDS_AS u32x4*)(ap[0] + m * 1024);
    SCHED();
#pragma unroll
    for (int j = 0; j < 24; ++j) {
      if ((j & 3) == 3 && j < 23) {
        if (j == 3) WAIT_V(8); else if (j == 7) WAIT_V(16); else if (j == 11) WAIT_V(17); else if (j == 15) WAIT_V(5);
        BARRIER();
        if (j < 8) LOADA(4 + (j >> 2));
      }
#pragma unroll
      for (int n = 0; n < 8; ++n) {
        acc[n & 3][n >> 2] = MFMA16_BF(A[j & 1][n & 3], B[j % NB][n >> 2], acc[n & 3][n >> 2]);
        if (n < 4 && j < 23) A[(j + 1) & 1][n] = *(const LDS_AS u32x4*)(ap[((j + 1) >> 3) & 1] + ((j + 1) & 7) * 8192 + n * 1024);
        SCHED();
      }
      if (j == 3) {
#pragma unroll
        for (int k = NB; k < 24; ++k) LOADB(k);
      }
      if (j == 10) fv = ld_dev(ws + WS_FV + (l < 41 ? l : 0) * 8);
      if (j == 15) {
        WAIT_V(0);
        early = all_lanes(fv == tag);
        asm volatile("" ::: "memory");
        if (early) LOADV();
#pragma unroll
        for (int ks = 0; ks < 5; ++ks) mi[ks] = *(const u32x2*)&g_T80.i[ct][ks][l][0];
        const rsrc_t rx = mk_rsrc(x, 2048 * 640 * 2);
#pragma unroll
        for (int r = 0; r < 2; ++r) {
          orow[r] = s_of_rho(p3 * 64 + 8 * ((4 * g + 2 * kh + r) & 7) + 4 * (g >> 1) + (w & 3)) * 1280 + min(d1s + i16, 79) * 16;
          xres[r] = __builtin_amdgcn_raw_buffer_load_b128(rx, orow[r], 0, 0);
        }
      }
      SCHED();
      if (j == 11) TS_MARK();
    }
#undef LOADA
#undef LOADB
    TS_MARK();
    if (!early) {
      wait_flags(ws + WS_FV + (l < 41 ? l : 0) * 8, tag);
      LOADV();
    }
#undef LOADV
    char* tp = sm + 65536 + kh * 32768 + l * 16;
#pragma unroll
    for (int m = 0; m < 4; ++m) *(f32x4*)(tp + (m * 8 + (c0 ^ 1)) * 1024) = acc[m][1];
    if (g == 0) {
#pragma unroll
      for (int m = 0; m < 4; ++m) V[0][m] = (V[0][m] & 0xffffu) | (V[10][m] << 16);
    }
#pragma unroll
    for (int ks = 0; ks < 5; ++ks)
#pragma unroll
      for (int m = 0; m < 4; ++m) acc[m][0] = MFMA16K16_BF(((u32x2){V[2 * ks][m], V[2 * ks + 1][m]}), mi[ks], acc[m][0]);
#pragma unroll
    for (int m = 0; m < 4; ++m) *(f32x4*)(tp + (m * 8 + c0) * 1024) = acc[m][0];
    BARRIER();
    TS_MARK();
    f32x2 pp[2][8];
#pragma unroll
    for (int k = 0; k < 2; ++k)
#pragma unroll
      for (int c = 0; c < 8; ++c) pp[k][c] = *(const f32x2*)(sm + 65536 + k * 32768 + ((w & 3) * 8 + c) * 1024 + l * 16 + kh * 8);
    SCHED();
    const rsrc_t ro = mk_rsrc(out, 2048 * 640 * 2);
#pragma unroll
    for (int r = 0; r < 2; ++r) {
      u32x4 o;
#pragma unroll
      for (int c = 0; c < 4; ++c)
        o[c] = pk_bf((pp[0][2 * c][r] + pp[1][2 * c][r]) + bf_lo(xres[r][c]), (pp[0][2 * c + 1][r] + pp[1][2 * c + 1][r]) + bf_hi(xres[r][c]));
      if (i16 < nd1) ST128(o, ro, orow[r]);
    }
    TS_MARK();
    TS_END(0, 0);
    return;
  }
  const int aux = idx * 8 + xcd;
  u64 tag;
  f16x2 tc[8], tsn[8];
  u32x4 G1[8], G3[8];
  u32x4 wo_[3];
  u16x2 m2 = {0, 0};
  const u16* wl;
  unsigned wdst;
  int wst;
  u32x4 dv[8];
  const int djob = w < 4 ? aux * 4 + w : (w == 4 ? 319 - aux : 288);
  {
    const int row = 4 * w + g, i16 = l & 15;
    dma1(l * 16, (const char*)&g_T80 + w * 2048, smb + A_T80 + w * 2048);
    dma1(l * 16, (const char*)&g_T80 + w * 2048 + 1024, smb + A_T80 + w * 2048 + 1024);
    const u32x4* xp = (const u32x4*)(x + s_of_rho(aux * 32 + row) * 640) + i16;
    u32x4 xv[5];
#pragma unroll
    for (int c = 0; c < 5; ++c) xv[c] = xp[c * 16];
    const u64 ep = ld_dev(ws + WS_EP);
    SCHED();
    {
      const float a0 = (float)(aux * w) * (1.f / 640.f), a1 = (float)(l & 31) * (1.f / 2048.f);
#pragma unroll
      for (int e = 0; e < 8; ++e) {
        const float a = a0 + a1 * (float)(2 * (e & 1) + 8 * (e >> 1) + 4 * (l >> 5)), a2 = a + a1;
        tc[e] = as_h2(pk_h(__builtin_amdgcn_cosf(a), __builtin_amdgcn_cosf(a2)));
        tsn[e] = as_h2(pk_h(__builtin_amdgcn_sinf(a), __builtin_amdgcn_sinf(a2)));
        asm volatile("" : "+v"(tc[e]), "+v"(tsn[e]));
      }
    }
    SCHED();
    WAIT_V(6);
    BARRIER();
    u32x4 fm[15];
#pragma unroll
    for (int u = 0; u < 15; ++u) fm[u] = *(const u32x4*)(sm + A_T80 + u * 1024 + l * 16);
    SCHED();
    TS_MARK();
    float f[40];
#pragma unroll
    for (int c = 0; c < 5; ++c)
#pragma unroll
      for (int j = 0; j < 4; ++j) { f[c * 8 + 2 * j] = bf_lo(xv[c][j]); f[c * 8 + 2 * j + 1] = bf_hi(xv[c][j]); }
    tag = next_epoch(ep);
    asm volatile("" ::"v"(tag), "v"(f[39]));
    SCHED();
    {
      const char* wsrc = (const char*)(w < 4 ? w_r : w_i) + (64 * (4 * (aux >> 3) + (w & 3)) + 8 * (aux & 7)) * 642;
#pragma unroll
      for (int i = 0; i < 5; ++i) dma1nt(l * 16, wsrc + i * 1024, smb + A_W + w * 5152 + i * 1024);
      if (l == 0) dma1nt(0, wsrc + 5120, smb + A_W + w * 5152 + 5120);
      dma1(l * 16, (const char*)&g_TF + w * 2048, smb + A_TF + w * 2048);
      dma1(l * 16, (const char*)&g_TF + w * 2048 + 1024, smb + A_TF + w * 2048 + 1024);
    }
    if (djob < 288) {
      const int F = djob / 6, jc = djob - F * 6, lg = min(l / 14, 3), d1 = min(13 * jc + (jc < 2 ? jc : 2) + min(l - lg * 14, 13), 79);
      const rsrc_t rd_ = mk_rsrc(wd, 1536 * 640 * 2);
#pragma unroll
      for (int j = 0; j < 8; ++j) dv[j] = __builtin_amdgcn_raw_buffer_load_b128(rd_, (32 * F + 4 * lg + (j & 3) + 16 * (j >> 2)) * 1280 + d1 * 16, 0, 0);
    }
    SCHED();
    float s4[4] = {0.f, 0.f, 0.f, 0.f};
#pragma unroll
    for (int e = 0; e < 40; ++e) s4[e & 3] += f[e];
    const float mean = sum16((s4[0] + s4[1]) + (s4[2] + s4[3])) * (1.f / 640.f);
    float q4[4] = {0.f, 0.f, 0.f, 0.f};
#pragma unroll
    for (int e = 0; e < 40; ++e) { f[e] -= mean; q4[e & 3] += f[e] * f[e]; }
    const float mb = rnd_bf(mean), vb = rnd_bf(sum16((q4[0] + q4[1]) + (q4[2] + q4[3])) * (1.f / 640.f));
    const float sd = rnd_bf(__builtin_amdgcn_sqrtf(rnd_bf(vb + 1e-5f)));
    const float rs = __builtin_amdgcn_rcpf(sd), off = (mean - mb) * rs;
    if (i16 == 0) st_dev(ws + WS_ST + (aux * 32 + row) * 8, (tag << 32) | (f2u(mb) >> 16) | f2u(sd));
#pragma unroll
    for (int c = 0; c < 5; ++c) {
      u32x4 oh;
#pragma unroll
      for (int j = 0; j < 4; ++j) oh[j] = pk_h(f[c * 8 + 2 * j] * rs + off, f[c * 8 + 2 * j + 1] * rs + off);
      *(u32x4*)(sm + A_XH + row * 1344 + (c * 16 + i16) * 16) = oh;
    }
    TS_MARK();
    {
      const int h = l >> 5, p = l & 3;
      const char* rd = sm + A_XH + (4 * w + 2 * (g & 1) + (p >> 1)) * 1344 + (8 * h + ((l & 15) >> 2)) * 16 + (p & 1) * 8;
      u32x4 ax[5];
#pragma unroll
      for (int ks = 0; ks < 5; ++ks) {
        const u32x2 lo = tr_read(rd + ks * 256), hi = tr_read(rd + ks * 256 + 64);
        ax[ks] = (u32x4){lo[0], lo[1], hi[0], hi[1]};
      }
      SCHED();
      const int ol = l & 31;
      char* ut = sm + A_UT + (w >> 1) * 10304 + (ol >> 1) * 256 + (w & 1) * 8;
      const int ub = (8 * h + (ol & 1)) ^ (ol & 14);
      f32x16 acc[3];
#pragma unroll
      for (int nt = 0; nt < 3; ++nt) {
#pragma unroll
        for (int e = 0; e < 16; ++e) acc[nt][e] = 0.f;
#pragma unroll
        for (int ks = 0; ks < 5; ++ks) acc[nt] = MFMA32_H(ax[ks], fm[nt * 5 + ks], acc[nt]);
      }
#pragma unroll
      for (int nt = 0; nt < 3; ++nt)
        if (nt < 2 || ol < 16) {
#pragma unroll
          for (int dl = 0; dl < 4; ++dl)
            *(u32x2*)(ut + nt * 4096 + ((ub ^ (2 * dl)) << 4)) = (u32x2){pk_h(acc[nt][dl], acc[nt][4 + dl]), pk_h(acc[nt][8 + dl], acc[nt][12 + dl])};
        }
    }
    WAIT_V(1);
    BARRIER();
    TS_MARK();
    {
      u32x4 uv[5];
#pragma unroll
      for (int i = 0; i < 5; ++i) {
        const int seg = i * 128 + (tid >> 2);
        uv[i] = *(const u32x4*)(sm + A_UT + (tid & 3) * 10304 + ((seg ^ ((seg >> 3) & 14)) << 4));
      }
      SCHED();
#pragma unroll
      for (int i = 0; i < 5; ++i) ST128(uv[i], rw, WS_UB + (i * 8 + w) * CP + aux * 1024 + l * 16);
    }
    if (djob < 288 && l < 56) {
      const int F = djob / 6;
#pragma unroll
      for (int c = 0; c < 8; ++c) {
        u32x4 o;
#pragma unroll
        for (int j = 0; j < 4; ++j) o[j] = __builtin_amdgcn_perm(dv[2 * j + 1][c >> 1], dv[2 * j][c >> 1], (c & 1) ? 0x07060302u : 0x05040100u);
        ST128(o, rw, WS_BF + ((djob - F * 6) * 48 + F) * 7168 + c * 896 + l * 16);
      }
    }
    SCHED();
    const int eq = l & 3, kb = (l >> 2) & 7, cl = 2 * (w & 3) + (l >> 5), half = w >> 2;
    int fr = 64 * (4 * (aux >> 3) + eq) + 8 * (aux & 7) + kb;
    if (half) fr = (2048 - fr) & 2047;
    wl = (const u16*)(sm + A_W) + eq * 2576 + kb * 321 + (half ? 80 - cl : cl);
    wdst = WS_WT + cl * CP + (half * 2048 + (fr >> 8) * 256 + (fr & 63) * 4 + ((fr >> 6) & 3)) * 16;
    wst = half ? -8 : 8;
#define WT_READ(i0)                                                                                                   \
  do {                                                                                                                \
    _Pragma("unroll") for (int i_ = 0; i_ < 3; ++i_)                                                                  \
      _Pragma("unroll") for (int j_ = 0; j_ < 4; ++j_)                                                                \
        wo_[i_][j_] = __builtin_bit_cast(unsigned, ((u16x2){(wl + wst * ((i0) + i_))[80 * j_], (wl + wst * ((i0) + i_))[10304 + 80 * j_]})); \
    _Pragma("unroll") for (int i_ = 0; i_ < 3; ++i_) {                                                                \
      _Pragma("unroll") for (int j_ = 0; j_ < 4; ++j_) m2 = __builtin_elementwise_max(m2, __builtin_bit_cast(u16x2, wo_[i_][j_] & 0x7fff7fffu)); \
      if (w >= 4) wo_[i_] = (u32x4){wo_[i_][3], wo_[i_][2], wo_[i_][1], wo_[i_][0]} ^ 0x80000000u;                    \
      asm volatile("" : "+v"(wo_[i_]));                                                                               \
    }                                                                                                                 \
  } while (0)
#define WT_STORE(i0)                                                                                                  \
  do {                                                                                                                \
    _Pragma("unroll") for (int i_ = 0; i_ < 3; ++i_)                                                                  \
      if ((i0) + i_ < 5 || ((w & 3) == 0 && l < 32)) ST128(wo_[i_], rw, wdst + ((i0) + i_) * 8 * CP);                 \
  } while (0)
    WT_READ(0);
    SCHED();
    TS_MARK();
    WAIT_V(0);
    __builtin_amdgcn_s_barrier();
    if (tid == 0) st_dev(ws + WS_F1 + aux * 8, tag);
    TS_MARK();
    WT_STORE(0);
    SCHED();
    WT_READ(3);
    WT_STORE(3);
    const unsigned mx = max64(max((unsigned)m2[0], (unsigned)m2[1]));
    if (l == 0) ((unsigned*)(sm + A_MX))[w] = mx;
    SCHED();
    TS_MARK();
  }
#define PUBLISH_F2()                                                                                                  \
  do {                                                                                                                \
    if (tid == 0) {                                                                                                   \
      const u32x4 m0_ = *(const u32x4*)(sm + A_MX), m1_ = *(const u32x4*)(sm + A_MX + 16);                            \
      const unsigned mx_ = max(max(max(m0_[0], m0_[1]), max(m0_[2], m0_[3])), max(max(m1_[0], m1_[1]), max(m1_[2], m1_[3]))); \
      st_dev(ws + WS_F2 + aux * 8, (tag & ~0xffffull) | mx_);                                                         \
    }                                                                                                                 \
  } while (0)
  WAIT_V(0);
  BARRIER();
  PUBLISH_F2();
  TS_MARK();
  if (aux < 41) {
    const int c1 = aux, c1s = c1 == 40 ? 0 : c1;
    const bool real_cls = c1s == 0;
    char* eb = sm + A_E;
    const u32x4 g8 = *(const u32x4*)&g_TF.g8[l][0][0];
#pragma unroll
    for (int i = 0; i < 8; ++i) { G1[i] = *(const u32x4*)(sm + A_TF + i * 1024 + l * 16); G3[i] = *(const u32x4*)(sm + A_TF + 8192 + i * 1024 + l * 16); }
    SCHED();
    wait_flags(ws + WS_F1 + l * 8, tag);
    TS_MARK();
    u32x4 ar[4], ai[4];
    {
      const unsigned uo = WS_UB + c1s * CP + (l >> 2) * 1024 + w * 128 + (l & 3) * 16;
#pragma unroll
      for (int i = 0; i < 4; ++i) {
        ar[i] = LD128_DEV(rw, uo + (c1 == 40 ? 64 : 0) + i * 16384);
        ai[i] = LD128_DEV(rw, uo + 64 + i * 16384);
      }
    }
    u64 f2 = ld_dev(ws + WS_F2 + l * 8);
#pragma unroll
    for (int i = 0; i < 8; ++i) asm volatile("" : "+v"(G1[i]), "+v"(G3[i]));
    SCHED();
    if (real_cls) {
#pragma unroll
      for (int i = 0; i < 4; ++i) ai[i] = (u32x4){0u, 0u, 0u, 0u};
    }
    char* erow = eb + (2 * w) * 4096 + ((l ^ e_key(w)) << 4);
    f32x16 zero;
#pragma unroll
    for (int e = 0; e < 16; ++e) zero[e] = 0.f;
    u32x2 wv[16];
    const unsigned wbase = WS_WT + c1 * CP + (g >> 1) * 32768 + (g & 1) * 8 + (l & 15) * 16 + ((4 * ((w >> 1) & 1) + (w & 1)) * 64 + 32 * (w >> 2)) * 64;
    bool early;
    {
      f32x16 dr[2], di[2];
#pragma unroll
      for (int t = 0; t < 2; ++t) {
        dr[t] = zero; di[t] = zero;
#pragma unroll
        for (int ks = 0; ks < 2; ++ks) {
          const u32x4 C = G1[(t * 2 + ks) * 2], S = G1[(t * 2 + ks) * 2 + 1];
          dr[t] = MFMA32_H(ar[t * 2 + ks], C, dr[t]);
          di[t] = MFMA32_H(ai[t * 2 + ks], C, di[t]);
          dr[t] = MFMA32_H(ai[t * 2 + ks], S, dr[t]);
          di[t] = MFMA32_H(neg_h(ar[t * 2 + ks]), S, di[t]);
        }
      }
      SCHED();
      TS_MARK();
      early = all_lanes((f2 >> 16) == (tag >> 16));
      asm volatile("" ::: "memory");
      if (early) {
#pragma unroll
        for (int i = 0; i < 16; ++i) wv[i] = LD64_DEV(rw, wbase + (i & 1) * 8192 + (i >> 1) * 256);
      }
      SCHED();
      u32x4 br[2][2], bi[2][2];
      bfly_tw<false>(dr, di, tc, tsn, br, bi);
#pragma unroll
      for (int t2 = 0; t2 < 2; ++t2) {
        f32x16 xr_ = zero, xi_ = zero;
#pragma unroll
        for (int ks = 0; ks < 2; ++ks) {
          const u32x4 C = G3[(t2 * 2 + ks) * 2], S = G3[(t2 * 2 + ks) * 2 + 1];
          xr_ = MFMA32_H(C, br[t2][ks], xr_);
          xi_ = MFMA32_H(C, bi[t2][ks], xi_);
          xr_ = MFMA32_H(S, bi[t2][ks], xr_);
          xi_ = MFMA32_H(S, neg_h(br[t2][ks]), xi_);
        }
        u32x4 o0[2], o1[2];
        pack_h(xr_, o0);
        pack_h(xi_, o1);
#pragma unroll
        for (int s2 = 0; s2 < 2; ++s2) {
          *(u32x4*)(erow + (t2 * 2 + s2) * 1024) = o0[s2];
          *(u32x4*)(erow + 4096 + (t2 * 2 + s2) * 1024) = swap_halves(o1[s2]);
        }
      }
    }
    SCHED();
    TS_MARK();
    if (!early) {
      f2 = wait_flags(ws + WS_F2 + l * 8, tag);
#pragma unroll
      for (int i = 0; i < 16; ++i) wv[i] = LD64_DEV(rw, wbase + (i & 1) * 8192 + (i >> 1) * 256);
    }
    float osc;
    u32x2 gi;
    {
      int ex = (int)(max64((unsigned)f2 & 0xffffu) >> 7);
      ex = ex < 8 ? 8 : (ex > 240 ? 240 : ex);
      const float up = u2f((unsigned)(250 - ex) << 23);
      gi = (u32x2){pk_bf(bf_lo(g8[2]) * up, bf_hi(g8[2]) * up), pk_bf(bf_lo(g8[3]) * up, bf_hi(g8[3]) * up)};
      osc = u2f((unsigned)(ex + 2) << 23) * ((real_cls ? 2.f : 4.f) * 256.f / (239.f * 640.f));
    }
    BARRIER();
    TS_MARK();
    {
      const int rr = 4 * g + ((l & 15) >> 2), wrw = l & 15;
      const int rdo = rr * 4096 + w * 512 + (((l & 3) ^ e_key(rr >> 1)) << 4) + (rr & 1) * 8;
      const int wro = wrw * 4096 + w * 512 + ((g ^ e_key(wrw >> 1)) << 4) + (wrw & 1) * 8;
      const u32x2 gf = {g8[0], g8[1]};
      const f32x4 z4 = {0.f, 0.f, 0.f, 0.f};
      u32x2 ev[16];
#pragma unroll
      for (int i = 0; i < 16; ++i) ev[i] = tr_read(eb + (rdo ^ (((i >> 1) & 3) * 64 + (i & 1) * 8)) + (i >> 3) * 256);
      SCHED();
      f32x4 xx[16], vv[16];
      xx[0] = MFMA16K16_H(gf, ev[0], z4);
      xx[1] = MFMA16K16_H(gf, ev[1], z4);
#pragma unroll
      for (int i = 0; i < 18; ++i) {
        if (i + 2 < 16) xx[i + 2] = MFMA16K16_H(gf, ev[i + 2], z4);
        if (i < 16) {
          const f32x4 xs = xx[i];
          const float r0 = bf_lo(wv[i][0]), i0 = bf_hi(wv[i][0]), r1 = bf_lo(wv[i][1]), i1 = bf_hi(wv[i][1]);
          const u32x2 y = {pk_bf(xs[0] * r0 - xs[1] * i0, xs[0] * i0 + xs[1] * r0), pk_bf(xs[2] * r1 - xs[3] * i1, xs[2] * i1 + xs[3] * r1)};
          vv[i] = MFMA16K16_BF(y, gi, z4);
        }
        if (i >= 2) {
          const int k = i - 2;
          *(u32x2*)(eb + (wro ^ (((k >> 1) & 3) * 64 + (k & 1) * 8)) + (k >> 3) * 256) = (u32x2){pk_h(vv[k][0], vv[k][1]), pk_h(vv[k][2], vv[k][3])};
        }
        SCHED();
      }
    }
    BARRIER();
    TS_MARK();
    {
      f32x16 dr[2], di[2];
      u32x4 yr[4], yi[4];
#pragma unroll
      for (int i = 0; i < 4; ++i) { yr[i] = *(const u32x4*)(erow + i * 1024); yi[i] = *(const u32x4*)(erow + 4096 + i * 1024); }
      SCHED();
#pragma unroll
      for (int t2 = 0; t2 < 2; ++t2) {
        dr[t2] = zero; di[t2] = zero;
#pragma unroll
        for (int s2 = 0; s2 < 2; ++s2) {
          const u32x4 pr_ = yr[t2 * 2 + s2], pi_ = swap_halves(yi[t2 * 2 + s2]);
          const u32x4 C = G1[(t2 * 2 + s2) * 2], S = G1[(t2 * 2 + s2) * 2 + 1];
          dr[t2] = MFMA32_H(pr_, C, dr[t2]);
          di[t2] = MFMA32_H(pi_, C, di[t2]);
          dr[t2] = MFMA32_H(neg_h(pi_), S, dr[t2]);
          di[t2] = MFMA32_H(pr_, S, di[t2]);
        }
      }
      u32x4 qr[2][2], qi[2][2];
      bfly_tw<true>(dr, di, tc, tsn, qr, qi);
      const unsigned vo = WS_VT + (w * 41 + c1) * VP + l * 16;
#pragma unroll
      for (int t = 0; t < 2; ++t) {
        f32x16 zr = zero, zq = zero;
#pragma unroll
        for (int ks = 0; ks < 2; ++ks) {
          const u32x4 C = G3[(t * 2 + ks) * 2], S = G3[(t * 2 + ks) * 2 + 1];
          zr = MFMA32_H(C, qr[t][ks], zr);
          zq = MFMA32_H(C, qi[t][ks], zq);
          zr = MFMA32_H(S, neg_h(qi[t][ks]), zr);
          zq = MFMA32_H(S, qr[t][ks], zq);
        }
#pragma unroll
        for (int u = 0; u < 4; ++u) {
          u32x4 o;
#pragma unroll
          for (int j = 0; j < 4; ++j) o[j] = pk_bf(zr[4 * u + j] * osc, zq[4 * u + j] * osc);
          ST128(o, rw, vo + (t * 4 + u) * 1024);
        }
      }
    }
    WAIT_V(0);
    __builtin_amdgcn_s_barrier();
    if (tid == 0) st_dev(ws + WS_FV + c1 * 8, tag);
    TS_END(0, 1);
    return;
  }
  if (aux == 63 && w == 0) {
    wait_flags(ws + WS_F1 + l * 8, tag);
#pragma unroll 1
    for (int i = 0; i < 3; ++i) wait_flags(ws + WS_FA + i * 512 + l * 8, tag);
    if (l == 0) st_dev(ws + WS_EP, tag);
  }
  TS_END(0, 2);
}

#if defined(__HIP_DEVICE_COMPILE__)
#pragma clang attribute pop
#endif

extern "C" void kernel_launch(void* const* d_in, const int* in_sizes, int n_in, void* d_out, int out_size, void* d_ws, size_t ws_size,
                              hipStream_t stream) {
  const u16* x = (const u16*)d_in[0];
  const u16* w_r = (const u16*)d_in[1];
  const u16* w_i = (const u16*)d_in[2];
  const u16* wg = (const u16*)d_in[4];
  const u16* wu = (const u16*)d_in[5];
  const u16* wd = (const u16*)d_in[6];
  char* ws = (char*)d_ws;
  ka<<<256, 512, 0, stream>>>(x, w_r, w_i, wg, wu, wd, ws, (u16*)d_out);
}
```

```cpp
#include <hip/hip_runtime.h>
#include <hip/hip_bf16.h>

typedef unsigned short u16;
typedef __attribute__((ext_vector_type(8))) __bf16 bf16x8;
typedef __attribute__((ext_vector_type(2))) __bf16 bf16x2;
typedef __attribute__((ext_vector_type(8))) _Float16 f16x8;
typedef __attribute__((ext_vector_type(4))) _Float16 f16x4;
typedef __attribute__((ext_vector_type(2))) _Float16 f16x2;
typedef __attribute__((ext_vector_type(4))) short s16x4;
typedef __attribute__((ext_vector_type(2))) float f32x2;
typedef __attribute__((ext_vector_type(4))) float f32x4;
typedef __attribute__((ext_vector_type(16))) float f32x16;
typedef __attribute__((ext_vector_type(4))) unsigned u32x4;
typedef __attribute__((ext_vector_type(2))) unsigned u32x2;
typedef __attribute__((ext_vector_type(2))) u16 u16x2;
typedef __amdgpu_buffer_rsrc_t rsrc_t;
#define DI __device__ __forceinline__
#define LDS_AS __attribute__((address_space(3)))

namespace tb {
constexpr double kPi = 3.14159265358979323846264338327950288;
constexpr double sinT(double x) { double x2 = x * x, t = x, s = x; for (int i = 1; i < 12; ++i) { t *= -x2 / ((2 * i) * (2 * i + 1)); s += t; } return s; }
constexpr double cosT(double x) { double x2 = x * x, t = 1, s = 1; for (int i = 1; i < 12; ++i) { t *= -x2 / ((2 * i - 1) * (2 * i)); s += t; } return s; }
constexpr double cos2pi(int q, int den) {
  q %= den; if (2 * q > den) q = den - q;
  double sg = 1; if (4 * q > den) { q = den / 2 - q; sg = -1; }
  if (8 * q > den) return sg * sinT(2 * kPi * (den / 4 - q) / den);
  return sg * cosT(2 * kPi * q / den);
}
constexpr u16 bfbits(double d) { unsigned u = __builtin_bit_cast(unsigned, (float)d); u += 0x7fffu + ((u >> 16) & 1u); return (u16)(u >> 16); }
constexpr u16 hbits(double d) { return __builtin_bit_cast(u16, (_Float16)d); }
constexpr int krow(int s, int h, int j) { return 16 * s + 8 * (j >> 2) + 4 * h + (j & 3); }
template <int N> struct CT { double c[N]; };
template <int N> constexpr CT<N> mkC() { CT<N> c{}; for (int i = 0; i < N; ++i) c.c[i] = cos2pi(i, N); return c; }
constexpr CT<64> c64 = mkC<64>();
constexpr CT<80> c80 = mkC<80>();
constexpr double m80(int o, int d) { return o == 1 ? c80.c[(40 * d) % 80] : c80.c[((o >> 1) * d + ((o & 1) ? 20 : 0)) % 80]; }
struct alignas(16) TabF {
  u16 g1[2][2][2][64][8];
  u16 g3[2][2][2][64][8];
  u16 g8[64][2][4];
};
constexpr TabF mkTabF() {
  TabF T{};
  for (int t = 0; t < 2; ++t) for (int ks = 0; ks < 2; ++ks) for (int l = 0; l < 64; ++l) for (int j = 0; j < 8; ++j) {
    const int r = l & 31, k = krow(ks, l >> 5, j);
    const int q1 = (r * (2 * k + t)) & 63, q3 = (k * (2 * r + t)) & 63;
    T.g1[t][ks][0][l][j] = hbits(c64.c[q1] * 0.25); T.g1[t][ks][1][l][j] = hbits(c64.c[(q1 + 48) & 63] * 0.25);
    T.g3[t][ks][0][l][j] = hbits(c64.c[q3] * 0.25); T.g3[t][ks][1][l][j] = hbits(c64.c[(q3 + 48) & 63] * 0.25);
  }
  for (int l = 0; l < 64; ++l) for (int e = 0; e < 4; ++e) {
    const int m = l & 15, k = 4 * (l >> 4) + e, q = (8 * (m >> 1) * (k >> 1)) & 63;
    const double C = c64.c[q], S = c64.c[(q + 48) & 63];
    T.g8[l][0][e] = hbits(0.25 * ((m & 1) == (k & 1) ? C : ((m & 1) ? -S : S)));
    T.g8[l][1][e] = bfbits(239. / 256. * ((m & 1) == (k & 1) ? C : ((k & 1) ? -S : S)));
  }
  return T;
}
struct alignas(16) Tab80 {
  u16 f[3][5][64][8];
  u16 pad[512];
  u16 i[6][5][64][4];
};
constexpr Tab80 mkTab80() {
  Tab80 T{};
  for (int n = 0; n < 3; ++n) for (int ks = 0; ks < 5; ++ks) for (int l = 0; l < 64; ++l) for (int j = 0; j < 8; ++j)
    T.f[n][ks][l][j] = 32 * n + (l & 31) < 80 ? hbits(m80(32 * n + (l & 31), 16 * ks + 8 * (l >> 5) + j)) : (u16)0;
  for (int n = 0; n < 6; ++n) for (int ks = 0; ks < 5; ++ks) for (int l = 0; l < 64; ++l) for (int e = 0; e < 4; ++e)
    T.i[n][ks][l][e] = bfbits(m80(16 * ks + 4 * (l >> 4) + e, 13 * n + (n < 2 ? n : 2) + (l & 15)));
  return T;
}
}
static __device__ const tb::TabF g_TF = tb::mkTabF();
static __device__ const tb::Tab80 g_T80 = tb::mkTab80();

constexpr unsigned WS_EP = 0;
constexpr unsigned WS_F1 = 256;
constexpr unsigned WS_F2 = WS_F1 + 512;
constexpr unsigned WS_FV = WS_F2 + 512;
constexpr unsigned WS_FH = WS_FV + 512;
constexpr unsigned WS_FA = 4096;
constexpr unsigned WS_ST = 8192;
constexpr unsigned CP = 65536 + 256;
constexpr unsigned WS_UB = WS_ST + 2048 * 8;
constexpr unsigned WS_WT = WS_UB + 40 * CP;
constexpr unsigned WS_AF = WS_WT + 41 * CP;
constexpr unsigned WS_BF = WS_AF + 32 * 48 * 4096;
constexpr unsigned VP = 8192 + 256;
constexpr unsigned WS_VT = WS_BF + 6 * 48 * 7168;
constexpr unsigned WS_END = WS_VT + 328 * VP;

#if defined(__HIP_DEVICE_COMPILE__)
#pragma clang attribute push(__attribute__((target("no-packed-fp32-ops"))), apply_to = function)
#endif
DI float u2f(unsigned u) { return __builtin_bit_cast(float, u); }
DI unsigned f2u(float f) { return __builtin_bit_cast(unsigned, f); }
DI bool all_lanes(bool p) { return __builtin_amdgcn_ballot_w64(!p) == 0; }
DI int s_of_rho(int rho) {
  const int t = rho >> 10, ks = (rho >> 9) & 1, h = (rho >> 8) & 1, r = (rho >> 3) & 31, jh = (rho >> 2) & 1, jl = rho & 3;
  return 64 * (16 * ks + 8 * jh + 4 * h + jl) + 32 * t + r;
}
DI float bf_lo(unsigned u) { return u2f(u << 16); }
DI float bf_hi(unsigned u) { return u2f(u & 0xffff0000u); }
DI unsigned pk_bf(float a, float b) { f32x2 f = {a, b}; return __builtin_bit_cast(unsigned, __builtin_convertvector(f, bf16x2)); }
DI unsigned pk_h(float a, float b) { f32x2 f = {a, b}; return __builtin_bit_cast(unsigned, __builtin_convertvector(f, f16x2)); }
DI float rnd_bf(float a) { return bf_lo(pk_bf(a, 0.f)); }
#define MFMA16_BF(a, b, c) __builtin_amdgcn_mfma_f32_16x16x32_bf16(__builtin_bit_cast(bf16x8, a), __builtin_bit_cast(bf16x8, b), c, 0, 0, 0)
#define MFMA32_BF(a, b, c) __builtin_amdgcn_mfma_f32_32x32x16_bf16(__builtin_bit_cast(bf16x8, a), __builtin_bit_cast(bf16x8, b), c, 0, 0, 0)
#define MFMA32_H(a, b, c) __builtin_amdgcn_mfma_f32_32x32x16_f16(__builtin_bit_cast(f16x8, a), __builtin_bit_cast(f16x8, b), c, 0, 0, 0)
#define MFMA16K16_H(a, b, c) __builtin_amdgcn_mfma_f32_16x16x16f16(__builtin_bit_cast(f16x4, a), __builtin_bit_cast(f16x4, b), c, 0, 0, 0)
#define MFMA16K16_BF(a, b, c) __builtin_amdgcn_mfma_f32_16x16x16bf16_1k(__builtin_bit_cast(s16x4, a), __builtin_bit_cast(s16x4, b), c, 0, 0, 0)
DI u32x2 tr_read(const char* p) {
  s16x4 v = __builtin_amdgcn_ds_read_tr16_b64_v4i16((LDS_AS s16x4*)(p));
  return __builtin_bit_cast(u32x2, v);
}
DI rsrc_t mk_rsrc(const void* p, unsigned bytes) { return __builtin_amdgcn_make_buffer_rsrc((void*)p, 0, bytes, 0x00020000); }
#define ST128(v, r, off) __builtin_amdgcn_raw_buffer_store_b128(v, r, off, 0, 16)
#define ST64(v, r, off) __builtin_amdgcn_raw_buffer_store_b64(v, r, off, 0, 16)
#define WAIT_V(n) asm volatile("s_waitcnt vmcnt(%0)" ::"n"(n) : "memory")
#define WAIT_L(n) asm volatile("s_waitcnt lgkmcnt(%0)" ::"n"(n) : "memory")
#define SCHED() __builtin_amdgcn_sched_barrier(0)
#define BARRIER() do { WAIT_L(0); __builtin_amdgcn_s_barrier(); asm volatile("" ::: "memory"); } while (0)
DI void dma1(unsigned voff, const void* sbase, unsigned lds) {
  asm volatile("s_mov_b32 m0, %2\n\ts_nop 3\n\tglobal_load_lds_dwordx4 %0, %1" ::"v"(voff), "s"(sbase), "s"(lds) : "memory");
}
DI void dma1nt(unsigned voff, const void* sbase, unsigned lds) {
  asm volatile("s_mov_b32 m0, %2\n\ts_nop 3\n\tglobal_load_lds_dwordx4 %0, %1 nt" ::"v"(voff), "s"(sbase), "s"(lds) : "memory");
}
template <int OFF> DI void dma1q(unsigned voff, const void* sbase, unsigned lds) {
  asm volatile("s_add_u32 m0, %2, %3\n\ts_nop 0\n\tglobal_load_lds_dwordx4 %0, %1" ::"v"(voff), "s"(sbase), "s"(lds), "n"(OFF) : "memory", "scc");
}
template <int CTRL> DI unsigned dpp_u(unsigned x) { return (unsigned)__builtin_amdgcn_mov_dpp((int)x, CTRL, 0xf, 0xf, true); }
template <int CTRL> DI float dpp_f(float x) { return u2f(dpp_u<CTRL>(f2u(x))); }
DI float sum16(float x) {
  x += dpp_f<0xB1>(x); x += dpp_f<0x4E>(x); x += dpp_f<0x141>(x); x += dpp_f<0x128>(x);
  return x;
}
DI float sum_rows(float x) {
  const auto s = __builtin_amdgcn_permlane16_swap(f2u(x), f2u(x), false, false);
  x = u2f(s[0]) + u2f(s[1]);
  const auto t = __builtin_amdgcn_permlane32_swap(f2u(x), f2u(x), false, false);
  return u2f(t[0]) + u2f(t[1]);
}
typedef unsigned long long u64;
typedef __attribute__((address_space(1))) u64 gu64;
#define RLX_AGENT __ATOMIC_RELAXED, __HIP_MEMORY_SCOPE_AGENT
DI u64 ld_dev(const char* p) { return __hip_atomic_load((gu64*)p, RLX_AGENT); }
DI void st_dev(char* p, u64 v) { __hip_atomic_store((gu64*)p, v, RLX_AGENT); }
DI u64 next_epoch(u64 e) { return e * 0x9E3779B97F4A7C15ull + 0x632BE59BD9B4E019ull; }
constexpr unsigned SPIN_CAP = 1u << 18;
DI u64 wait_flags(const char* p, u64 tag) {
  u64 v = 0;
#pragma unroll 1
  for (unsigned n = 0; n < SPIN_CAP; ++n) {
    v = ld_dev(p);
    if (all_lanes((v >> 16) == (tag >> 16))) break;
    __builtin_amdgcn_s_sleep(1);
  }
  asm volatile("" ::: "memory");
  return v;
}
#define LD128_DEV(r, off) __builtin_amdgcn_raw_buffer_load_b128(r, off, 0, 16)
#define LD128_DEV2(r, off, soff) __builtin_amdgcn_raw_buffer_load_b128(r, off, soff, 16)
#define LD64_DEV(r, off) __builtin_amdgcn_raw_buffer_load_b64(r, off, 0, 16)
DI unsigned max64(unsigned x) {
  x = max(x, dpp_u<0xB1>(x)); x = max(x, dpp_u<0x4E>(x)); x = max(x, dpp_u<0x141>(x)); x = max(x, dpp_u<0x128>(x));
  const auto s = __builtin_amdgcn_permlane16_swap(x, x, false, false);
  x = max(s[0], s[1]);
  const auto t = __builtin_amdgcn_permlane32_swap(x, x, false, false);
  return max(t[0], t[1]);
}
DI unsigned lds_addr(const char* p) { return (unsigned)(size_t)(LDS_AS const char*)p; }

#define TS_DECL
#define TS_SET(n)
#define TS_MARK()
#define TS_END(k, role)

#define MAPV 0
#define PFPOL "nt"
#define WPF 0
#define DMAW dma1
constexpr int CODE_LINES = 290;
constexpr int STAGE_B = 49152;
constexpr int G_CS = 3 * STAGE_B;
constexpr int G_ST = G_CS + 512;
constexpr int G_PF = G_ST + 2048;
constexpr int G_TAG = G_PF + 1024;
constexpr int G_SN = G_TAG + 1024;
constexpr int A_TF = 0;
constexpr int A_T80 = 16384;
constexpr int A_XH = 32768;
constexpr int A_UT = A_XH + 32 * 1344;
constexpr int A_W = A_UT + 4 * 10304;
constexpr int A_MX = A_W + 8 * 5152;
constexpr int A_E = 16384;
constexpr int KA_LDS = A_MX + 64;
static_assert(G_SN + 6144 <= KA_LDS && KA_LDS <= 163840, "lds");

DI void pack_h(const f32x16& v, u32x4 (&o)[2]) {
#pragma unroll
  for (int s = 0; s < 2; ++s)
#pragma unroll
    for (int j = 0; j < 4; ++j) o[s][j] = pk_h(v[8 * s + 2 * j], v[8 * s + 2 * j + 1]);
}
DI f16x2 as_h2(unsigned u) { return __builtin_bit_cast(f16x2, u); }
DI unsigned as_u(f16x2 v) { return __builtin_bit_cast(unsigned, v); }
template <bool INV> DI void bfly_tw(const f32x16 (&dr)[2], const f32x16 (&di)[2], const f16x2 (&tc)[8], const f16x2 (&ts)[8],
                                    u32x4 (&er)[2][2], u32x4 (&ei)[2][2]) {
#pragma unroll
  for (int e = 0; e < 8; ++e) {
    const f16x2 r0 = as_h2(pk_h(dr[0][2 * e], dr[0][2 * e + 1])), r1 = as_h2(pk_h(dr[1][2 * e], dr[1][2 * e + 1]));
    const f16x2 i0 = as_h2(pk_h(di[0][2 * e], di[0][2 * e + 1])), i1 = as_h2(pk_h(di[1][2 * e], di[1][2 * e + 1]));
#pragma unroll
    for (int t = 0; t < 2; ++t) {
      const f16x2 sr = t ? r0 - r1 : r0 + r1, si = t ? i0 - i1 : i0 + i1;
      const f16x2 a = sr * tc[e], c = si * tc[e];
      er[t][e >> 2][e & 3] = as_u(__builtin_elementwise_fma(INV ? -si : si, ts[e], a));
      ei[t][e >> 2][e & 3] = as_u(__builtin_elementwise_fma(INV ? sr : -sr, ts[e], c));
    }
  }
}
DI u32x4 neg_h(u32x4 v) { return v ^ 0x80008000u; }
DI u32x4 swap_halves(u32x4 v) { return (u32x4){v[2], v[3], v[0], v[1]}; }
DI int e_key(int d2) { return ((d2 & 2) << 2) | ((d2 & 1) << 2) | ((d2 & 4) >> 1) | ((d2 & 2) >> 1); }

__global__ __launch_bounds__(512, 2) void ka(const u16* __restrict__ x, const u16* __restrict__ w_r, const u16* __restrict__ w_i,
                                             const u16* __restrict__ wg, const u16* __restrict__ wu, const u16* __restrict__ wd,
                                             char* __restrict__ ws, u16* __restrict__ out) {
  __shared__ __attribute__((aligned(1024))) char sm[KA_LDS];
  u64 pc0;
  asm volatile("s_getpc_b64 %0" : "=s"(pc0));
  pc0 &= ~255ull;
  const int b = blockIdx.x, tid = threadIdx.x, l = tid & 63;
  const int w = __builtin_amdgcn_readfirstlane(tid >> 6);
  const int xcd = b & 7, idx = b >> 3;
  const unsigned smb = lds_addr(sm);
  const rsrc_t rw = mk_rsrc(ws, WS_END);
  const int g = l >> 4;
  TS_DECL TS_MARK();
  if (idx >= 8) {
    const int gi = idx - 8;
    const int panel = (xcd >> 2) * 4 + (gi & 3), ft = (xcd & 3) * 6 + (gi >> 2);
    const int gwg = gi * 8 + xcd;
    const int p3 = xcd * 4 + (gi & 3), ct = gi >> 2, d1s = 13 * ct + (ct < 2 ? ct : 2), nd1 = ct < 2 ? 14 : 13;
    const int i16 = l & 15, kh = w >> 2, c0 = 2 * (w & 3) + kh;
    constexpr int NB = 18;
    u32x4 B[NB][2];
    const unsigned bo = WS_BF + (ct * 48 + kh) * 7168 + g * 224 + min(i16, 13) * 16;
#define LOADB(j) do { B[(j) % NB][0] = LD128_DEV2(rw, bo + c0 * 896, (j) * 14336); B[(j) % NB][1] = LD128_DEV2(rw, bo + (c0 ^ 1) * 896, (j) * 14336); } while (0)
    if (w >= 4) {
      const int lw = w - 4, r8 = l >> 3, c8 = l & 7;
      asm volatile("s_mov_b32 m0, %2\n\ts_nop 3\n\tglobal_load_lds_dword %0, %1 sc1" ::"v"(WS_EP + (l & 1) * 4), "s"(ws), "s"(smb + G_TAG + lw * 256) : "memory");
      unsigned xo[2], woff[2];
#pragma unroll
      for (int v = 0; v < 2; ++v) {
        xo[v] = (s_of_rho(panel * 256 + 2 * lw + v) + r8) * 1280 + ((c8 ^ ((r8 >> 1) | ((lw >> 1) << 2))) << 4);
        woff[v] = ((16 * lw + 8 * v + r8) * 1536 + ft * 64) * 2 + ((c8 ^ ((((r8 >> 1) & 1) | (v << 1)) << 1)) << 4);
      }
      const char *xs = (const char*)x, *gs = (const char*)wg, *us = (const char*)wu;
      const unsigned dx = smb + lw * 8192, dw = smb + 32768 + lw * 2048;
#define STAGE(off)                                                                                   \
  do {                                                                                               \
    _Pragma("unroll") for (int v_ = 0; v_ < 2; ++v_) {                                               \
      dma1(woff[v_], gs, dw + (off) + v_ * 1024);                                                    \
      dma1(woff[v_], us, dw + (off) + 8192 + v_ * 1024);                                             \
    }                                                                                                \
    _Pragma("unroll") for (int i_ = 0; i_ < 8; ++i_) dma1(xo[i_ >> 2], xs + (i_ & 3) * 10240, dx + (off) + i_ * 1024); \
    xs += 128; gs += 64 * 3072; us += 64 * 3072;                                                     \
  } while (0)
      STAGE(0);
      STAGE(STAGE_B);
      WAIT_V(12);
      __builtin_amdgcn_s_barrier();
      const u64 tag = next_epoch(*(const u64*)(sm + G_TAG + lw * 256));
      const unsigned fao = WS_FA + ((ct * 48 + (l < 48 ? l : 0)) % 192) * 16;
      const int job = gwg + 192 * lw;
      if (job < 288) {
        const int jc = job / 48, F = job - jc * 48, lg = min(l / 14, 3), d1 = min(13 * jc + (jc < 2 ? jc : 2) + min(l - lg * 14, 13), 79);
        const unsigned d0 = (32 * F + 4 * lg) * 1280 + d1 * 16, d16 = d0 + 16 * 1280;
        u32x4 dv[8];
        asm volatile("global_load_dwordx4 %0, %8, %10\n\tglobal_load_dwordx4 %1, %8, %10 offset:1280\n\tglobal_load_dwordx4 %2, %8, %10 offset:2560\n\t"
                     "global_load_dwordx4 %3, %8, %10 offset:3840\n\tglobal_load_dwordx4 %4, %9, %10\n\tglobal_load_dwordx4 %5, %9, %10 offset:1280\n\t"
                     "global_load_dwordx4 %6, %9, %10 offset:2560\n\tglobal_load_dwordx4 %7, %9, %10 offset:3840"
                     : "=&v"(dv[0]), "=&v"(dv[1]), "=&v"(dv[2]), "=&v"(dv[3]), "=&v"(dv[4]), "=&v"(dv[5]), "=&v"(dv[6]), "=&v"(dv[7])
                     : "v"(d0), "v"(d16), "s"(wd) : "memory");
        STAGE(2 * STAGE_B);
        WAIT_V(20);
        __builtin_amdgcn_s_barrier();
        STAGE(0);
        asm volatile("s_waitcnt vmcnt(12)" : "+v"(dv[0]), "+v"(dv[1]), "+v"(dv[2]), "+v"(dv[3]), "+v"(dv[4]), "+v"(dv[5]), "+v"(dv[6]), "+v"(dv[7])::"memory");
        if (l < 56) {
#pragma unroll
          for (int c = 0; c < 8; ++c) {
            u32x4 o;
#pragma unroll
            for (int j = 0; j < 4; ++j) o[j] = __builtin_amdgcn_perm(dv[2 * j + 1][c >> 1], dv[2 * j][c >> 1], (c & 1) ? 0x07060302u : 0x05040100u);
            ST128(o, rw, WS_BF + job * 7168 + c * 896 + l * 16);
          }
        }
      } else {
        STAGE(2 * STAGE_B);
        WAIT_V(12);
        __builtin_amdgcn_s_barrier();
        STAGE(0);
        WAIT_V(12);
      }
      __builtin_amdgcn_s_barrier();
      STAGE(STAGE_B);
      if (job < 288) WAIT_V(20); else WAIT_V(12);
      __builtin_amdgcn_s_barrier();
      STAGE(2 * STAGE_B);
      WAIT_V(12);
      __builtin_amdgcn_s_barrier();
      if (tid == 256) st_dev(ws + WS_FA + gwg * 16, tag);
      STAGE(0);
      WAIT_V(12);
      __builtin_amdgcn_s_barrier();
      STAGE(STAGE_B);
      WAIT_V(12);
      __builtin_amdgcn_s_barrier();
      asm volatile("s_mov_b32 m0, %2\n\ts_nop 3\n\tglobal_load_lds_dwordx4 %0, %1 sc1" ::"v"(fao), "s"(ws), "s"(smb + G_SN + lw * 1536) : "memory");
      if (l < 32)
        asm volatile("s_mov_b32 m0, %2\n\ts_nop 3\n\tglobal_load_lds_dwordx4 %0, %1 sc1" ::"v"(WS_ST + (panel * 256 + lw * 64) * 8 + l * 16), "s"(ws),
                     "s"(smb + G_SN + lw * 1536 + 1024) : "memory");
      STAGE(2 * STAGE_B);
      WAIT_V(12);
      __builtin_amdgcn_s_barrier();
      STAGE(0);
      {
        u64 st = *(const u64*)(sm + G_SN + lw * 1536 + 1024 + l * 8);
        const u64 fa = *(const u64*)(sm + G_SN + lw * 1536 + l * 16);
        if (!all_lanes(fa == tag && (unsigned)(st >> 32) == (unsigned)tag)) {
          wait_flags(ws + fao, tag);
#pragma unroll 1
          for (unsigned n = 0; n < SPIN_CAP; ++n) {
            st = ld_dev(ws + WS_ST + (panel * 256 + tid - 256) * 8);
            if (all_lanes((unsigned)(st >> 32) == (unsigned)tag)) break;
            __builtin_amdgcn_s_sleep(4);
          }
        }
        asm volatile("" ::: "memory");
        const float rs = __builtin_amdgcn_rcpf(bf_hi((unsigned)st));
        *(f32x2*)(sm + G_ST + (tid - 256) * 8) = (f32x2){bf_lo((unsigned)st), rs * rs};
      }
      LOADB(0); LOADB(1); LOADB(2);
      SCHED();
      WAIT_V(18);
      WAIT_L(0);
#undef STAGE
      __builtin_amdgcn_s_barrier();
      const char* bsrc = ws + WS_BF + ct * 48 * 7168 + 2 * lw * 896;
      const unsigned bl = g * 224 + min(i16, 13) * 16;
#define STAGEB(j0)                                                                                                    \
  do {                                                                                                                \
    _Pragma("unroll") for (int i_ = (j0); i_ < (j0) + 6; ++i_)                                                        \
      asm volatile("s_mov_b32 m0, %3\n\ts_nop 0\n\tglobal_load_lds_dwordx4 %0, %2 sc1\n\tglobal_load_lds_dwordx4 %1, %2 offset:1024 sc1" \
                   ::"v"(bl + 128), "v"(bl), "s"(bsrc - 128 + i_ * 14336), "s"(smb + i_ * 8192 + lw * 2048) : "memory"); \
  } while (0)
      STAGEB(6);
      LOADB(3); LOADB(4);
      SCHED();
      WAIT_V(22);
      __builtin_amdgcn_s_barrier();
      STAGEB(12);
      LOADB(5); LOADB(6);
      SCHED();
      __builtin_amdgcn_s_barrier();
      STAGEB(0);
#undef STAGEB
#pragma unroll
      for (int j = 7; j < 12; ++j) LOADB(j);
      SCHED();
      WAIT_V(10);
      __builtin_amdgcn_s_barrier();
#pragma unroll
      for (int j = 12; j < 18; ++j) LOADB(j);
    } else {
    const int wr = w >> 1, fh = w & 1;
    {
    const int q = (l & 15) >> 2;
    const int xb0 = (i16 >> 3) * 16384 + (wr * 16 + (l & 7)) * 128 + ((g ^ (((l & 7) >> 1) | ((i16 >> 3) << 2))) << 4);
    const int wb0 = (32768 + (8 * g + q) * 128 + (((fh * 2) ^ (((q >> 1) & 1) | ((g & 1) << 1))) << 5) + (l & 3) * 8) ^ (wr << 13);
    const char* xr[2];
    const char* wp[4];
#define SET_BUF(off)                                                                                       \
  do {                                                                                                     \
    xr[0] = sm + (off) + xb0; xr[1] = sm + (off) + (xb0 ^ 64);                                             \
    _Pragma("unroll") for (int i_ = 0; i_ < 4; ++i_) wp[i_] = sm + (off) + (wb0 ^ ((i_ & 1) << 5) ^ ((i_ >> 1) << 13)); \
  } while (0)
    f32x4 acc[4][8];
#pragma unroll
    for (int i = 0; i < 4; ++i)
#pragma unroll
      for (int r = 0; r < 8; ++r) acc[i][r] = (f32x4){0.f, 0.f, 0.f, 0.f};
    f32x4 cs[2] = {{0.f, 0.f, 0.f, 0.f}, {0.f, 0.f, 0.f, 0.f}};
    u32x4 ones = {0x3f803f80u, 0x3f803f80u, 0x3f803f80u, 0x3f803f80u};
    asm volatile("" : "+v"(ones));
    u32x4 xf[2][8];
    u32x2 wf[2][8];
#define RD(ks, n)                                                                                          \
  do {                                                                                                     \
    if ((n) >= 2 && (n) < 10) xf[ks][(n) - 2] = *(const u32x4*)(xr[ks] + (((n) - 2) & 3) * 4096 + (((n) - 2) >> 2) * 1024); \
    else { const int m_ = (n) < 2 ? (n) : (n) - 8; wf[ks][m_] = tr_read(wp[m_ >> 1] + (ks) * 4096 + (m_ & 1) * 512); } \
  } while (0)
#define WF(ks, i) ((u32x4){wf[ks][2 * (i)][0], wf[ks][2 * (i)][1], wf[ks][2 * (i) + 1][0], wf[ks][2 * (i) + 1][1]})
#define KSTEP(ks, READS, CSUM)                                                                             \
  do {                                                                                                     \
    _Pragma("unroll") for (int n_ = 0; n_ < 32; ++n_) {                                                    \
      acc[n_ >> 3][n_ & 7] = MFMA16_BF(WF(ks, n_ >> 3), xf[ks][n_ & 7], acc[n_ >> 3][n_ & 7]);             \
      if (READS && n_ < 16) RD((ks) ^ 1, n_);                                                              \
      if (CSUM && n_ >= 30) cs[n_ & 1] = MFMA16_BF(WF(ks, n_ & 1), ones, cs[n_ & 1]);                      \
      SCHED();                                                                                             \
    }                                                                                                      \
  } while (0)
    SET_BUF(0);
    TS_MARK();
    BARRIER();
    TS_MARK();
#pragma unroll
    for (int n = 0; n < 16; ++n) RD(0, n);
    SCHED();
    KSTEP(0, true, 1);
    int nxt = STAGE_B;
#pragma unroll 1
    for (int u = 0; u < 9; ++u) {
      BARRIER();
      SET_BUF(nxt);
      SCHED();
      KSTEP(1, true, 1);
      KSTEP(0, true, 1);
      nxt = nxt == 2 * STAGE_B ? 0 : nxt + STAGE_B;
    }
    TS_MARK();
#pragma unroll
    for (int i = 0; i < 2; ++i) {
      cs[i] = MFMA16_BF(WF(1, i), ones, cs[i]);
      if ((l & 15) == 0) *(f32x4*)(sm + G_CS + w * 128 + i * 64 + g * 16) = cs[i];
    }
    BARRIER();
    f32x4 co[2];
    f32x4 st[4];
#pragma unroll
    for (int i = 0; i < 2; ++i) co[i] = *(const f32x4*)(sm + G_CS + (w ^ 2) * 128 + i * 64 + g * 16);
#pragma unroll
    for (int r = 0; r < 4; ++r) st[r] = *(const f32x4*)(sm + G_ST + (wr * 128 + (r >> 1) * 64 + 8 * (l & 7) + 4 * (i16 >> 3) + (r & 1) * 2) * 8);
    SCHED();
    TS_MARK();
    const unsigned ao = WS_AF + ((panel * 4 + wr * 2) * 48 + ft * 2 + fh) * 4096 + l * 16;
#pragma unroll
    for (int r = 0; r < 10; ++r) {
      if (r < 8) {
#pragma unroll
        for (int i = 0; i < 4; ++i) acc[i][r] = MFMA16_BF(WF(1, i), xf[1][r], acc[i][r]);
      }
      if (r >= 2) {
        const int e = r - 2;
        const float mb = st[e >> 1][(e & 1) * 2], r2 = st[e >> 1][(e & 1) * 2 + 1];
        u32x4 o;
#pragma unroll
        for (int i = 0; i < 2; ++i) {
          const f32x4 hv = ((acc[i][e] - mb * cs[i]) * r2) * (acc[2 + i][e] - mb * co[i]);
          o[2 * i] = pk_bf(hv[0], hv[1]); o[2 * i + 1] = pk_bf(hv[2], hv[3]);
        }
        ST128(o, rw, ao + (e >> 2) * 48 * 4096 + (e & 3) * 1024);
      }
      SCHED();
    }
#undef SET_BUF
#undef RD
#undef WF
#undef KSTEP
    }
    SCHED();
    TS_MARK();
    WAIT_V(0);
    __builtin_amdgcn_s_barrier();
    asm volatile("" ::: "memory");
    TS_MARK();
    const u64 tag = next_epoch(*(const u64*)(sm + G_TAG));
    if (tid == 0) st_dev(ws + WS_FH + (panel * 32 + ft) * 8, tag);
    SCHED();
#pragma unroll
    for (int j = 0; j < 18; ++j)
#pragma unroll
      for (int i = 0; i < 2; ++i) B[j][i] = *(const u32x4*)(sm + j * 8192 + (2 * w + i) * 1024 + l * 16);
    SCHED();
    if (w == 0) wait_flags(ws + WS_FH + (xcd * 32 + (l < 24 ? l : 0)) * 8, tag);
    }
#pragma unroll
    for (int j = 0; j < NB; ++j) asm volatile("" : "+v"(B[j][0]), "+v"(B[j][1]));
    SCHED();
    TS_SET(7);
    BARRIER();
    asm volatile("" ::: "memory");
    TS_MARK();
    const char* asrc = ws + WS_AF + p3 * 48 * 4096 + w * 4096;
#define LOADA(ch)                                                                                                     \
  asm volatile("s_mov_b32 m0, %2\n\ts_nop 3\n\tglobal_load_lds_dwordx4 %0, %1 sc1\n\tglobal_load_lds_dwordx4 %0, %1 offset:1024 sc1\n\t" \
               "global_load_lds_dwordx4 %0, %1 offset:2048 sc1\n\tglobal_load_lds_dwordx4 %0, %1 offset:3072 sc1" ::"v"(l * 16), \
               "s"(asrc + (ch) * 32768), "s"(smb + ((ch) & 3) * 32768 + w * 4096) : "memory")
    LOADA(0); LOADA(1);
    const u64 tag = next_epoch(*(const u64*)(sm + G_TAG));
    f32x4 acc[4][2];
#pragma unroll
    for (int m = 0; m < 4; ++m) { acc[m][0] = (f32x4){0.f, 0.f, 0.f, 0.f}; acc[m][1] = acc[m][0]; }
    const LDS_AS char* ap[2];
#pragma unroll
    for (int i = 0; i < 2; ++i) { ap[i] = (const LDS_AS char*)sm + i * 65536 + kh * 4096 + l * 16; asm volatile("" : "+v"(ap[i])); }
    u32x4 A[3][4], V[11], xres[2];
    u32x2 mi[5];
    unsigned orow[2];
    u64 fv = 0;
    const unsigned vo = WS_VT + (c0 * 41 + 2 * g) * VP + (p3 >> 3) * 2048 + ((l >> 3) & 1) * 1024 + ((p3 & 7) * 8 + (l & 7)) * 16;
#define LOADV()                                                                                                       \
  do {                                                                                                                \
    _Pragma("unroll") for (int u = 0; u < 10; ++u) V[u] = LD128_DEV2(rw, vo, ((u >> 1) * 8 + (u & 1)) * VP);         \
    V[10] = LD128_DEV2(rw, vo - 2 * g * VP, 40 * VP);                                                                 \
  } while (0)
    bool early = false;
    WAIT_V(4);
    BARRIER();
    TS_MARK();
#define ZPART()                                                                                                       \
  do {                                                                                                                \
    if (g == 0) {                                                                                                     \
      _Pragma("unroll") for (int m = 0; m < 4; ++m) V[0][m] = (V[0][m] & 0xffffu) | (V[10][m] << 16);     \
    }                                                                                                                 \
    _Pragma("unroll") for (int ks = 0; ks < 5; ++ks)                                                                  \
      _Pragma("unroll") for (int m = 0; m < 4; ++m)                                                                   \
        acc[m][0] = MFMA16K16_BF(((u32x2){V[2 * ks][m], V[2 * ks + 1][m]}), mi[ks], acc[m][0]);                       \
  } while (0)
#pragma unroll
    for (int m = 0; m < 8; ++m) A[m >> 2][m & 3] = *(const LDS_AS u32x4*)(ap[0] + (m >> 2) * 8192 + (m & 3) * 1024);
    SCHED();
#pragma unroll
    for (int j = 0; j < 24; ++j) {
      if ((j & 3) == 2 && j < 22) {
        if (j == 2) WAIT_V(8); else if (j == 6) WAIT_V(14); else if (j == 10) WAIT_V(21); else if (j == 14) WAIT_V(7);
        BARRIER();
      }
#pragma unroll
      for (int n = 0; n < 8; ++n) {
        acc[n & 3][n >> 2] = MFMA16_BF(A[j % 3][n & 3], B[j % NB][n >> 2], acc[n & 3][n >> 2]);
        if (n < 4 && j < 22) A[(j + 2) % 3][n] = *(const LDS_AS u32x4*)(ap[((j + 2) >> 3) & 1] + ((j + 2) & 7) * 8192 + n * 1024);
        if (n == 4) {
          if (j < 3) LOADA(2 + j);
          if (j == 6) LOADA(5);
          if (j >= 3 && j < 6) LOADB(j + 15);
          if (j >= 7 && j < 10) LOADB(j + 14);
          if (j == 9) fv = ld_dev(ws + WS_FV + (l < 41 ? l : 0) * 8);
          if (j == 12) {
#pragma unroll
            for (int ks = 0; ks < 5; ++ks) mi[ks] = *(const u32x2*)&g_T80.i[ct][ks][l][0];
            const rsrc_t rx = mk_rsrc(x, 2048 * 640 * 2);
#pragma unroll
            for (int r = 0; r < 2; ++r) {
              orow[r] = s_of_rho(p3 * 64 + 8 * ((4 * g + 2 * kh + r) & 7) + 4 * (g >> 1) + (w & 3)) * 1280 + min(d1s + i16, 79) * 16;
              xres[r] = __builtin_amdgcn_raw_buffer_load_b128(rx, orow[r], 0, 0);
            }
          }
        }
        SCHED();
      }
      if (j == 14) {
        early = all_lanes(fv == tag);
        asm volatile("" ::: "memory");
        LOADV();
      }
      if (j == 21) {
        if (!early) {
          wait_flags(ws + WS_FV + (l < 41 ? l : 0) * 8, tag);
          LOADV();
        }
#pragma unroll
        for (int u = 0; u < 11; ++u) asm volatile("" : "+v"(V[u]));
        ZPART();
      }
      SCHED();
      if (j == 11) TS_MARK();
    }
#undef LOADA
#undef LOADB
    TS_MARK();
#undef LOADV
#undef ZPART
    char* tp = sm + 65536 + kh * 16384 + l * 8;
#pragma unroll
    for (int m = 0; m < 4; ++m)
#pragma unroll
      for (int i = 0; i < 2; ++i)
        *(u32x2*)(tp + (m * 8 + (c0 ^ i)) * 512) = (u32x2){pk_bf(acc[m][i][0], acc[m][i][1]), pk_bf(acc[m][i][2], acc[m][i][3])};
    BARRIER();
    TS_MARK();
    unsigned pp[2][8];
#pragma unroll
    for (int k = 0; k < 2; ++k)
#pragma unroll
      for (int c = 0; c < 8; ++c) pp[k][c] = *(const unsigned*)(sm + 65536 + k * 16384 + ((w & 3) * 8 + c) * 512 + l * 8 + kh * 4);
    SCHED();
    const rsrc_t ro = mk_rsrc(out, 2048 * 640 * 2);
#pragma unroll
    for (int r = 0; r < 2; ++r) {
      u32x4 o;
#pragma unroll
      for (int c = 0; c < 4; ++c) {
        const float a = r ? bf_hi(pp[0][2 * c]) + bf_hi(pp[1][2 * c]) : bf_lo(pp[0][2 * c]) + bf_lo(pp[1][2 * c]);
        const float b2 = r ? bf_hi(pp[0][2 * c + 1]) + bf_hi(pp[1][2 * c + 1]) : bf_lo(pp[0][2 * c + 1]) + bf_lo(pp[1][2 * c + 1]);
        o[c] = pk_bf(a + bf_lo(xres[r][c]), b2 + bf_hi(xres[r][c]));
      }
      if (i16 < nd1) ST128(o, ro, orow[r]);
    }
    TS_MARK();
    TS_END(0, 0);
    return;
  }
  const int aux = idx * 8 + xcd;
  u64 tag;
  f16x2 tc[8], tsn[8];
  u32x4 G1[8], G3[8];
  u32x4 wo_[3];
  u16x2 m2 = {0, 0};
  const u16* wl;
  unsigned wdst;
  int wst;
  {
    const int row = 4 * w + g, i16 = l & 15;
    dma1(l * 16, (const char*)&g_T80 + w * 2048, smb + A_T80 + w * 2048);
    dma1(l * 16, (const char*)&g_T80 + w * 2048 + 1024, smb + A_T80 + w * 2048 + 1024);
    const u32x4* xp = (const u32x4*)(x + s_of_rho(aux * 32 + row) * 640) + i16;
    u32x4 xv[5];
#pragma unroll
    for (int c = 0; c < 5; ++c) xv[c] = xp[c * 16];
    const u64 ep = ld_dev(ws + WS_EP);
    SCHED();
    {
      const float a0 = (float)(aux * w) * (1.f / 640.f), a1 = (float)(l & 31) * (1.f / 2048.f);
#pragma unroll
      for (int e = 0; e < 8; ++e) {
        const float a = a0 + a1 * (float)(2 * (e & 1) + 8 * (e >> 1) + 4 * (l >> 5)), a2 = a + a1;
        tc[e] = as_h2(pk_h(__builtin_amdgcn_cosf(a), __builtin_amdgcn_cosf(a2)));
        tsn[e] = as_h2(pk_h(__builtin_amdgcn_sinf(a), __builtin_amdgcn_sinf(a2)));
        asm volatile("" : "+v"(tc[e]), "+v"(tsn[e]));
      }
    }
    SCHED();
    WAIT_V(6);
    BARRIER();
    u32x4 fm[15];
#pragma unroll
    for (int u = 0; u < 15; ++u) fm[u] = *(const u32x4*)(sm + A_T80 + u * 1024 + l * 16);
    SCHED();
    TS_MARK();
    float f[40];
#pragma unroll
    for (int c = 0; c < 5; ++c)
#pragma unroll
      for (int j = 0; j < 4; ++j) { f[c * 8 + 2 * j] = bf_lo(xv[c][j]); f[c * 8 + 2 * j + 1] = bf_hi(xv[c][j]); }
    tag = next_epoch(ep);
    asm volatile("" ::"v"(tag), "v"(f[39]));
    SCHED();
    {
      const char* wsrc = (const char*)(w < 4 ? w_r : w_i) + (64 * (4 * (aux >> 3) + (w & 3)) + 8 * (aux & 7)) * 642;
#pragma unroll
      for (int i = 0; i < 5; ++i) DMAW(l * 16, wsrc + i * 1024, smb + A_W + w * 5152 + i * 1024);
      if (l == 0) DMAW(0, wsrc + 5120, smb + A_W + w * 5152 + 5120);
      dma1(l * 16, (const char*)&g_TF + w * 2048, smb + A_TF + w * 2048);
      dma1(l * 16, (const char*)&g_TF + w * 2048 + 1024, smb + A_TF + w * 2048 + 1024);
    }
    SCHED();
    float s4[4] = {0.f, 0.f, 0.f, 0.f};
#pragma unroll
    for (int e = 0; e < 40; ++e) s4[e & 3] += f[e];
    const float mean = sum16((s4[0] + s4[1]) + (s4[2] + s4[3])) * (1.f / 640.f);
    float q4[4] = {0.f, 0.f, 0.f, 0.f};
#pragma unroll
    for (int e = 0; e < 40; ++e) { f[e] -= mean; q4[e & 3] += f[e] * f[e]; }
    const float mb = rnd_bf(mean), vb = rnd_bf(sum16((q4[0] + q4[1]) + (q4[2] + q4[3])) * (1.f / 640.f));
    const float sd = rnd_bf(__builtin_amdgcn_sqrtf(rnd_bf(vb + 1e-5f)));
    const float rs = __builtin_amdgcn_rcpf(sd), off = (mean - mb) * rs;
    if (i16 == 0) st_dev(ws + WS_ST + (aux * 32 + row) * 8, (tag << 32) | (f2u(mb) >> 16) | f2u(sd));
#pragma unroll
    for (int c = 0; c < 5; ++c) {
      u32x4 oh;
#pragma unroll
      for (int j = 0; j < 4; ++j) oh[j] = pk_h(f[c * 8 + 2 * j] * rs + off, f[c * 8 + 2 * j + 1] * rs + off);
      *(u32x4*)(sm + A_XH + row * 1344 + (c * 16 + i16) * 16) = oh;
    }
    TS_MARK();
    {
      const int h = l >> 5, p = l & 3;
      const char* rd = sm + A_XH + (4 * w + 2 * (g & 1) + (p >> 1)) * 1344 + (8 * h + ((l & 15) >> 2)) * 16 + (p & 1) * 8;
      u32x4 ax[5];
#pragma unroll
      for (int ks = 0; ks < 5; ++ks) {
        const u32x2 lo = tr_read(rd + ks * 256), hi = tr_read(rd + ks * 256 + 64);
        ax[ks] = (u32x4){lo[0], lo[1], hi[0], hi[1]};
      }
      SCHED();
      const int ol = l & 31;
      char* ut = sm + A_UT + (w >> 1) * 10304 + (ol >> 1) * 256 + (w & 1) * 8;
      const int ub = (8 * h + (ol & 1)) ^ (ol & 14);
      f32x16 acc[3];
#pragma unroll
      for (int nt = 0; nt < 3; ++nt) {
#pragma unroll
        for (int e = 0; e < 16; ++e) acc[nt][e] = 0.f;
#pragma unroll
        for (int ks = 0; ks < 5; ++ks) acc[nt] = MFMA32_H(ax[ks], fm[nt * 5 + ks], acc[nt]);
      }
#pragma unroll
      for (int nt = 0; nt < 3; ++nt)
        if (nt < 2 || ol < 16) {
#pragma unroll
          for (int dl = 0; dl < 4; ++dl)
            *(u32x2*)(ut + nt * 4096 + ((ub ^ (2 * dl)) << 4)) = (u32x2){pk_h(acc[nt][dl], acc[nt][4 + dl]), pk_h(acc[nt][8 + dl], acc[nt][12 + dl])};
        }
    }
    WAIT_V(1);
    BARRIER();
    TS_MARK();
    {
      u32x4 uv[5];
#pragma unroll
      for (int i = 0; i < 5; ++i) {
        const int seg = i * 128 + (tid >> 2);
        uv[i] = *(const u32x4*)(sm + A_UT + (tid & 3) * 10304 + ((seg ^ ((seg >> 3) & 14)) << 4));
      }
      SCHED();
#pragma unroll
      for (int i = 0; i < 5; ++i) ST128(uv[i], rw, WS_UB + (i * 8 + w) * CP + aux * 1024 + l * 16);
    }
    SCHED();
    const int eq = l & 3, kb = (l >> 2) & 7, cl = 2 * (w & 3) + (l >> 5), half = w >> 2;
    int fr = 64 * (4 * (aux >> 3) + eq) + 8 * (aux & 7) + kb;
    if (half) fr = (2048 - fr) & 2047;
    wl = (const u16*)(sm + A_W) + eq * 2576 + kb * 321 + (half ? 80 - cl : cl);
    wdst = WS_WT + cl * CP + (half * 2048 + (fr >> 8) * 256 + (fr & 63) * 4 + ((fr >> 6) & 3)) * 16;
    wst = half ? -8 : 8;
#define WT_READ(i0)                                                                                                   \
  do {                                                                                                                \
    _Pragma("unroll") for (int i_ = 0; i_ < 3; ++i_)                                                                  \
      _Pragma("unroll") for (int j_ = 0; j_ < 4; ++j_)                                                                \
        wo_[i_][j_] = __builtin_bit_cast(unsigned, ((u16x2){(wl + wst * ((i0) + i_))[80 * j_], (wl + wst * ((i0) + i_))[10304 + 80 * j_]})); \
    _Pragma("unroll") for (int i_ = 0; i_ < 3; ++i_) {                                                                \
      _Pragma("unroll") for (int j_ = 0; j_ < 4; ++j_) m2 = __builtin_elementwise_max(m2, __builtin_bit_cast(u16x2, wo_[i_][j_] & 0x7fff7fffu)); \
      if (w >= 4) wo_[i_] = (u32x4){wo_[i_][3], wo_[i_][2], wo_[i_][1], wo_[i_][0]} ^ 0x80000000u;                    \
      asm volatile("" : "+v"(wo_[i_]));                                                                               \
    }                                                                                                                 \
  } while (0)
#define WT_STORE(i0)                                                                                                  \
  do {                                                                                                                \
    _Pragma("unroll") for (int i_ = 0; i_ < 3; ++i_)                                                                  \
      if ((i0) + i_ < 5 || ((w & 3) == 0 && l < 32)) ST128(wo_[i_], rw, wdst + ((i0) + i_) * 8 * CP);                 \
  } while (0)
    WT_READ(0);
    SCHED();
    TS_MARK();
    WAIT_V(0);
    __builtin_amdgcn_s_barrier();
    if (tid == 0) st_dev(ws + WS_F1 + aux * 8, tag);
    TS_MARK();
    WT_STORE(0);
    SCHED();
    WT_READ(3);
    WT_STORE(3);
    const unsigned mx = max64(max((unsigned)m2[0], (unsigned)m2[1]));
    if (l == 0) ((unsigned*)(sm + A_MX))[w] = mx;
    SCHED();
    TS_MARK();
  }
#define PUBLISH_F2()                                                                                                  \
  do {                                                                                                                \
    if (tid == 0) {                                                                                                   \
      const u32x4 m0_ = *(const u32x4*)(sm + A_MX), m1_ = *(const u32x4*)(sm + A_MX + 16);                            \
      const unsigned mx_ = max(max(max(m0_[0], m0_[1]), max(m0_[2], m0_[3])), max(max(m1_[0], m1_[1]), max(m1_[2], m1_[3]))); \
      st_dev(ws + WS_F2 + aux * 8, (tag & ~0xffffull) | mx_);                                                         \
    }                                                                                                                 \
  } while (0)
  WAIT_V(0);
  BARRIER();
  PUBLISH_F2();
  TS_MARK();
  if (aux < 41) {
    const int c1 = aux, c1s = c1 == 40 ? 0 : c1;
    const bool real_cls = c1s == 0;
    char* eb = sm + A_E;
    const u32x4 g8 = *(const u32x4*)&g_TF.g8[l][0][0];
#pragma unroll
    for (int i = 0; i < 8; ++i) { G1[i] = *(const u32x4*)(sm + A_TF + i * 1024 + l * 16); G3[i] = *(const u32x4*)(sm + A_TF + 8192 + i * 1024 + l * 16); }
    SCHED();
    wait_flags(ws + WS_F1 + l * 8, tag);
    TS_MARK();
    u32x4 ar[4], ai[4];
    {
      const unsigned uo = WS_UB + c1s * CP + (l >> 2) * 1024 + w * 128 + (l & 3) * 16;
#pragma unroll
      for (int i = 0; i < 4; ++i) {
        ar[i] = LD128_DEV(rw, uo + (c1 == 40 ? 64 : 0) + i * 16384);
        ai[i] = LD128_DEV(rw, uo + 64 + i * 16384);
      }
    }
    u64 f2 = ld_dev(ws + WS_F2 + l * 8);
#pragma unroll
    for (int i = 0; i < 8; ++i) asm volatile("" : "+v"(G1[i]), "+v"(G3[i]));
    SCHED();
    if (real_cls) {
#pragma unroll
      for (int i = 0; i < 4; ++i) ai[i] = (u32x4){0u, 0u, 0u, 0u};
    }
    char* erow = eb + (2 * w) * 4096 + ((l ^ e_key(w)) << 4);
    f32x16 zero;
#pragma unroll
    for (int e = 0; e < 16; ++e) zero[e] = 0.f;
    u32x2 wv[16];
    const unsigned wbase = WS_WT + c1 * CP + (g >> 1) * 32768 + (g & 1) * 8 + (l & 15) * 16 + ((4 * ((w >> 1) & 1) + (w & 1)) * 64 + 32 * (w >> 2)) * 64;
    bool early;
    {
      f32x16 dr[2], di[2];
#pragma unroll
      for (int t = 0; t < 2; ++t) {
        dr[t] = zero; di[t] = zero;
#pragma unroll
        for (int ks = 0; ks < 2; ++ks) {
          const u32x4 C = G1[(t * 2 + ks) * 2], S = G1[(t * 2 + ks) * 2 + 1];
          dr[t] = MFMA32_H(ar[t * 2 + ks], C, dr[t]);
          di[t] = MFMA32_H(ai[t * 2 + ks], C, di[t]);
          dr[t] = MFMA32_H(ai[t * 2 + ks], S, dr[t]);
          di[t] = MFMA32_H(neg_h(ar[t * 2 + ks]), S, di[t]);
        }
      }
      SCHED();
      TS_MARK();
      early = all_lanes((f2 >> 16) == (tag >> 16));
      asm volatile("" ::: "memory");
      if (early) {
#pragma unroll
        for (int i = 0; i < 16; ++i) wv[i] = LD64_DEV(rw, wbase + (i & 1) * 8192 + (i >> 1) * 256);
      }
      SCHED();
      u32x4 br[2][2], bi[2][2];
      bfly_tw<false>(dr, di, tc, tsn, br, bi);
#pragma unroll
      for (int t2 = 0; t2 < 2; ++t2) {
        f32x16 xr_ = zero, xi_ = zero;
#pragma unroll
        for (int ks = 0; ks < 2; ++ks) {
          const u32x4 C = G3[(t2 * 2 + ks) * 2], S = G3[(t2 * 2 + ks) * 2 + 1];
          xr_ = MFMA32_H(C, br[t2][ks], xr_);
          xi_ = MFMA32_H(C, bi[t2][ks], xi_);
          xr_ = MFMA32_H(S, bi[t2][ks], xr_);
          xi_ = MFMA32_H(S, neg_h(br[t2][ks]), xi_);
        }
        u32x4 o0[2], o1[2];
        pack_h(xr_, o0);
        pack_h(xi_, o1);
#pragma unroll
        for (int s2 = 0; s2 < 2; ++s2) {
          *(u32x4*)(erow + (t2 * 2 + s2) * 1024) = o0[s2];
          *(u32x4*)(erow + 4096 + (t2 * 2 + s2) * 1024) = swap_halves(o1[s2]);
        }
      }
    }
    SCHED();
    TS_MARK();
    if (!early) {
      f2 = wait_flags(ws + WS_F2 + l * 8, tag);
#pragma unroll
      for (int i = 0; i < 16; ++i) wv[i] = LD64_DEV(rw, wbase + (i & 1) * 8192 + (i >> 1) * 256);
    }
    float osc;
    u32x2 gi;
    {
      int ex = (int)(max64((unsigned)f2 & 0xffffu) >> 7);
      ex = ex < 8 ? 8 : (ex > 240 ? 240 : ex);
      const float up = u2f((unsigned)(250 - ex) << 23);
      gi = (u32x2){pk_bf(bf_lo(g8[2]) * up, bf_hi(g8[2]) * up), pk_bf(bf_lo(g8[3]) * up, bf_hi(g8[3]) * up)};
      osc = u2f((unsigned)(ex + 2) << 23) * ((real_cls ? 2.f : 4.f) * 256.f / (239.f * 640.f));
    }
    BARRIER();
    TS_MARK();
    {
      const int rr = 4 * g + ((l & 15) >> 2), wrw = l & 15;
      const int rdo = rr * 4096 + w * 512 + (((l & 3) ^ e_key(rr >> 1)) << 4) + (rr & 1) * 8;
      const int wro = wrw * 4096 + w * 512 + ((g ^ e_key(wrw >> 1)) << 4) + (wrw & 1) * 8;
      const u32x2 gf = {g8[0], g8[1]};
      const f32x4 z4 = {0.f, 0.f, 0.f, 0.f};
      u32x2 ev[16];
#pragma unroll
      for (int i = 0; i < 16; ++i) ev[i] = tr_read(eb + (rdo ^ (((i >> 1) & 3) * 64 + (i & 1) * 8)) + (i >> 3) * 256);
      SCHED();
      f32x4 xx[16], vv[16];
      xx[0] = MFMA16K16_H(gf, ev[0], z4);
      xx[1] = MFMA16K16_H(gf, ev[1], z4);
#pragma unroll
      for (int i = 0; i < 18; ++i) {
        if (i + 2 < 16) xx[i + 2] = MFMA16K16_H(gf, ev[i + 2], z4);
        if (i < 16) {
          const f32x4 xs = xx[i];
          const float r0 = bf_lo(wv[i][0]), i0 = bf_hi(wv[i][0]), r1 = bf_lo(wv[i][1]), i1 = bf_hi(wv[i][1]);
          const u32x2 y = {pk_bf(xs[0] * r0 - xs[1] * i0, xs[0] * i0 + xs[1] * r0), pk_bf(xs[2] * r1 - xs[3] * i1, xs[2] * i1 + xs[3] * r1)};
          vv[i] = MFMA16K16_BF(y, gi, z4);
        }
        if (i >= 2) {
          const int k = i - 2;
          *(u32x2*)(eb + (wro ^ (((k >> 1) & 3) * 64 + (k & 1) * 8)) + (k >> 3) * 256) = (u32x2){pk_h(vv[k][0], vv[k][1]), pk_h(vv[k][2], vv[k][3])};
        }
        SCHED();
      }
    }
    BARRIER();
    TS_MARK();
    {
      f32x16 dr[2], di[2];
      u32x4 yr[4], yi[4];
#pragma unroll
      for (int i = 0; i < 4; ++i) { yr[i] = *(const u32x4*)(erow + i * 1024); yi[i] = *(const u32x4*)(erow + 4096 + i * 1024); }
      SCHED();
#pragma unroll
      for (int t2 = 0; t2 < 2; ++t2) {
        dr[t2] = zero; di[t2] = zero;
#pragma unroll
        for (int s2 = 0; s2 < 2; ++s2) {
          const u32x4 pr_ = yr[t2 * 2 + s2], pi_ = swap_halves(yi[t2 * 2 + s2]);
          const u32x4 C = G1[(t2 * 2 + s2) * 2], S = G1[(t2 * 2 + s2) * 2 + 1];
          dr[t2] = MFMA32_H(pr_, C, dr[t2]);
          di[t2] = MFMA32_H(pi_, C, di[t2]);
          dr[t2] = MFMA32_H(neg_h(pi_), S, dr[t2]);
          di[t2] = MFMA32_H(pr_, S, di[t2]);
        }
      }
      u32x4 qr[2][2], qi[2][2];
      bfly_tw<true>(dr, di, tc, tsn, qr, qi);
      const unsigned vo = WS_VT + (w * 41 + c1) * VP + l * 16;
#pragma unroll
      for (int t = 0; t < 2; ++t) {
        f32x16 zr = zero, zq = zero;
#pragma unroll
        for (int ks = 0; ks < 2; ++ks) {
          const u32x4 C = G3[(t * 2 + ks) * 2], S = G3[(t * 2 + ks) * 2 + 1];
          zr = MFMA32_H(C, qr[t][ks], zr);
          zq = MFMA32_H(C, qi[t][ks], zq);
          zr = MFMA32_H(S, neg_h(qi[t][ks]), zr);
          zq = MFMA32_H(S, qr[t][ks], zq);
        }
#pragma unroll
        for (int u = 0; u < 4; ++u) {
          u32x4 o;
#pragma unroll
          for (int j = 0; j < 4; ++j) o[j] = pk_bf(zr[4 * u + j] * osc, zq[4 * u + j] * osc);
          ST128(o, rw, vo + (t * 4 + u) * 1024);
        }
      }
    }
    WAIT_V(0);
    __builtin_amdgcn_s_barrier();
    if (tid == 0) st_dev(ws + WS_FV + c1 * 8, tag);
    TS_END(0, 1);
    return;
  }
  if (aux == 63 && w == 0) {
    wait_flags(ws + WS_F1 + l * 8, tag);
#pragma unroll 1
    for (int i = 0; i < 3; ++i) wait_flags(ws + WS_FA + i * 1024 + l * 16, tag);
    if (l == 0) st_dev(ws + WS_EP, tag);
  }
  TS_END(0, 2);
}

#if defined(__HIP_DEVICE_COMPILE__)
#pragma clang attribute pop
#endif

extern "C" void kernel_launch(void* const* d_in, const int* in_sizes, int n_in, void* d_out, int out_size, void* d_ws, size_t ws_size,
                              hipStream_t stream) {
  const u16* x = (const u16*)d_in[0];
  const u16* w_r = (const u16*)d_in[1];
  const u16* w_i = (const u16*)d_in[2];
  const u16* wg = (const u16*)d_in[4];
  const u16* wu = (const u16*)d_in[5];
  const u16* wd = (const u16*)d_in[6];
  char* ws = (char*)d_ws;
  ka<<<256, 512, 0, stream>>>(x, w_r, w_i, wg, wu, wd, ws, (u16*)d_out);
}
```

```cpp
#include <hip/hip_runtime.h>
#include <hip/hip_bf16.h>
#include <cstdio>
#include <vector>
#include <algorithm>
#define TS 1

typedef unsigned short u16;
typedef __attribute__((ext_vector_type(8))) __bf16 bf16x8;
typedef __attribute__((ext_vector_type(2))) __bf16 bf16x2;
typedef __attribute__((ext_vector_type(8))) _Float16 f16x8;
typedef __attribute__((ext_vector_type(4))) _Float16 f16x4;
typedef __attribute__((ext_vector_type(2))) _Float16 f16x2;
typedef __attribute__((ext_vector_type(4))) short s16x4;
typedef __attribute__((ext_vector_type(2))) float f32x2;
typedef __attribute__((ext_vector_type(4))) float f32x4;
typedef __attribute__((ext_vector_type(16))) float f32x16;
typedef __attribute__((ext_vector_type(4))) unsigned u32x4;
typedef __attribute__((ext_vector_type(2))) unsigned u32x2;
typedef __attribute__((ext_vector_type(2))) u16 u16x2;
typedef __amdgpu_buffer_rsrc_t rsrc_t;
#define DI __device__ __forceinline__
#define LDS_AS __attribute__((address_space(3)))

namespace tb {
constexpr double kPi = 3.14159265358979323846264338327950288;
constexpr double sinT(double x) { double x2 = x * x, t = x, s = x; for (int i = 1; i < 12; ++i) { t *= -x2 / ((2 * i) * (2 * i + 1)); s += t; } return s; }
constexpr double cosT(double x) { double x2 = x * x, t = 1, s = 1; for (int i = 1; i < 12; ++i) { t *= -x2 / ((2 * i - 1) * (2 * i)); s += t; } return s; }
constexpr double cos2pi(int q, int den) {
  q %= den; if (2 * q > den) q = den - q;
  double sg = 1; if (4 * q > den) { q = den / 2 - q; sg = -1; }
  if (8 * q > den) return sg * sinT(2 * kPi * (den / 4 - q) / den);
  return sg * cosT(2 * kPi * q / den);
}
constexpr u16 bfbits(double d) { unsigned u = __builtin_bit_cast(unsigned, (float)d); u += 0x7fffu + ((u >> 16) & 1u); return (u16)(u >> 16); }
constexpr u16 hbits(double d) { return __builtin_bit_cast(u16, (_Float16)d); }
constexpr int krow(int s, int h, int j) { return 16 * s + 8 * (j >> 2) + 4 * h + (j & 3); }
template <int N> struct CT { double c[N]; };
template <int N> constexpr CT<N> mkC() { CT<N> c{}; for (int i = 0; i < N; ++i) c.c[i] = cos2pi(i, N); return c; }
constexpr CT<64> c64 = mkC<64>();
constexpr CT<80> c80 = mkC<80>();
constexpr double m80(int o, int d) { return o == 1 ? c80.c[(40 * d) % 80] : c80.c[((o >> 1) * d + ((o & 1) ? 20 : 0)) % 80]; }
struct alignas(16) TabF {
  u16 g1[2][2][2][64][8];
  u16 g3[2][2][2][64][8];
  u16 g8[64][2][4];
};
constexpr TabF mkTabF() {
  TabF T{};
  for (int t = 0; t < 2; ++t) for (int ks = 0; ks < 2; ++ks) for (int l = 0; l < 64; ++l) for (int j = 0; j < 8; ++j) {
    const int r = l & 31, k = krow(ks, l >> 5, j);
    const int q1 = (r * (2 * k + t)) & 63, q3 = (k * (2 * r + t)) & 63;
    T.g1[t][ks][0][l][j] = hbits(c64.c[q1] * 0.25); T.g1[t][ks][1][l][j] = hbits(c64.c[(q1 + 48) & 63] * 0.25);
    T.g3[t][ks][0][l][j] = hbits(c64.c[q3] * 0.25); T.g3[t][ks][1][l][j] = hbits(c64.c[(q3 + 48) & 63] * 0.25);
  }
  for (int l = 0; l < 64; ++l) for (int e = 0; e < 4; ++e) {
    const int m = l & 15, k = 4 * (l >> 4) + e, q = (8 * (m >> 1) * (k >> 1)) & 63;
    const double C = c64.c[q], S = c64.c[(q + 48) & 63];
    T.g8[l][0][e] = hbits(0.25 * ((m & 1) == (k & 1) ? C : ((m & 1) ? -S : S)));
    T.g8[l][1][e] = bfbits(239. / 256. * ((m & 1) == (k & 1) ? C : ((k & 1) ? -S : S)));
  }
  return T;
}
struct alignas(16) Tab80 {
  u16 f[3][5][64][8];
  u16 pad[512];
  u16 i[6][5][64][4];
};
constexpr Tab80 mkTab80() {
  Tab80 T{};
  for (int n = 0; n < 3; ++n) for (int ks = 0; ks < 5; ++ks) for (int l = 0; l < 64; ++l) for (int j = 0; j < 8; ++j)
    T.f[n][ks][l][j] = 32 * n + (l & 31) < 80 ? hbits(m80(32 * n + (l & 31), 16 * ks + 8 * (l >> 5) + j)) : (u16)0;
  for (int n = 0; n < 6; ++n) for (int ks = 0; ks < 5; ++ks) for (int l = 0; l < 64; ++l) for (int e = 0; e < 4; ++e)
    T.i[n][ks][l][e] = bfbits(m80(16 * ks + 4 * (l >> 4) + e, 13 * n + (n < 2 ? n : 2) + (l & 15)));
  return T;
}
}
static __device__ const tb::TabF g_TF = tb::mkTabF();
static __device__ const tb::Tab80 g_T80 = tb::mkTab80();

constexpr unsigned WS_EP = 0;
constexpr unsigned WS_F1 = 256;
constexpr unsigned WS_F2 = WS_F1 + 512;
constexpr unsigned WS_FV = WS_F2 + 512;
constexpr unsigned WS_FH = WS_FV + 512;
constexpr unsigned WS_FA = 4096;
constexpr unsigned WS_ST = 8192;
constexpr unsigned CP = 65536 + 256;
constexpr unsigned WS_UB = WS_ST + 2048 * 8;
constexpr unsigned WS_WT = WS_UB + 40 * CP;
constexpr unsigned WS_AF = WS_WT + 41 * CP;
constexpr unsigned WS_BF = WS_AF + 32 * 48 * 4096;
constexpr unsigned VP = 8192 + 256;
constexpr unsigned WS_VT = WS_BF + 6 * 48 * 7168;
constexpr unsigned WS_END = WS_VT + 328 * VP;

#if defined(__HIP_DEVICE_COMPILE__)
#pragma clang attribute push(__attribute__((target("no-packed-fp32-ops"))), apply_to = function)
#endif
DI float u2f(unsigned u) { return __builtin_bit_cast(float, u); }
DI unsigned f2u(float f) { return __builtin_bit_cast(unsigned, f); }
DI bool all_lanes(bool p) { return __builtin_amdgcn_ballot_w64(!p) == 0; }
DI int s_of_rho(int rho) {
  const int t = rho >> 10, ks = (rho >> 9) & 1, h = (rho >> 8) & 1, r = (rho >> 3) & 31, jh = (rho >> 2) & 1, jl = rho & 3;
  return 64 * (16 * ks + 8 * jh + 4 * h + jl) + 32 * t + r;
}
DI float bf_lo(unsigned u) { return u2f(u << 16); }
DI float bf_hi(unsigned u) { return u2f(u & 0xffff0000u); }
DI unsigned pk_bf(float a, float b) { f32x2 f = {a, b}; return __builtin_bit_cast(unsigned, __builtin_convertvector(f, bf16x2)); }
DI unsigned pk_h(float a, float b) { f32x2 f = {a, b}; return __builtin_bit_cast(unsigned, __builtin_convertvector(f, f16x2)); }
DI float rnd_bf(float a) { return bf_lo(pk_bf(a, 0.f)); }
#define MFMA16_BF(a, b, c) __builtin_amdgcn_mfma_f32_16x16x32_bf16(__builtin_bit_cast(bf16x8, a), __builtin_bit_cast(bf16x8, b), c, 0, 0, 0)
#define MFMA32_BF(a, b, c) __builtin_amdgcn_mfma_f32_32x32x16_bf16(__builtin_bit_cast(bf16x8, a), __builtin_bit_cast(bf16x8, b), c, 0, 0, 0)
#define MFMA32_H(a, b, c) __builtin_amdgcn_mfma_f32_32x32x16_f16(__builtin_bit_cast(f16x8, a), __builtin_bit_cast(f16x8, b), c, 0, 0, 0)
#define MFMA16K16_H(a, b, c) __builtin_amdgcn_mfma_f32_16x16x16f16(__builtin_bit_cast(f16x4, a), __builtin_bit_cast(f16x4, b), c, 0, 0, 0)
#define MFMA16K16_BF(a, b, c) __builtin_amdgcn_mfma_f32_16x16x16bf16_1k(__builtin_bit_cast(s16x4, a), __builtin_bit_cast(s16x4, b), c, 0, 0, 0)
DI u32x2 tr_read(const char* p) {
  s16x4 v = __builtin_amdgcn_ds_read_tr16_b64_v4i16((LDS_AS s16x4*)(p));
  return __builtin_bit_cast(u32x2, v);
}
DI rsrc_t mk_rsrc(const void* p, unsigned bytes) { return __builtin_amdgcn_make_buffer_rsrc((void*)p, 0, bytes, 0x00020000); }
#define ST128(v, r, off) __builtin_amdgcn_raw_buffer_store_b128(v, r, off, 0, 16)
#define ST64(v, r, off) __builtin_amdgcn_raw_buffer_store_b64(v, r, off, 0, 16)
#define WAIT_V(n) asm volatile("s_waitcnt vmcnt(%0)" ::"n"(n) : "memory")
#define WAIT_L(n) asm volatile("s_waitcnt lgkmcnt(%0)" ::"n"(n) : "memory")
#define SCHED() __builtin_amdgcn_sched_barrier(0)
#define BARRIER() do { WAIT_L(0); __builtin_amdgcn_s_barrier(); asm volatile("" ::: "memory"); } while (0)
DI void dma1(unsigned voff, const void* sbase, unsigned lds) {
  asm volatile("s_mov_b32 m0, %2\n\ts_nop 3\n\tglobal_load_lds_dwordx4 %0, %1" ::"v"(voff), "s"(sbase), "s"(lds) : "memory");
}
DI void dma1nt(unsigned voff, const void* sbase, unsigned lds) {
  asm volatile("s_mov_b32 m0, %2\n\ts_nop 3\n\tglobal_load_lds_dwordx4 %0, %1 nt" ::"v"(voff), "s"(sbase), "s"(lds) : "memory");
}
template <int OFF> DI void dma1q(unsigned voff, const void* sbase, unsigned lds) {
  asm volatile("s_add_u32 m0, %2, %3\n\ts_nop 0\n\tglobal_load_lds_dwordx4 %0, %1" ::"v"(voff), "s"(sbase), "s"(lds), "n"(OFF) : "memory", "scc");
}
template <int CTRL> DI unsigned dpp_u(unsigned x) { return (unsigned)__builtin_amdgcn_mov_dpp((int)x, CTRL, 0xf, 0xf, true); }
template <int CTRL> DI float dpp_f(float x) { return u2f(dpp_u<CTRL>(f2u(x))); }
DI float sum16(float x) {
  x += dpp_f<0xB1>(x); x += dpp_f<0x4E>(x); x += dpp_f<0x141>(x); x += dpp_f<0x128>(x);
  return x;
}
DI float sum_rows(float x) {
  const auto s = __builtin_amdgcn_permlane16_swap(f2u(x), f2u(x), false, false);
  x = u2f(s[0]) + u2f(s[1]);
  const auto t = __builtin_amdgcn_permlane32_swap(f2u(x), f2u(x), false, false);
  return u2f(t[0]) + u2f(t[1]);
}
typedef unsigned long long u64;
typedef __attribute__((address_space(1))) u64 gu64;
#define RLX_AGENT __ATOMIC_RELAXED, __HIP_MEMORY_SCOPE_AGENT
DI u64 ld_dev(const char* p) { return __hip_atomic_load((gu64*)p, RLX_AGENT); }
DI void st_dev(char* p, u64 v) { __hip_atomic_store((gu64*)p, v, RLX_AGENT); }
DI u64 next_epoch(u64 e) { return e * 0x9E3779B97F4A7C15ull + 0x632BE59BD9B4E019ull; }
constexpr unsigned SPIN_CAP = 1u << 18;
DI u64 wait_flags(const char* p, u64 tag) {
  u64 v = 0;
#pragma unroll 1
  for (unsigned n = 0; n < SPIN_CAP; ++n) {
    v = ld_dev(p);
    if (all_lanes((v >> 16) == (tag >> 16))) break;
    __builtin_amdgcn_s_sleep(1);
  }
  asm volatile("" ::: "memory");
  return v;
}
#define LD128_DEV(r, off) __builtin_amdgcn_raw_buffer_load_b128(r, off, 0, 16)
#define LD128_DEV2(r, off, soff) __builtin_amdgcn_raw_buffer_load_b128(r, off, soff, 16)
#define LD64_DEV(r, off) __builtin_amdgcn_raw_buffer_load_b64(r, off, 0, 16)
DI unsigned max64(unsigned x) {
  x = max(x, dpp_u<0xB1>(x)); x = max(x, dpp_u<0x4E>(x)); x = max(x, dpp_u<0x141>(x)); x = max(x, dpp_u<0x128>(x));
  const auto s = __builtin_amdgcn_permlane16_swap(x, x, false, false);
  x = max(s[0], s[1]);
  const auto t = __builtin_amdgcn_permlane32_swap(x, x, false, false);
  return max(t[0], t[1]);
}
DI unsigned lds_addr(const char* p) { return (unsigned)(size_t)(LDS_AS const char*)p; }

#define TS_NB1 256
#define TS_NB2 0
#define TS_NB3 0
#define TS_DECL unsigned ts_[15] = {}, tc_[15] = {}; int tsn_ = 0;
#define TS_MARK() do { SCHED(); ts_[tsn_] = (unsigned)__builtin_amdgcn_s_memrealtime(); tc_[tsn_++] = (unsigned)__builtin_amdgcn_s_memtime(); __builtin_amdgcn_s_waitcnt(0xC07F); SCHED(); } while (0)
#define TS_END(k, role) do { WAIT_V(0); TS_MARK(); if (threadIdx.x == TS_T) { unsigned long long* p_ = (unsigned long long*)((char*)ws + (64u << 20)) + ((k) * 1024 + blockIdx.x) * 16; \
    for (int i_ = 0; i_ < 15; ++i_) p_[i_] = ts_[i_] | ((unsigned long long)(tc_[i_] - tc_[0]) << 32); p_[15] = (0x5354ull << 32) | (tsn_ << 8) | (role); } } while (0)
#define TS_SET(n) tsn_ = n
#define TS_T 0

#define MAPV 0
#define PFPOL "nt"
#define WPF 0
#define DMAW dma1
constexpr int CODE_LINES = 290;
constexpr int STAGE_B = 49152;
constexpr int G_CS = 3 * STAGE_B;
constexpr int G_ST = G_CS + 512;
constexpr int G_PF = G_ST + 2048;
constexpr int G_TAG = G_PF + 1024;
constexpr int G_SN = G_TAG + 1024;
constexpr int A_TF = 0;
constexpr int A_T80 = 16384;
constexpr int A_XH = 32768;
constexpr int A_UT = A_XH + 32 * 1344;
constexpr int A_W = A_UT + 4 * 10304;
constexpr int A_MX = A_W + 8 * 5152;
constexpr int A_E = 16384;
constexpr int KA_LDS = A_MX + 64;
static_assert(G_SN + 6144 <= KA_LDS && KA_LDS <= 163840, "lds");

DI void pack_h(const f32x16& v, u32x4 (&o)[2]) {
#pragma unroll
  for (int s = 0; s < 2; ++s)
#pragma unroll
    for (int j = 0; j < 4; ++j) o[s][j] = pk_h(v[8 * s + 2 * j], v[8 * s + 2 * j + 1]);
}
DI f16x2 as_h2(unsigned u) { return __builtin_bit_cast(f16x2, u); }
DI unsigned as_u(f16x2 v) { return __builtin_bit_cast(unsigned, v); }
template <bool INV> DI void bfly_tw(const f32x16 (&dr)[2], const f32x16 (&di)[2], const f16x2 (&tc)[8], const f16x2 (&ts)[8],
                                    u32x4 (&er)[2][2], u32x4 (&ei)[2][2]) {
#pragma unroll
  for (int e = 0; e < 8; ++e) {
    const f16x2 r0 = as_h2(pk_h(dr[0][2 * e], dr[0][2 * e + 1])), r1 = as_h2(pk_h(dr[1][2 * e], dr[1][2 * e + 1]));
    const f16x2 i0 = as_h2(pk_h(di[0][2 * e], di[0][2 * e + 1])), i1 = as_h2(pk_h(di[1][2 * e], di[1][2 * e + 1]));
#pragma unroll
    for (int t = 0; t < 2; ++t) {
      const f16x2 sr = t ? r0 - r1 : r0 + r1, si = t ? i0 - i1 : i0 + i1;
      const f16x2 a = sr * tc[e], c = si * tc[e];
      er[t][e >> 2][e & 3] = as_u(__builtin_elementwise_fma(INV ? -si : si, ts[e], a));
      ei[t][e >> 2][e & 3] = as_u(__builtin_elementwise_fma(INV ? sr : -sr, ts[e], c));
    }
  }
}
DI u32x4 neg_h(u32x4 v) { return v ^ 0x80008000u; }
DI u32x4 swap_halves(u32x4 v) { return (u32x4){v[2], v[3], v[0], v[1]}; }
DI int e_key(int d2) { return ((d2 & 2) << 2) | ((d2 & 1) << 2) | ((d2 & 4) >> 1) | ((d2 & 2) >> 1); }

__global__ __launch_bounds__(512, 2) void ka(const u16* __restrict__ x, const u16* __restrict__ w_r, const u16* __restrict__ w_i,
                                             const u16* __restrict__ wg, const u16* __restrict__ wu, const u16* __restrict__ wd,
                                             char* __restrict__ ws, u16* __restrict__ out) {
  __shared__ __attribute__((aligned(1024))) char sm[KA_LDS];
  u64 pc0;
  asm volatile("s_getpc_b64 %0" : "=s"(pc0));
  pc0 &= ~255ull;
  const int b = blockIdx.x, tid = threadIdx.x, l = tid & 63;
  const int w = __builtin_amdgcn_readfirstlane(tid >> 6);
  const int xcd = b & 7, idx = b >> 3;
  const unsigned smb = lds_addr(sm);
  const rsrc_t rw = mk_rsrc(ws, WS_END);
  const int g = l >> 4;
  TS_DECL TS_MARK();
  if (idx >= 8) {
    const int gi = idx - 8;
    const int panel = (xcd >> 2) * 4 + (gi & 3), ft = (xcd & 3) * 6 + (gi >> 2);
    const int gwg = gi * 8 + xcd;
    const int p3 = xcd * 4 + (gi & 3), ct = gi >> 2, d1s = 13 * ct + (ct < 2 ? ct : 2), nd1 = ct < 2 ? 14 : 13;
    const int i16 = l & 15, kh = w >> 2, c0 = 2 * (w & 3) + kh;
    constexpr int NB = 18;
    u32x4 B[NB][2];
    const unsigned bo = WS_BF + (ct * 48 + kh) * 7168 + g * 224 + min(i16, 13) * 16;
#define LOADB(j) do { B[(j) % NB][0] = LD128_DEV2(rw, bo + c0 * 896, (j) * 14336); B[(j) % NB][1] = LD128_DEV2(rw, bo + (c0 ^ 1) * 896, (j) * 14336); } while (0)
    if (w >= 4) {
      const int lw = w - 4, r8 = l >> 3, c8 = l & 7;
      asm volatile("s_mov_b32 m0, %2\n\ts_nop 3\n\tglobal_load_lds_dword %0, %1 sc1" ::"v"(WS_EP + (l & 1) * 4), "s"(ws), "s"(smb + G_TAG + lw * 256) : "memory");
      unsigned xo[2], woff[2];
#pragma unroll
      for (int v = 0; v < 2; ++v) {
        xo[v] = (s_of_rho(panel * 256 + 2 * lw + v) + r8) * 1280 + ((c8 ^ ((r8 >> 1) | ((lw >> 1) << 2))) << 4);
        woff[v] = ((16 * lw + 8 * v + r8) * 1536 + ft * 64) * 2 + ((c8 ^ ((((r8 >> 1) & 1) | (v << 1)) << 1)) << 4);
      }
      const char *xs = (const char*)x, *gs = (const char*)wg, *us = (const char*)wu;
      const unsigned dx = smb + lw * 8192, dw = smb + 32768 + lw * 2048;
#define STAGE(off)                                                                                   \
  do {                                                                                               \
    _Pragma("unroll") for (int v_ = 0; v_ < 2; ++v_) {                                               \
      dma1(woff[v_], gs, dw + (off) + v_ * 1024);                                                    \
      dma1(woff[v_], us, dw + (off) + 8192 + v_ * 1024);                                             \
    }                                                                                                \
    _Pragma("unroll") for (int i_ = 0; i_ < 8; ++i_) dma1(xo[i_ >> 2], xs + (i_ & 3) * 10240, dx + (off) + i_ * 1024); \
    xs += 128; gs += 64 * 3072; us += 64 * 3072;                                                     \
  } while (0)
      STAGE(0);
      STAGE(STAGE_B);
      WAIT_V(12);
      __builtin_amdgcn_s_barrier();
      const u64 tag = next_epoch(*(const u64*)(sm + G_TAG + lw * 256));
      const unsigned fao = WS_FA + ((ct * 48 + (l < 48 ? l : 0)) % 192) * 16;
      const int job = gwg + 192 * lw;
      if (job < 288) {
        const int jc = job / 48, F = job - jc * 48, lg = min(l / 14, 3), d1 = min(13 * jc + (jc < 2 ? jc : 2) + min(l - lg * 14, 13), 79);
        const unsigned d0 = (32 * F + 4 * lg) * 1280 + d1 * 16, d16 = d0 + 16 * 1280;
        u32x4 dv[8];
        asm volatile("global_load_dwordx4 %0, %8, %10\n\tglobal_load_dwordx4 %1, %8, %10 offset:1280\n\tglobal_load_dwordx4 %2, %8, %10 offset:2560\n\t"
                     "global_load_dwordx4 %3, %8, %10 offset:3840\n\tglobal_load_dwordx4 %4, %9, %10\n\tglobal_load_dwordx4 %5, %9, %10 offset:1280\n\t"
                     "global_load_dwordx4 %6, %9, %10 offset:2560\n\tglobal_load_dwordx4 %7, %9, %10 offset:3840"
                     : "=&v"(dv[0]), "=&v"(dv[1]), "=&v"(dv[2]), "=&v"(dv[3]), "=&v"(dv[4]), "=&v"(dv[5]), "=&v"(dv[6]), "=&v"(dv[7])
                     : "v"(d0), "v"(d16), "s"(wd) : "memory");
        STAGE(2 * STAGE_B);
        WAIT_V(20);
        __builtin_amdgcn_s_barrier();
        STAGE(0);
        asm volatile("s_waitcnt vmcnt(12)" : "+v"(dv[0]), "+v"(dv[1]), "+v"(dv[2]), "+v"(dv[3]), "+v"(dv[4]), "+v"(dv[5]), "+v"(dv[6]), "+v"(dv[7])::"memory");
        if (l < 56) {
#pragma unroll
          for (int c = 0; c < 8; ++c) {
            u32x4 o;
#pragma unroll
            for (int j = 0; j < 4; ++j) o[j] = __builtin_amdgcn_perm(dv[2 * j + 1][c >> 1], dv[2 * j][c >> 1], (c & 1) ? 0x07060302u : 0x05040100u);
            ST128(o, rw, WS_BF + job * 7168 + c * 896 + l * 16);
          }
        }
      } else {
        STAGE(2 * STAGE_B);
        WAIT_V(12);
        __builtin_amdgcn_s_barrier();
        STAGE(0);
        WAIT_V(12);
      }
      __builtin_amdgcn_s_barrier();
      STAGE(STAGE_B);
      if (job < 288) WAIT_V(20); else WAIT_V(12);
      __builtin_amdgcn_s_barrier();
      STAGE(2 * STAGE_B);
      WAIT_V(12);
      __builtin_amdgcn_s_barrier();
      if (tid == 256) st_dev(ws + WS_FA + gwg * 16, tag);
      STAGE(0);
      WAIT_V(12);
      __builtin_amdgcn_s_barrier();
      STAGE(STAGE_B);
      WAIT_V(12);
      __builtin_amdgcn_s_barrier();
      asm volatile("s_mov_b32 m0, %2\n\ts_nop 3\n\tglobal_load_lds_dwordx4 %0, %1 sc1" ::"v"(fao), "s"(ws), "s"(smb + G_SN + lw * 1536) : "memory");
      if (l < 32)
        asm volatile("s_mov_b32 m0, %2\n\ts_nop 3\n\tglobal_load_lds_dwordx4 %0, %1 sc1" ::"v"(WS_ST + (panel * 256 + lw * 64) * 8 + l * 16), "s"(ws),
                     "s"(smb + G_SN + lw * 1536 + 1024) : "memory");
      STAGE(2 * STAGE_B);
      WAIT_V(12);
      __builtin_amdgcn_s_barrier();
      STAGE(0);
      {
        u64 st = *(const u64*)(sm + G_SN + lw * 1536 + 1024 + l * 8);
        const u64 fa = *(const u64*)(sm + G_SN + lw * 1536 + l * 16);
        if (!all_lanes(fa == tag && (unsigned)(st >> 32) == (unsigned)tag)) {
          wait_flags(ws + fao, tag);
#pragma unroll 1
          for (unsigned n = 0; n < SPIN_CAP; ++n) {
            st = ld_dev(ws + WS_ST + (panel * 256 + tid - 256) * 8);
            if (all_lanes((unsigned)(st >> 32) == (unsigned)tag)) break;
            __builtin_amdgcn_s_sleep(4);
          }
        }
        asm volatile("" ::: "memory");
        const float rs = __builtin_amdgcn_rcpf(bf_hi((unsigned)st));
        *(f32x2*)(sm + G_ST + (tid - 256) * 8) = (f32x2){bf_lo((unsigned)st), rs * rs};
      }
      LOADB(0); LOADB(1); LOADB(2);
      SCHED();
      WAIT_V(18);
      WAIT_L(0);
#undef STAGE
      __builtin_amdgcn_s_barrier();
      const char* bsrc = ws + WS_BF + ct * 48 * 7168 + 2 * lw * 896;
      const unsigned bl = g * 224 + min(i16, 13) * 16;
#define STAGEB(j0)                                                                                                    \
  do {                                                                                                                \
    _Pragma("unroll") for (int i_ = (j0); i_ < (j0) + 6; ++i_)                                                        \
      asm volatile("s_mov_b32 m0, %3\n\ts_nop 0\n\tglobal_load_lds_dwordx4 %0, %2 sc1\n\tglobal_load_lds_dwordx4 %1, %2 offset:1024 sc1" \
                   ::"v"(bl + 128), "v"(bl), "s"(bsrc - 128 + i_ * 14336), "s"(smb + i_ * 8192 + lw * 2048) : "memory"); \
  } while (0)
      STAGEB(6);
      LOADB(3); LOADB(4);
      SCHED();
      WAIT_V(22);
      __builtin_amdgcn_s_barrier();
      STAGEB(12);
      LOADB(5); LOADB(6);
      SCHED();
      __builtin_amdgcn_s_barrier();
      STAGEB(0);
#undef STAGEB
#pragma unroll
      for (int j = 7; j < 12; ++j) LOADB(j);
      SCHED();
      WAIT_V(10);
      __builtin_amdgcn_s_barrier();
#pragma unroll
      for (int j = 12; j < 18; ++j) LOADB(j);
    } else {
    if (w == 3) {
      asm volatile("s_mov_b32 m0, %2\n\ts_nop 3\n\tglobal_load_lds_dword %0, %1" ::"v"(min((gi % 5) * 64 + l, CODE_LINES - 1) * 128), "s"(pc0),
                   "s"(smb + G_CS) : "memory");
    }
    const int wr = w >> 1, fh = w & 1;
    {
    const int q = (l & 15) >> 2;
    const int xb0 = (i16 >> 3) * 16384 + (wr * 16 + (l & 7)) * 128 + ((g ^ (((l & 7) >> 1) | ((i16 >> 3) << 2))) << 4);
    const int wb0 = (32768 + (8 * g + q) * 128 + (((fh * 2) ^ (((q >> 1) & 1) | ((g & 1) << 1))) << 5) + (l & 3) * 8) ^ (wr << 13);
    const char* xr[2];
    const char* wp[4];
#define SET_BUF(off)                                                                                       \
  do {                                                                                                     \
    xr[0] = sm + (off) + xb0; xr[1] = sm + (off) + (xb0 ^ 64);                                             \
    _Pragma("unroll") for (int i_ = 0; i_ < 4; ++i_) wp[i_] = sm + (off) + (wb0 ^ ((i_ & 1) << 5) ^ ((i_ >> 1) << 13)); \
  } while (0)
    f32x4 acc[4][8];
#pragma unroll
    for (int i = 0; i < 4; ++i)
#pragma unroll
      for (int r = 0; r < 8; ++r) acc[i][r] = (f32x4){0.f, 0.f, 0.f, 0.f};
    f32x4 cs[2] = {{0.f, 0.f, 0.f, 0.f}, {0.f, 0.f, 0.f, 0.f}};
    u32x4 ones = {0x3f803f80u, 0x3f803f80u, 0x3f803f80u, 0x3f803f80u};
    asm volatile("" : "+v"(ones));
    u32x4 xf[2][8];
    u32x2 wf[2][8];
#define RD(ks, n)                                                                                          \
  do {                                                                                                     \
    if ((n) >= 2 && (n) < 10) xf[ks][(n) - 2] = *(const u32x4*)(xr[ks] + (((n) - 2) & 3) * 4096 + (((n) - 2) >> 2) * 1024); \
    else { const int m_ = (n) < 2 ? (n) : (n) - 8; wf[ks][m_] = tr_read(wp[m_ >> 1] + (ks) * 4096 + (m_ & 1) * 512); } \
  } while (0)
#define WF(ks, i) ((u32x4){wf[ks][2 * (i)][0], wf[ks][2 * (i)][1], wf[ks][2 * (i) + 1][0], wf[ks][2 * (i) + 1][1]})
#define KSTEP(ks, READS, CSUM)                                                                             \
  do {                                                                                                     \
    _Pragma("unroll") for (int n_ = 0; n_ < 32; ++n_) {                                                    \
      acc[n_ >> 3][n_ & 7] = MFMA16_BF(WF(ks, n_ >> 3), xf[ks][n_ & 7], acc[n_ >> 3][n_ & 7]);             \
      if (READS && n_ < 16) RD((ks) ^ 1, n_);                                                              \
      if (CSUM && n_ >= 30) cs[n_ & 1] = MFMA16_BF(WF(ks, n_ & 1), ones, cs[n_ & 1]);                      \
      SCHED();                                                                                             \
    }                                                                                                      \
  } while (0)
    SET_BUF(0);
    TS_MARK();
    BARRIER();
    TS_MARK();
#pragma unroll
    for (int n = 0; n < 16; ++n) RD(0, n);
    SCHED();
    KSTEP(0, true, 1);
    int nxt = STAGE_B;
#pragma unroll 1
    for (int u = 0; u < 9; ++u) {
      BARRIER();
      SET_BUF(nxt);
      SCHED();
      KSTEP(1, true, 1);
      KSTEP(0, true, 1);
      nxt = nxt == 2 * STAGE_B ? 0 : nxt + STAGE_B;
    }
    TS_MARK();
#pragma unroll
    for (int i = 0; i < 2; ++i) {
      cs[i] = MFMA16_BF(WF(1, i), ones, cs[i]);
      if ((l & 15) == 0) *(f32x4*)(sm + G_CS + w * 128 + i * 64 + g * 16) = cs[i];
    }
    BARRIER();
    f32x4 co[2];
    f32x4 st[4];
#pragma unroll
    for (int i = 0; i < 2; ++i) co[i] = *(const f32x4*)(sm + G_CS + (w ^ 2) * 128 + i * 64 + g * 16);
#pragma unroll
    for (int r = 0; r < 4; ++r) st[r] = *(const f32x4*)(sm + G_ST + (wr * 128 + (r >> 1) * 64 + 8 * (l & 7) + 4 * (i16 >> 3) + (r & 1) * 2) * 8);
    SCHED();
    TS_MARK();
    const unsigned ao = WS_AF + ((panel * 4 + wr * 2) * 48 + ft * 2 + fh) * 4096 + l * 16;
#pragma unroll
    for (int r = 0; r < 10; ++r) {
      if (r < 8) {
#pragma unroll
        for (int i = 0; i < 4; ++i) acc[i][r] = MFMA16_BF(WF(1, i), xf[1][r], acc[i][r]);
      }
      if (r >= 2) {
        const int e = r - 2;
        const float mb = st[e >> 1][(e & 1) * 2], r2 = st[e >> 1][(e & 1) * 2 + 1];
        u32x4 o;
#pragma unroll
        for (int i = 0; i < 2; ++i) {
          const f32x4 hv = ((acc[i][e] - mb * cs[i]) * r2) * (acc[2 + i][e] - mb * co[i]);
          o[2 * i] = pk_bf(hv[0], hv[1]); o[2 * i + 1] = pk_bf(hv[2], hv[3]);
        }
        ST128(o, rw, ao + (e >> 2) * 48 * 4096 + (e & 3) * 1024);
      }
      SCHED();
    }
#undef SET_BUF
#undef RD
#undef WF
#undef KSTEP
    }
    SCHED();
    TS_MARK();
    WAIT_V(0);
    __builtin_amdgcn_s_barrier();
    asm volatile("" ::: "memory");
    TS_MARK();
    const u64 tag = next_epoch(*(const u64*)(sm + G_TAG));
    if (tid == 0) st_dev(ws + WS_FH + (panel * 32 + ft) * 8, tag);
    SCHED();
#pragma unroll
    for (int j = 0; j < 18; ++j)
#pragma unroll
      for (int i = 0; i < 2; ++i) B[j][i] = *(const u32x4*)(sm + j * 8192 + (2 * w + i) * 1024 + l * 16);
    SCHED();
    if (w == 0) wait_flags(ws + WS_FH + (xcd * 32 + (l < 24 ? l : 0)) * 8, tag);
    }
#pragma unroll
    for (int j = 0; j < NB; ++j) asm volatile("" : "+v"(B[j][0]), "+v"(B[j][1]));
    SCHED();
    TS_SET(7);
    BARRIER();
    asm volatile("" ::: "memory");
    TS_MARK();
    const char* asrc = ws + WS_AF + p3 * 48 * 4096 + w * 4096;
#define LOADA(ch)                                                                                                     \
  asm volatile("s_mov_b32 m0, %2\n\ts_nop 3\n\tglobal_load_lds_dwordx4 %0, %1 sc1\n\tglobal_load_lds_dwordx4 %0, %1 offset:1024 sc1\n\t" \
               "global_load_lds_dwordx4 %0, %1 offset:2048 sc1\n\tglobal_load_lds_dwordx4 %0, %1 offset:3072 sc1" ::"v"(l * 16), \
               "s"(asrc + (ch) * 32768), "s"(smb + ((ch) & 3) * 32768 + w * 4096) : "memory")
    LOADA(0); LOADA(1);
    const u64 tag = next_epoch(*(const u64*)(sm + G_TAG));
    f32x4 acc[4][2];
#pragma unroll
    for (int m = 0; m < 4; ++m) { acc[m][0] = (f32x4){0.f, 0.f, 0.f, 0.f}; acc[m][1] = acc[m][0]; }
    const LDS_AS char* ap[2];
#pragma unroll
    for (int i = 0; i < 2; ++i) { ap[i] = (const LDS_AS char*)sm + i * 65536 + kh * 4096 + l * 16; asm volatile("" : "+v"(ap[i])); }
    u32x4 A[3][4], V[11], xres[2];
    u32x2 mi[5];
    unsigned orow[2];
    u64 fv = 0;
    const unsigned vo = WS_VT + (c0 * 41 + 2 * g) * VP + (p3 >> 3) * 2048 + ((l >> 3) & 1) * 1024 + ((p3 & 7) * 8 + (l & 7)) * 16;
#define LOADV()                                                                                                       \
  do {                                                                                                                \
    _Pragma("unroll") for (int u = 0; u < 10; ++u) V[u] = LD128_DEV2(rw, vo, ((u >> 1) * 8 + (u & 1)) * VP);         \
    V[10] = LD128_DEV2(rw, vo - 2 * g * VP, 40 * VP);                                                                 \
  } while (0)
    bool early = false;
    WAIT_V(4);
    BARRIER();
    TS_MARK();
#define ZPART()                                                                                                       \
  do {                                                                                                                \
    if (g == 0) {                                                                                                     \
      _Pragma("unroll") for (int m = 0; m < 4; ++m) V[0][m] = (V[0][m] & 0xffffu) | (V[10][m] << 16);     \
    }                                                                                                                 \
    _Pragma("unroll") for (int ks = 0; ks < 5; ++ks)                                                                  \
      _Pragma("unroll") for (int m = 0; m < 4; ++m)                                                                   \
        acc[m][0] = MFMA16K16_BF(((u32x2){V[2 * ks][m], V[2 * ks + 1][m]}), mi[ks], acc[m][0]);                       \
  } while (0)
#pragma unroll
    for (int m = 0; m < 8; ++m) A[m >> 2][m & 3] = *(const LDS_AS u32x4*)(ap[0] + (m >> 2) * 8192 + (m & 3) * 1024);
    SCHED();
#pragma unroll
    for (int j = 0; j < 24; ++j) {
      if ((j & 3) == 2 && j < 22) {
        if (j == 2) WAIT_V(8); else if (j == 6) WAIT_V(14); else if (j == 10) WAIT_V(21); else if (j == 14) WAIT_V(7);
        BARRIER();
      }
#pragma unroll
      for (int n = 0; n < 8; ++n) {
        acc[n & 3][n >> 2] = MFMA16_BF(A[j % 3][n & 3], B[j % NB][n >> 2], acc[n & 3][n >> 2]);
        if (n < 4 && j < 22) A[(j + 2) % 3][n] = *(const LDS_AS u32x4*)(ap[((j + 2) >> 3) & 1] + ((j + 2) & 7) * 8192 + n * 1024);
        if (n == 4) {
          if (j < 3) LOADA(2 + j);
          if (j == 6) LOADA(5);
          if (j >= 3 && j < 6) LOADB(j + 15);
          if (j >= 7 && j < 10) LOADB(j + 14);
          if (j == 9) fv = ld_dev(ws + WS_FV + (l < 41 ? l : 0) * 8);
          if (j == 12) {
#pragma unroll
            for (int ks = 0; ks < 5; ++ks) mi[ks] = *(const u32x2*)&g_T80.i[ct][ks][l][0];
            const rsrc_t rx = mk_rsrc(x, 2048 * 640 * 2);
#pragma unroll
            for (int r = 0; r < 2; ++r) {
              orow[r] = s_of_rho(p3 * 64 + 8 * ((4 * g + 2 * kh + r) & 7) + 4 * (g >> 1) + (w & 3)) * 1280 + min(d1s + i16, 79) * 16;
              xres[r] = __builtin_amdgcn_raw_buffer_load_b128(rx, orow[r], 0, 0);
            }
          }
        }
        SCHED();
      }
      if (j == 14) {
        early = all_lanes(fv == tag);
        asm volatile("" ::: "memory");
        LOADV();
      }
      if (j == 21) {
        if (!early) {
          wait_flags(ws + WS_FV + (l < 41 ? l : 0) * 8, tag);
          LOADV();
        }
#pragma unroll
        for (int u = 0; u < 11; ++u) asm volatile("" : "+v"(V[u]));
        ZPART();
      }
      SCHED();
      if (j == 11) TS_MARK();
    }
#undef LOADA
#undef LOADB
    TS_MARK();
#undef LOADV
#undef ZPART
    char* tp = sm + 65536 + kh * 16384 + l * 8;
#pragma unroll
    for (int m = 0; m < 4; ++m)
#pragma unroll
      for (int i = 0; i < 2; ++i)
        *(u32x2*)(tp + (m * 8 + (c0 ^ i)) * 512) = (u32x2){pk_bf(acc[m][i][0], acc[m][i][1]), pk_bf(acc[m][i][2], acc[m][i][3])};
    BARRIER();
    TS_MARK();
    unsigned pp[2][8];
#pragma unroll
    for (int k = 0; k < 2; ++k)
#pragma unroll
      for (int c = 0; c < 8; ++c) pp[k][c] = *(const unsigned*)(sm + 65536 + k * 16384 + ((w & 3) * 8 + c) * 512 + l * 8 + kh * 4);
    SCHED();
    const rsrc_t ro = mk_rsrc(out, 2048 * 640 * 2);
#pragma unroll
    for (int r = 0; r < 2; ++r) {
      u32x4 o;
#pragma unroll
      for (int c = 0; c < 4; ++c) {
        const float a = r ? bf_hi(pp[0][2 * c]) + bf_hi(pp[1][2 * c]) : bf_lo(pp[0][2 * c]) + bf_lo(pp[1][2 * c]);
        const float b2 = r ? bf_hi(pp[0][2 * c + 1]) + bf_hi(pp[1][2 * c + 1]) : bf_lo(pp[0][2 * c + 1]) + bf_lo(pp[1][2 * c + 1]);
        o[c] = pk_bf(a + bf_lo(xres[r][c]), b2 + bf_hi(xres[r][c]));
      }
      if (i16 < nd1) ST128(o, ro, orow[r]);
    }
    TS_MARK();
    TS_END(0, 0);
    return;
  }
  const int aux = idx * 8 + xcd;
  u64 tag;
  f16x2 tc[8], tsn[8];
  u32x4 G1[8], G3[8];
  u32x4 wo_[3];
  u16x2 m2 = {0, 0};
  const u16* wl;
  unsigned wdst;
  int wst;
  {
    const int row = 4 * w + g, i16 = l & 15;
    dma1(l * 16, (const char*)&g_T80 + w * 2048, smb + A_T80 + w * 2048);
    dma1(l * 16, (const char*)&g_T80 + w * 2048 + 1024, smb + A_T80 + w * 2048 + 1024);
    const u32x4* xp = (const u32x4*)(x + s_of_rho(aux * 32 + row) * 640) + i16;
    u32x4 xv[5];
#pragma unroll
    for (int c = 0; c < 5; ++c) xv[c] = xp[c * 16];
    const u64 ep = ld_dev(ws + WS_EP);
    SCHED();
    {
      const float a0 = (float)(aux * w) * (1.f / 640.f), a1 = (float)(l & 31) * (1.f / 2048.f);
#pragma unroll
      for (int e = 0; e < 8; ++e) {
        const float a = a0 + a1 * (float)(2 * (e & 1) + 8 * (e >> 1) + 4 * (l >> 5)), a2 = a + a1;
        tc[e] = as_h2(pk_h(__builtin_amdgcn_cosf(a), __builtin_amdgcn_cosf(a2)));
        tsn[e] = as_h2(pk_h(__builtin_amdgcn_sinf(a), __builtin_amdgcn_sinf(a2)));
        asm volatile("" : "+v"(tc[e]), "+v"(tsn[e]));
      }
    }
    SCHED();
    WAIT_V(6);
    BARRIER();
    u32x4 fm[15];
#pragma unroll
    for (int u = 0; u < 15; ++u) fm[u] = *(const u32x4*)(sm + A_T80 + u * 1024 + l * 16);
    SCHED();
    TS_MARK();
    float f[40];
#pragma unroll
    for (int c = 0; c < 5; ++c)
#pragma unroll
      for (int j = 0; j < 4; ++j) { f[c * 8 + 2 * j] = bf_lo(xv[c][j]); f[c * 8 + 2 * j + 1] = bf_hi(xv[c][j]); }
    tag = next_epoch(ep);
    asm volatile("" ::"v"(tag), "v"(f[39]));
    SCHED();
    {
      const char* wsrc = (const char*)(w < 4 ? w_r : w_i) + (64 * (4 * (aux >> 3) + (w & 3)) + 8 * (aux & 7)) * 642;
#pragma unroll
      for (int i = 0; i < 5; ++i) DMAW(l * 16, wsrc + i * 1024, smb + A_W + w * 5152 + i * 1024);
      if (l == 0) DMAW(0, wsrc + 5120, smb + A_W + w * 5152 + 5120);
      dma1(l * 16, (const char*)&g_TF + w * 2048, smb + A_TF + w * 2048);
      dma1(l * 16, (const char*)&g_TF + w * 2048 + 1024, smb + A_TF + w * 2048 + 1024);
    }
    SCHED();
    float s4[4] = {0.f, 0.f, 0.f, 0.f};
#pragma unroll
    for (int e = 0; e < 40; ++e) s4[e & 3] += f[e];
    const float mean = sum16((s4[0] + s4[1]) + (s4[2] + s4[3])) * (1.f / 640.f);
    float q4[4] = {0.f, 0.f, 0.f, 0.f};
#pragma unroll
    for (int e = 0; e < 40; ++e) { f[e] -= mean; q4[e & 3] += f[e] * f[e]; }
    const float mb = rnd_bf(mean), vb = rnd_bf(sum16((q4[0] + q4[1]) + (q4[2] + q4[3])) * (1.f / 640.f));
    const float sd = rnd_bf(__builtin_amdgcn_sqrtf(rnd_bf(vb + 1e-5f)));
    const float rs = __builtin_amdgcn_rcpf(sd), off = (mean - mb) * rs;
    if (i16 == 0) st_dev(ws + WS_ST + (aux * 32 + row) * 8, (tag << 32) | (f2u(mb) >> 16) | f2u(sd));
#pragma unroll
    for (int c = 0; c < 5; ++c) {
      u32x4 oh;
#pragma unroll
      for (int j = 0; j < 4; ++j) oh[j] = pk_h(f[c * 8 + 2 * j] * rs + off, f[c * 8 + 2 * j + 1] * rs + off);
      *(u32x4*)(sm + A_XH + row * 1344 + (c * 16 + i16) * 16) = oh;
    }
    TS_MARK();
    {
      const int h = l >> 5, p = l & 3;
      const char* rd = sm + A_XH + (4 * w + 2 * (g & 1) + (p >> 1)) * 1344 + (8 * h + ((l & 15) >> 2)) * 16 + (p & 1) * 8;
      u32x4 ax[5];
#pragma unroll
      for (int ks = 0; ks < 5; ++ks) {
        const u32x2 lo = tr_read(rd + ks * 256), hi = tr_read(rd + ks * 256 + 64);
        ax[ks] = (u32x4){lo[0], lo[1], hi[0], hi[1]};
      }
      SCHED();
      const int ol = l & 31;
      char* ut = sm + A_UT + (w >> 1) * 10304 + (ol >> 1) * 256 + (w & 1) * 8;
      const int ub = (8 * h + (ol & 1)) ^ (ol & 14);
      f32x16 acc[3];
#pragma unroll
      for (int nt = 0; nt < 3; ++nt) {
#pragma unroll
        for (int e = 0; e < 16; ++e) acc[nt][e] = 0.f;
#pragma unroll
        for (int ks = 0; ks < 5; ++ks) acc[nt] = MFMA32_H(ax[ks], fm[nt * 5 + ks], acc[nt]);
      }
#pragma unroll
      for (int nt = 0; nt < 3; ++nt)
        if (nt < 2 || ol < 16) {
#pragma unroll
          for (int dl = 0; dl < 4; ++dl)
            *(u32x2*)(ut + nt * 4096 + ((ub ^ (2 * dl)) << 4)) = (u32x2){pk_h(acc[nt][dl], acc[nt][4 + dl]), pk_h(acc[nt][8 + dl], acc[nt][12 + dl])};
        }
    }
    WAIT_V(1);
    BARRIER();
    TS_MARK();
    {
      u32x4 uv[5];
#pragma unroll
      for (int i = 0; i < 5; ++i) {
        const int seg = i * 128 + (tid >> 2);
        uv[i] = *(const u32x4*)(sm + A_UT + (tid & 3) * 10304 + ((seg ^ ((seg >> 3) & 14)) << 4));
      }
      SCHED();
#pragma unroll
      for (int i = 0; i < 5; ++i) ST128(uv[i], rw, WS_UB + (i * 8 + w) * CP + aux * 1024 + l * 16);
    }
    SCHED();
    const int eq = l & 3, kb = (l >> 2) & 7, cl = 2 * (w & 3) + (l >> 5), half = w >> 2;
    int fr = 64 * (4 * (aux >> 3) + eq) + 8 * (aux & 7) + kb;
    if (half) fr = (2048 - fr) & 2047;
    wl = (const u16*)(sm + A_W) + eq * 2576 + kb * 321 + (half ? 80 - cl : cl);
    wdst = WS_WT + cl * CP + (half * 2048 + (fr >> 8) * 256 + (fr & 63) * 4 + ((fr >> 6) & 3)) * 16;
    wst = half ? -8 : 8;
#define WT_READ(i0)                                                                                                   \
  do {                                                                                                                \
    _Pragma("unroll") for (int i_ = 0; i_ < 3; ++i_)                                                                  \
      _Pragma("unroll") for (int j_ = 0; j_ < 4; ++j_)                                                                \
        wo_[i_][j_] = __builtin_bit_cast(unsigned, ((u16x2){(wl + wst * ((i0) + i_))[80 * j_], (wl + wst * ((i0) + i_))[10304 + 80 * j_]})); \
    _Pragma("unroll") for (int i_ = 0; i_ < 3; ++i_) {                                                                \
      _Pragma("unroll") for (int j_ = 0; j_ < 4; ++j_) m2 = __builtin_elementwise_max(m2, __builtin_bit_cast(u16x2, wo_[i_][j_] & 0x7fff7fffu)); \
      if (w >= 4) wo_[i_] = (u32x4){wo_[i_][3], wo_[i_][2], wo_[i_][1], wo_[i_][0]} ^ 0x80000000u;                    \
      asm volatile("" : "+v"(wo_[i_]));                                                                               \
    }                                                                                                                 \
  } while (0)
#define WT_STORE(i0)                                                                                                  \
  do {                                                                                                                \
    _Pragma("unroll") for (int i_ = 0; i_ < 3; ++i_)                                                                  \
      if ((i0) + i_ < 5 || ((w & 3) == 0 && l < 32)) ST128(wo_[i_], rw, wdst + ((i0) + i_) * 8 * CP);                 \
  } while (0)
    WT_READ(0);
    SCHED();
    TS_MARK();
    WAIT_V(0);
    __builtin_amdgcn_s_barrier();
    if (tid == 0) st_dev(ws + WS_F1 + aux * 8, tag);
    TS_MARK();
    WT_STORE(0);
    SCHED();
    WT_READ(3);
    WT_STORE(3);
    const unsigned mx = max64(max((unsigned)m2[0], (unsigned)m2[1]));
    if (l == 0) ((unsigned*)(sm + A_MX))[w] = mx;
    SCHED();
    TS_MARK();
  }
#define PUBLISH_F2()                                                                                                  \
  do {                                                                                                                \
    if (tid == 0) {                                                                                                   \
      const u32x4 m0_ = *(const u32x4*)(sm + A_MX), m1_ = *(const u32x4*)(sm + A_MX + 16);                            \
      const unsigned mx_ = max(max(max(m0_[0], m0_[1]), max(m0_[2], m0_[3])), max(max(m1_[0], m1_[1]), max(m1_[2], m1_[3]))); \
      st_dev(ws + WS_F2 + aux * 8, (tag & ~0xffffull) | mx_);                                                         \
    }                                                                                                                 \
  } while (0)
  WAIT_V(0);
  BARRIER();
  PUBLISH_F2();
  TS_MARK();
  if (aux < 41) {
    const int c1 = aux, c1s = c1 == 40 ? 0 : c1;
    const bool real_cls = c1s == 0;
    char* eb = sm + A_E;
    const u32x4 g8 = *(const u32x4*)&g_TF.g8[l][0][0];
#pragma unroll
    for (int i = 0; i < 8; ++i) { G1[i] = *(const u32x4*)(sm + A_TF + i * 1024 + l * 16); G3[i] = *(const u32x4*)(sm + A_TF + 8192 + i * 1024 + l * 16); }
    SCHED();
    wait_flags(ws + WS_F1 + l * 8, tag);
    TS_MARK();
    u32x4 ar[4], ai[4];
    {
      const unsigned uo = WS_UB + c1s * CP + (l >> 2) * 1024 + w * 128 + (l & 3) * 16;
#pragma unroll
      for (int i = 0; i < 4; ++i) {
        ar[i] = LD128_DEV(rw, uo + (c1 == 40 ? 64 : 0) + i * 16384);
        ai[i] = LD128_DEV(rw, uo + 64 + i * 16384);
      }
    }
    u64 f2 = ld_dev(ws + WS_F2 + l * 8);
#pragma unroll
    for (int i = 0; i < 8; ++i) asm volatile("" : "+v"(G1[i]), "+v"(G3[i]));
    SCHED();
    if (real_cls) {
#pragma unroll
      for (int i = 0; i < 4; ++i) ai[i] = (u32x4){0u, 0u, 0u, 0u};
    }
    char* erow = eb + (2 * w) * 4096 + ((l ^ e_key(w)) << 4);
    f32x16 zero;
#pragma unroll
    for (int e = 0; e < 16; ++e) zero[e] = 0.f;
    u32x2 wv[16];
    const unsigned wbase = WS_WT + c1 * CP + (g >> 1) * 32768 + (g & 1) * 8 + (l & 15) * 16 + ((4 * ((w >> 1) & 1) + (w & 1)) * 64 + 32 * (w >> 2)) * 64;
    bool early;
    {
      f32x16 dr[2], di[2];
#pragma unroll
      for (int t = 0; t < 2; ++t) {
        dr[t] = zero; di[t] = zero;
#pragma unroll
        for (int ks = 0; ks < 2; ++ks) {
          const u32x4 C = G1[(t * 2 + ks) * 2], S = G1[(t * 2 + ks) * 2 + 1];
          dr[t] = MFMA32_H(ar[t * 2 + ks], C, dr[t]);
          di[t] = MFMA32_H(ai[t * 2 + ks], C, di[t]);
          dr[t] = MFMA32_H(ai[t * 2 + ks], S, dr[t]);
          di[t] = MFMA32_H(neg_h(ar[t * 2 + ks]), S, di[t]);
        }
      }
      SCHED();
      TS_MARK();
      early = all_lanes((f2 >> 16) == (tag >> 16));
      asm volatile("" ::: "memory");
      if (early) {
#pragma unroll
        for (int i = 0; i < 16; ++i) wv[i] = LD64_DEV(rw, wbase + (i & 1) * 8192 + (i >> 1) * 256);
      }
      SCHED();
      u32x4 br[2][2], bi[2][2];
      bfly_tw<false>(dr, di, tc, tsn, br, bi);
#pragma unroll
      for (int t2 = 0; t2 < 2; ++t2) {
        f32x16 xr_ = zero, xi_ = zero;
#pragma unroll
        for (int ks = 0; ks < 2; ++ks) {
          const u32x4 C = G3[(t2 * 2 + ks) * 2], S = G3[(t2 * 2 + ks) * 2 + 1];
          xr_ = MFMA32_H(C, br[t2][ks], xr_);
          xi_ = MFMA32_H(C, bi[t2][ks], xi_);
          xr_ = MFMA32_H(S, bi[t2][ks], xr_);
          xi_ = MFMA32_H(S, neg_h(br[t2][ks]), xi_);
        }
        u32x4 o0[2], o1[2];
        pack_h(xr_, o0);
        pack_h(xi_, o1);
#pragma unroll
        for (int s2 = 0; s2 < 2; ++s2) {
          *(u32x4*)(erow + (t2 * 2 + s2) * 1024) = o0[s2];
          *(u32x4*)(erow + 4096 + (t2 * 2 + s2) * 1024) = swap_halves(o1[s2]);
        }
      }
    }
    SCHED();
    TS_MARK();
    if (!early) {
      f2 = wait_flags(ws + WS_F2 + l * 8, tag);
#pragma unroll
      for (int i = 0; i < 16; ++i) wv[i] = LD64_DEV(rw, wbase + (i & 1) * 8192 + (i >> 1) * 256);
    }
    float osc;
    u32x2 gi;
    {
      int ex = (int)(max64((unsigned)f2 & 0xffffu) >> 7);
      ex = ex < 8 ? 8 : (ex > 240 ? 240 : ex);
      const float up = u2f((unsigned)(250 - ex) << 23);
      gi = (u32x2){pk_bf(bf_lo(g8[2]) * up, bf_hi(g8[2]) * up), pk_bf(bf_lo(g8[3]) * up, bf_hi(g8[3]) * up)};
      osc = u2f((unsigned)(ex + 2) << 23) * ((real_cls ? 2.f : 4.f) * 256.f / (239.f * 640.f));
    }
    BARRIER();
    TS_MARK();
    {
      const int rr = 4 * g + ((l & 15) >> 2), wrw = l & 15;
      const int rdo = rr * 4096 + w * 512 + (((l & 3) ^ e_key(rr >> 1)) << 4) + (rr & 1) * 8;
      const int wro = wrw * 4096 + w * 512 + ((g ^ e_key(wrw >> 1)) << 4) + (wrw & 1) * 8;
      const u32x2 gf = {g8[0], g8[1]};
      const f32x4 z4 = {0.f, 0.f, 0.f, 0.f};
      u32x2 ev[16];
#pragma unroll
      for (int i = 0; i < 16; ++i) ev[i] = tr_read(eb + (rdo ^ (((i >> 1) & 3) * 64 + (i & 1) * 8)) + (i >> 3) * 256);
      SCHED();
      f32x4 xx[16], vv[16];
      xx[0] = MFMA16K16_H(gf, ev[0], z4);
      xx[1] = MFMA16K16_H(gf, ev[1], z4);
#pragma unroll
      for (int i = 0; i < 18; ++i) {
        if (i + 2 < 16) xx[i + 2] = MFMA16K16_H(gf, ev[i + 2], z4);
        if (i < 16) {
          const f32x4 xs = xx[i];
          const float r0 = bf_lo(wv[i][0]), i0 = bf_hi(wv[i][0]), r1 = bf_lo(wv[i][1]), i1 = bf_hi(wv[i][1]);
          const u32x2 y = {pk_bf(xs[0] * r0 - xs[1] * i0, xs[0] * i0 + xs[1] * r0), pk_bf(xs[2] * r1 - xs[3] * i1, xs[2] * i1 + xs[3] * r1)};
          vv[i] = MFMA16K16_BF(y, gi, z4);
        }
        if (i >= 2) {
          const int k = i - 2;
          *(u32x2*)(eb + (wro ^ (((k >> 1) & 3) * 64 + (k & 1) * 8)) + (k >> 3) * 256) = (u32x2){pk_h(vv[k][0], vv[k][1]), pk_h(vv[k][2], vv[k][3])};
        }
        SCHED();
      }
    }
    BARRIER();
    TS_MARK();
    {
      f32x16 dr[2], di[2];
      u32x4 yr[4], yi[4];
#pragma unroll
      for (int i = 0; i < 4; ++i) { yr[i] = *(const u32x4*)(erow + i * 1024); yi[i] = *(const u32x4*)(erow + 4096 + i * 1024); }
      SCHED();
#pragma unroll
      for (int t2 = 0; t2 < 2; ++t2) {
        dr[t2] = zero; di[t2] = zero;
#pragma unroll
        for (int s2 = 0; s2 < 2; ++s2) {
          const u32x4 pr_ = yr[t2 * 2 + s2], pi_ = swap_halves(yi[t2 * 2 + s2]);
          const u32x4 C = G1[(t2 * 2 + s2) * 2], S = G1[(t2 * 2 + s2) * 2 + 1];
          dr[t2] = MFMA32_H(pr_, C, dr[t2]);
          di[t2] = MFMA32_H(pi_, C, di[t2]);
          dr[t2] = MFMA32_H(neg_h(pi_), S, dr[t2]);
          di[t2] = MFMA32_H(pr_, S, di[t2]);
        }
      }
      u32x4 qr[2][2], qi[2][2];
      bfly_tw<true>(dr, di, tc, tsn, qr, qi);
      const unsigned vo = WS_VT + (w * 41 + c1) * VP + l * 16;
#pragma unroll
      for (int t = 0; t < 2; ++t) {
        f32x16 zr = zero, zq = zero;
#pragma unroll
        for (int ks = 0; ks < 2; ++ks) {
          const u32x4 C = G3[(t * 2 + ks) * 2], S = G3[(t * 2 + ks) * 2 + 1];
          zr = MFMA32_H(C, qr[t][ks], zr);
          zq = MFMA32_H(C, qi[t][ks], zq);
          zr = MFMA32_H(S, neg_h(qi[t][ks]), zr);
          zq = MFMA32_H(S, qr[t][ks], zq);
        }
#pragma unroll
        for (int u = 0; u < 4; ++u) {
          u32x4 o;
#pragma unroll
          for (int j = 0; j < 4; ++j) o[j] = pk_bf(zr[4 * u + j] * osc, zq[4 * u + j] * osc);
          ST128(o, rw, vo + (t * 4 + u) * 1024);
        }
      }
    }
    WAIT_V(0);
    __builtin_amdgcn_s_barrier();
    if (tid == 0) st_dev(ws + WS_FV + c1 * 8, tag);
    TS_END(0, 1);
    return;
  }
  if (aux == 63 && w == 0) {
    wait_flags(ws + WS_F1 + l * 8, tag);
#pragma unroll 1
    for (int i = 0; i < 3; ++i) wait_flags(ws + WS_FA + i * 1024 + l * 16, tag);
    if (l == 0) st_dev(ws + WS_EP, tag);
  }
  TS_END(0, 2);
}

#if defined(__HIP_DEVICE_COMPILE__)
#pragma clang attribute pop
#endif

extern "C" void kernel_launch(void* const* d_in, const int* in_sizes, int n_in, void* d_out, int out_size, void* d_ws, size_t ws_size,
                              hipStream_t stream) {
  const u16* x = (const u16*)d_in[0];
  const u16* w_r = (const u16*)d_in[1];
  const u16* w_i = (const u16*)d_in[2];
  const u16* wg = (const u16*)d_in[4];
  const u16* wu = (const u16*)d_in[5];
  const u16* wd = (const u16*)d_in[6];
  char* ws = (char*)d_ws;
  hipStreamCaptureStatus cs_;
  hipStreamIsCapturing(stream, &cs_);
  if (cs_ == hipStreamCaptureStatusNone) {
    hipPointerAttribute_t at; hipPointerGetAttributes(&at, d_ws);
    fprintf(stderr, "DIAG ptrs x %p wg %p ws %p out %p type %d flags %u managed %d\n", d_in[0], d_in[4], d_ws, d_out, (int)at.type, at.allocationFlags, at.isManaged);
    for (int pass = 0; pass < 2; ++pass) {
      for (int it = 0; it < (pass ? 50 : 1); ++it) ka<<<256, 512, 0, stream>>>(x, w_r, w_i, wg, wu, wd, ws, (u16*)d_out);
      hipStreamSynchronize(stream);
      std::vector<unsigned long long> h(256 * 16);
      hipMemcpy(h.data(), ws + (64u << 20), h.size() * 8, hipMemcpyDeviceToHost);
      unsigned k0 = ~0u;
      for (int b = 0; b < 256; ++b) k0 = std::min(k0, (unsigned)h[b * 16]);
      for (int role = 0; role < 2; ++role) {
        std::vector<double> v[15], c[15]; int ns = 0;
        for (int b = 0; b < 256; ++b) {
          unsigned long long* p = &h[b * 16];
          if ((int)(p[15] & 0xff) != role) continue;
          ns = (p[15] >> 8) & 0xff;
          for (int i = 0; i < ns; ++i) { v[i].push_back(((unsigned)p[i] - k0) * 0.01); c[i].push_back(i ? (double)((p[i] >> 32) - (p[i - 1] >> 32)) : 0.); }
        }
        fprintf(stderr, "DIAG p%d r%d:", pass, role);
        for (int i = 0; i < ns; ++i) { std::sort(v[i].begin(), v[i].end()); std::sort(c[i].begin(), c[i].end()); fprintf(stderr, " [%.1f %.1f|%.0f]", v[i][v[i].size() / 2], v[i].back(), c[i][c[i].size() / 2]); }
        fprintf(stderr, "\n");
      }
    }
  }
  ka<<<256, 512, 0, stream>>>(x, w_r, w_i, wg, wu, wd, ws, (u16*)d_out);
}
```

```cpp
#include <hip/hip_runtime.h>
#include <hip/hip_bf16.h>

typedef unsigned short u16;
typedef __attribute__((ext_vector_type(8))) __bf16 bf16x8;
typedef __attribute__((ext_vector_type(2))) __bf16 bf16x2;
typedef __attribute__((ext_vector_type(8))) _Float16 f16x8;
typedef __attribute__((ext_vector_type(4))) _Float16 f16x4;
typedef __attribute__((ext_vector_type(2))) _Float16 f16x2;
typedef __attribute__((ext_vector_type(4))) short s16x4;
typedef __attribute__((ext_vector_type(2))) float f32x2;
typedef __attribute__((ext_vector_type(4))) float f32x4;
typedef __attribute__((ext_vector_type(16))) float f32x16;
typedef __attribute__((ext_vector_type(4))) unsigned u32x4;
typedef __attribute__((ext_vector_type(2))) unsigned u32x2;
typedef __attribute__((ext_vector_type(2))) u16 u16x2;
typedef __amdgpu_buffer_rsrc_t rsrc_t;
#define DI __device__ __forceinline__
#define LDS_AS __attribute__((address_space(3)))

namespace tb {
constexpr double kPi = 3.14159265358979323846264338327950288;
constexpr double sinT(double x) { double x2 = x * x, t = x, s = x; for (int i = 1; i < 12; ++i) { t *= -x2 / ((2 * i) * (2 * i + 1)); s += t; } return s; }
constexpr double cosT(double x) { double x2 = x * x, t = 1, s = 1; for (int i = 1; i < 12; ++i) { t *= -x2 / ((2 * i - 1) * (2 * i)); s += t; } return s; }
constexpr double cos2pi(int q, int den) {
  q %= den; if (2 * q > den) q = den - q;
  double sg = 1; if (4 * q > den) { q = den / 2 - q; sg = -1; }
  if (8 * q > den) return sg * sinT(2 * kPi * (den / 4 - q) / den);
  return sg * cosT(2 * kPi * q / den);
}
constexpr u16 bfbits(double d) { unsigned u = __builtin_bit_cast(unsigned, (float)d); u += 0x7fffu + ((u >> 16) & 1u); return (u16)(u >> 16); }
constexpr u16 hbits(double d) { return __builtin_bit_cast(u16, (_Float16)d); }
constexpr int krow(int s, int h, int j) { return 16 * s + 8 * (j >> 2) + 4 * h + (j & 3); }
template <int N> struct CT { double c[N]; };
template <int N> constexpr CT<N> mkC() { CT<N> c{}; for (int i = 0; i < N; ++i) c.c[i] = cos2pi(i, N); return c; }
constexpr CT<64> c64 = mkC<64>();
constexpr CT<80> c80 = mkC<80>();
constexpr double m80(int o, int d) { return o == 1 ? c80.c[(40 * d) % 80] : c80.c[((o >> 1) * d + ((o & 1) ? 20 : 0)) % 80]; }
struct alignas(16) TabF {
  u16 g1[2][2][2][64][8];
  u16 g3[2][2][2][64][8];
  u16 g8[64][2][4];
};
constexpr TabF mkTabF() {
  TabF T{};
  for (int t = 0; t < 2; ++t) for (int ks = 0; ks < 2; ++ks) for (int l = 0; l < 64; ++l) for (int j = 0; j < 8; ++j) {
    const int r = l & 31, k = krow(ks, l >> 5, j);
    const int q1 = (r * (2 * k + t)) & 63, q3 = (k * (2 * r + t)) & 63;
    T.g1[t][ks][0][l][j] = hbits(c64.c[q1] * 0.25); T.g1[t][ks][1][l][j] = hbits(c64.c[(q1 + 48) & 63] * 0.25);
    T.g3[t][ks][0][l][j] = hbits(c64.c[q3] * 0.25); T.g3[t][ks][1][l][j] = hbits(c64.c[(q3 + 48) & 63] * 0.25);
  }
  for (int l = 0; l < 64; ++l) for (int e = 0; e < 4; ++e) {
    const int m = l & 15, k = 4 * (l >> 4) + e, q = (8 * (m >> 1) * (k >> 1)) & 63;
    const double C = c64.c[q], S = c64.c[(q + 48) & 63];
    T.g8[l][0][e] = hbits(0.25 * ((m & 1) == (k & 1) ? C : ((m & 1) ? -S : S)));
    T.g8[l][1][e] = bfbits(239. / 256. * ((m & 1) == (k & 1) ? C : ((k & 1) ? -S : S)));
  }
  return T;
}
struct alignas(16) Tab80 {
  u16 f[3][5][64][8];
  u16 pad[512];
  u16 i[6][5][64][4];
};
constexpr Tab80 mkTab80() {
  Tab80 T{};
  for (int n = 0; n < 3; ++n) for (int ks = 0; ks < 5; ++ks) for (int l = 0; l < 64; ++l) for (int j = 0; j < 8; ++j)
    T.f[n][ks][l][j] = 32 * n + (l & 31) < 80 ? hbits(m80(32 * n + (l & 31), 16 * ks + 8 * (l >> 5) + j)) : (u16)0;
  for (int n = 0; n < 6; ++n) for (int ks = 0; ks < 5; ++ks) for (int l = 0; l < 64; ++l) for (int e = 0; e < 4; ++e)
    T.i[n][ks][l][e] = bfbits(m80(16 * ks + 4 * (l >> 4) + e, 13 * n + (n < 2 ? n : 2) + (l & 15)));
  return T;
}
}
static __device__ const tb::TabF g_TF = tb::mkTabF();
static __device__ const tb::Tab80 g_T80 = tb::mkTab80();

constexpr unsigned WS_EP = 0;
constexpr unsigned WS_F1 = 256;
constexpr unsigned WS_F2 = WS_F1 + 512;
constexpr unsigned WS_FV = WS_F2 + 512;
constexpr unsigned WS_FH = WS_FV + 512;
constexpr unsigned WS_FA = 4096;
constexpr unsigned WS_ST = 8192;
constexpr unsigned CP = 65536 + 256;
constexpr unsigned WS_UB = WS_ST + 2048 * 8;
constexpr unsigned WS_WT = WS_UB + 40 * CP;
constexpr unsigned WS_AF = WS_WT + 41 * CP;
constexpr unsigned WS_BF = WS_AF + 32 * 48 * 4096;
constexpr unsigned VP = 8192 + 256;
constexpr unsigned WS_VT = WS_BF + 6 * 48 * 7168;
constexpr unsigned WS_END = WS_VT + 328 * VP;

#if defined(__HIP_DEVICE_COMPILE__)
#pragma clang attribute push(__attribute__((target("no-packed-fp32-ops"))), apply_to = function)
#endif
DI float u2f(unsigned u) { return __builtin_bit_cast(float, u); }
DI unsigned f2u(float f) { return __builtin_bit_cast(unsigned, f); }
DI bool all_lanes(bool p) { return __builtin_amdgcn_ballot_w64(!p) == 0; }
DI int s_of_rho(int rho) {
  const int t = rho >> 10, ks = (rho >> 9) & 1, h = (rho >> 8) & 1, r = (rho >> 3) & 31, jh = (rho >> 2) & 1, jl = rho & 3;
  return 64 * (16 * ks + 8 * jh + 4 * h + jl) + 32 * t + r;
}
DI float bf_lo(unsigned u) { return u2f(u << 16); }
DI float bf_hi(unsigned u) { return u2f(u & 0xffff0000u); }
DI unsigned pk_bf(float a, float b) { f32x2 f = {a, b}; return __builtin_bit_cast(unsigned, __builtin_convertvector(f, bf16x2)); }
DI unsigned pk_h(float a, float b) { f32x2 f = {a, b}; return __builtin_bit_cast(unsigned, __builtin_convertvector(f, f16x2)); }
DI float rnd_bf(float a) { return bf_lo(pk_bf(a, 0.f)); }
#define MFMA16_BF(a, b, c) __builtin_amdgcn_mfma_f32_16x16x32_bf16(__builtin_bit_cast(bf16x8, a), __builtin_bit_cast(bf16x8, b), c, 0, 0, 0)
#define MFMA32_BF(a, b, c) __builtin_amdgcn_mfma_f32_32x32x16_bf16(__builtin_bit_cast(bf16x8, a), __builtin_bit_cast(bf16x8, b), c, 0, 0, 0)
#define MFMA32_H(a, b, c) __builtin_amdgcn_mfma_f32_32x32x16_f16(__builtin_bit_cast(f16x8, a), __builtin_bit_cast(f16x8, b), c, 0, 0, 0)
#define MFMA16K16_H(a, b, c) __builtin_amdgcn_mfma_f32_16x16x16f16(__builtin_bit_cast(f16x4, a), __builtin_bit_cast(f16x4, b), c, 0, 0, 0)
#define MFMA16K16_BF(a, b, c) __builtin_amdgcn_mfma_f32_16x16x16bf16_1k(__builtin_bit_cast(s16x4, a), __builtin_bit_cast(s16x4, b), c, 0, 0, 0)
DI u32x2 tr_read(const char* p) {
  s16x4 v = __builtin_amdgcn_ds_read_tr16_b64_v4i16((LDS_AS s16x4*)(p));
  return __builtin_bit_cast(u32x2, v);
}
DI rsrc_t mk_rsrc(const void* p, unsigned bytes) { return __builtin_amdgcn_make_buffer_rsrc((void*)p, 0, bytes, 0x00020000); }
#define ST128(v, r, off) __builtin_amdgcn_raw_buffer_store_b128(v, r, off, 0, 16)
#define ST64(v, r, off) __builtin_amdgcn_raw_buffer_store_b64(v, r, off, 0, 16)
#define WAIT_V(n) asm volatile("s_waitcnt vmcnt(%0)" ::"n"(n) : "memory")
#define WAIT_L(n) asm volatile("s_waitcnt lgkmcnt(%0)" ::"n"(n) : "memory")
#define SCHED() __builtin_amdgcn_sched_barrier(0)
#define BARRIER() do { WAIT_L(0); __builtin_amdgcn_s_barrier(); asm volatile("" ::: "memory"); } while (0)
DI void dma1(unsigned voff, const void* sbase, unsigned lds) {
  asm volatile("s_mov_b32 m0, %2\n\ts_nop 3\n\tglobal_load_lds_dwordx4 %0, %1" ::"v"(voff), "s"(sbase), "s"(lds) : "memory");
}
DI void dma1nt(unsigned voff, const void* sbase, unsigned lds) {
  asm volatile("s_mov_b32 m0, %2\n\ts_nop 3\n\tglobal_load_lds_dwordx4 %0, %1 nt" ::"v"(voff), "s"(sbase), "s"(lds) : "memory");
}
template <int OFF> DI void dma1q(unsigned voff, const void* sbase, unsigned lds) {
  asm volatile("s_add_u32 m0, %2, %3\n\ts_nop 0\n\tglobal_load_lds_dwordx4 %0, %1" ::"v"(voff), "s"(sbase), "s"(lds), "n"(OFF) : "memory", "scc");
}
template <int CTRL> DI unsigned dpp_u(unsigned x) { return (unsigned)__builtin_amdgcn_mov_dpp((int)x, CTRL, 0xf, 0xf, true); }
template <int CTRL> DI float dpp_f(float x) { return u2f(dpp_u<CTRL>(f2u(x))); }
DI float sum16(float x) {
  x += dpp_f<0xB1>(x); x += dpp_f<0x4E>(x); x += dpp_f<0x141>(x); x += dpp_f<0x128>(x);
  return x;
}
DI float sum_rows(float x) {
  const auto s = __builtin_amdgcn_permlane16_swap(f2u(x), f2u(x), false, false);
  x = u2f(s[0]) + u2f(s[1]);
  const auto t = __builtin_amdgcn_permlane32_swap(f2u(x), f2u(x), false, false);
  return u2f(t[0]) + u2f(t[1]);
}
typedef unsigned long long u64;
typedef __attribute__((address_space(1))) u64 gu64;
#define RLX_AGENT __ATOMIC_RELAXED, __HIP_MEMORY_SCOPE_AGENT
DI u64 ld_dev(const char* p) { return __hip_atomic_load((gu64*)p, RLX_AGENT); }
DI void st_dev(char* p, u64 v) { __hip_atomic_store((gu64*)p, v, RLX_AGENT); }
DI u64 next_epoch(u64 e) { return e * 0x9E3779B97F4A7C15ull + 0x632BE59BD9B4E019ull; }
constexpr unsigned SPIN_CAP = 1u << 18;
DI u64 wait_flags(const char* p, u64 tag) {
  u64 v = 0;
#pragma unroll 1
  for (unsigned n = 0; n < SPIN_CAP; ++n) {
    v = ld_dev(p);
    if (all_lanes((v >> 16) == (tag >> 16))) break;
    __builtin_amdgcn_s_sleep(1);
  }
  asm volatile("" ::: "memory");
  return v;
}
#define LD128_DEV(r, off) __builtin_amdgcn_raw_buffer_load_b128(r, off, 0, 16)
#define LD128_DEV2(r, off, soff) __builtin_amdgcn_raw_buffer_load_b128(r, off, soff, 16)
#define LD64_DEV(r, off) __builtin_amdgcn_raw_buffer_load_b64(r, off, 0, 16)
DI unsigned max64(unsigned x) {
  x = max(x, dpp_u<0xB1>(x)); x = max(x, dpp_u<0x4E>(x)); x = max(x, dpp_u<0x141>(x)); x = max(x, dpp_u<0x128>(x));
  const auto s = __builtin_amdgcn_permlane16_swap(x, x, false, false);
  x = max(s[0], s[1]);
  const auto t = __builtin_amdgcn_permlane32_swap(x, x, false, false);
  return max(t[0], t[1]);
}
DI unsigned lds_addr(const char* p) { return (unsigned)(size_t)(LDS_AS const char*)p; }

#define TS_DECL
#define TS_SET(n)
#define TS_MARK()
#define TS_END(k, role)

#define MAPV 0
#define PFPOL "nt"
#define WPF 0
#define DMAW dma1
constexpr int CODE_LINES = 290;
constexpr int STAGE_B = 49152;
constexpr int G_CS = 3 * STAGE_B;
constexpr int G_ST = G_CS + 512;
constexpr int G_PF = G_ST + 2048;
constexpr int G_TAG = G_PF + 1024;
constexpr int G_SN = G_TAG + 1024;
constexpr int A_TF = 0;
constexpr int A_T80 = 16384;
constexpr int A_XH = 32768;
constexpr int A_UT = A_XH + 32 * 1344;
constexpr int A_W = A_UT + 4 * 10304;
constexpr int A_MX = A_W + 8 * 5152;
constexpr int A_E = 16384;
constexpr int KA_LDS = A_MX + 64;
static_assert(G_SN + 6144 <= KA_LDS && KA_LDS <= 163840, "lds");

DI void pack_h(const f32x16& v, u32x4 (&o)[2]) {
#pragma unroll
  for (int s = 0; s < 2; ++s)
#pragma unroll
    for (int j = 0; j < 4; ++j) o[s][j] = pk_h(v[8 * s + 2 * j], v[8 * s + 2 * j + 1]);
}
DI f16x2 as_h2(unsigned u) { return __builtin_bit_cast(f16x2, u); }
DI unsigned as_u(f16x2 v) { return __builtin_bit_cast(unsigned, v); }
template <bool INV> DI void bfly_tw(const f32x16 (&dr)[2], const f32x16 (&di)[2], const f16x2 (&tc)[8], const f16x2 (&ts)[8],
                                    u32x4 (&er)[2][2], u32x4 (&ei)[2][2]) {
#pragma unroll
  for (int e = 0; e < 8; ++e) {
    const f16x2 r0 = as_h2(pk_h(dr[0][2 * e], dr[0][2 * e + 1])), r1 = as_h2(pk_h(dr[1][2 * e], dr[1][2 * e + 1]));
    const f16x2 i0 = as_h2(pk_h(di[0][2 * e], di[0][2 * e + 1])), i1 = as_h2(pk_h(di[1][2 * e], di[1][2 * e + 1]));
#pragma unroll
    for (int t = 0; t < 2; ++t) {
      const f16x2 sr = t ? r0 - r1 : r0 + r1, si = t ? i0 - i1 : i0 + i1;
      const f16x2 a = sr * tc[e], c = si * tc[e];
      er[t][e >> 2][e & 3] = as_u(__builtin_elementwise_fma(INV ? -si : si, ts[e], a));
      ei[t][e >> 2][e & 3] = as_u(__builtin_elementwise_fma(INV ? sr : -sr, ts[e], c));
    }
  }
}
DI u32x4 neg_h(u32x4 v) { return v ^ 0x80008000u; }
DI u32x4 swap_halves(u32x4 v) { return (u32x4){v[2], v[3], v[0], v[1]}; }
DI int e_key(int d2) { return ((d2 & 2) << 2) | ((d2 & 1) << 2) | ((d2 & 4) >> 1) | ((d2 & 2) >> 1); }

__global__ __launch_bounds__(512, 2) void ka(const u16* __restrict__ x, const u16* __restrict__ w_r, const u16* __restrict__ w_i,
                                             const u16* __restrict__ wg, const u16* __restrict__ wu, const u16* __restrict__ wd,
                                             char* __restrict__ ws, u16* __restrict__ out) {
  __shared__ __attribute__((aligned(1024))) char sm[KA_LDS];
  const u64 tstart_ = __builtin_amdgcn_s_memrealtime();
  u64 pc0;
  asm volatile("s_getpc_b64 %0" : "=s"(pc0));
  pc0 &= ~255ull;
  const int b = blockIdx.x, tid = threadIdx.x, l = tid & 63;
  const int w = __builtin_amdgcn_readfirstlane(tid >> 6);
  const int xcd = b & 7, idx = b >> 3;
  const unsigned smb = lds_addr(sm);
  const rsrc_t rw = mk_rsrc(ws, WS_END);
  const int g = l >> 4;
  TS_DECL TS_MARK();
  if (idx >= 8) {
    const int gi = idx - 8;
    const int panel = (xcd >> 2) * 4 + (gi & 3), ft = (xcd & 3) * 6 + (gi >> 2);
    const int gwg = gi * 8 + xcd;
    const int p3 = xcd * 4 + (gi & 3), ct = gi >> 2, d1s = 13 * ct + (ct < 2 ? ct : 2), nd1 = ct < 2 ? 14 : 13;
    const int i16 = l & 15, kh = w >> 2, c0 = 2 * (w & 3) + kh;
    constexpr int NB = 18;
    u32x4 B[NB][2];
    const unsigned bo = WS_BF + (ct * 48 + kh) * 7168 + g * 224 + min(i16, 13) * 16;
#define LOADB(j) do { B[(j) % NB][0] = LD128_DEV2(rw, bo + c0 * 896, (j) * 14336); B[(j) % NB][1] = LD128_DEV2(rw, bo + (c0 ^ 1) * 896, (j) * 14336); } while (0)
    if (w >= 4) {
      const int lw = w - 4, r8 = l >> 3, c8 = l & 7;
      asm volatile("s_mov_b32 m0, %2\n\ts_nop 3\n\tglobal_load_lds_dword %0, %1 sc1" ::"v"(WS_EP + (l & 1) * 4), "s"(ws), "s"(smb + G_TAG + lw * 256) : "memory");
      unsigned xo[2], woff[2];
#pragma unroll
      for (int v = 0; v < 2; ++v) {
        xo[v] = (s_of_rho(panel * 256 + 2 * lw + v) + r8) * 1280 + ((c8 ^ ((r8 >> 1) | ((lw >> 1) << 2))) << 4);
        woff[v] = ((16 * lw + 8 * v + r8) * 1536 + ft * 64) * 2 + ((c8 ^ ((((r8 >> 1) & 1) | (v << 1)) << 1)) << 4);
      }
      const char *xs = (const char*)x, *gs = (const char*)wg, *us = (const char*)wu;
      const unsigned dx = smb + lw * 8192, dw = smb + 32768 + lw * 2048;
#define STAGE(off)                                                                                   \
  do {                                                                                               \
    _Pragma("unroll") for (int v_ = 0; v_ < 2; ++v_) {                                               \
      dma1(woff[v_], gs, dw + (off) + v_ * 1024);                                                    \
      dma1(woff[v_], us, dw + (off) + 8192 + v_ * 1024);                                             \
    }                                                                                                \
    _Pragma("unroll") for (int i_ = 0; i_ < 8; ++i_) dma1(xo[i_ >> 2], xs + (i_ & 3) * 10240, dx + (off) + i_ * 1024); \
    xs += 128; gs += 64 * 3072; us += 64 * 3072;                                                     \
  } while (0)
      STAGE(0);
      STAGE(STAGE_B);
      WAIT_V(12);
      __builtin_amdgcn_s_barrier();
      const u64 tag = next_epoch(*(const u64*)(sm + G_TAG + lw * 256));
      const unsigned fao = WS_FA + ((ct * 48 + (l < 48 ? l : 0)) % 192) * 16;
      const int job = gwg + 192 * lw;
      if (job < 288) {
        const int jc = job / 48, F = job - jc * 48, lg = min(l / 14, 3), d1 = min(13 * jc + (jc < 2 ? jc : 2) + min(l - lg * 14, 13), 79);
        const unsigned d0 = (32 * F + 4 * lg) * 1280 + d1 * 16, d16 = d0 + 16 * 1280;
        u32x4 dv[8];
        asm volatile("global_load_dwordx4 %0, %8, %10\n\tglobal_load_dwordx4 %1, %8, %10 offset:1280\n\tglobal_load_dwordx4 %2, %8, %10 offset:2560\n\t"
                     "global_load_dwordx4 %3, %8, %10 offset:3840\n\tglobal_load_dwordx4 %4, %9, %10\n\tglobal_load_dwordx4 %5, %9, %10 offset:1280\n\t"
                     "global_load_dwordx4 %6, %9, %10 offset:2560\n\tglobal_load_dwordx4 %7, %9, %10 offset:3840"
                     : "=&v"(dv[0]), "=&v"(dv[1]), "=&v"(dv[2]), "=&v"(dv[3]), "=&v"(dv[4]), "=&v"(dv[5]), "=&v"(dv[6]), "=&v"(dv[7])
                     : "v"(d0), "v"(d16), "s"(wd) : "memory");
        STAGE(2 * STAGE_B);
        WAIT_V(20);
        __builtin_amdgcn_s_barrier();
        STAGE(0);
        asm volatile("s_waitcnt vmcnt(12)" : "+v"(dv[0]), "+v"(dv[1]), "+v"(dv[2]), "+v"(dv[3]), "+v"(dv[4]), "+v"(dv[5]), "+v"(dv[6]), "+v"(dv[7])::"memory");
        if (l < 56) {
#pragma unroll
          for (int c = 0; c < 8; ++c) {
            u32x4 o;
#pragma unroll
            for (int j = 0; j < 4; ++j) o[j] = __builtin_amdgcn_perm(dv[2 * j + 1][c >> 1], dv[2 * j][c >> 1], (c & 1) ? 0x07060302u : 0x05040100u);
            ST128(o, rw, WS_BF + job * 7168 + c * 896 + l * 16);
          }
        }
      } else {
        STAGE(2 * STAGE_B);
        WAIT_V(12);
        __builtin_amdgcn_s_barrier();
        STAGE(0);
        WAIT_V(12);
      }
      __builtin_amdgcn_s_barrier();
      STAGE(STAGE_B);
      if (job < 288) WAIT_V(20); else WAIT_V(12);
      __builtin_amdgcn_s_barrier();
      STAGE(2 * STAGE_B);
      WAIT_V(12);
      __builtin_amdgcn_s_barrier();
      if (tid == 256) st_dev(ws + WS_FA + gwg * 16, tag);
      STAGE(0);
      WAIT_V(12);
      __builtin_amdgcn_s_barrier();
      STAGE(STAGE_B);
      WAIT_V(12);
      __builtin_amdgcn_s_barrier();
      asm volatile("s_mov_b32 m0, %2\n\ts_nop 3\n\tglobal_load_lds_dwordx4 %0, %1 sc1" ::"v"(fao), "s"(ws), "s"(smb + G_SN + lw * 1536) : "memory");
      if (l < 32)
        asm volatile("s_mov_b32 m0, %2\n\ts_nop 3\n\tglobal_load_lds_dwordx4 %0, %1 sc1" ::"v"(WS_ST + (panel * 256 + lw * 64) * 8 + l * 16), "s"(ws),
                     "s"(smb + G_SN + lw * 1536 + 1024) : "memory");
      STAGE(2 * STAGE_B);
      WAIT_V(12);
      __builtin_amdgcn_s_barrier();
      STAGE(0);
      {
        u64 st = *(const u64*)(sm + G_SN + lw * 1536 + 1024 + l * 8);
        const u64 fa = *(const u64*)(sm + G_SN + lw * 1536 + l * 16);
        if (!all_lanes(fa == tag && (unsigned)(st >> 32) == (unsigned)tag)) {
          wait_flags(ws + fao, tag);
#pragma unroll 1
          for (unsigned n = 0; n < SPIN_CAP; ++n) {
            st = ld_dev(ws + WS_ST + (panel * 256 + tid - 256) * 8);
            if (all_lanes((unsigned)(st >> 32) == (unsigned)tag)) break;
            __builtin_amdgcn_s_sleep(4);
          }
        }
        asm volatile("" ::: "memory");
        const float rs = __builtin_amdgcn_rcpf(bf_hi((unsigned)st));
        *(f32x2*)(sm + G_ST + (tid - 256) * 8) = (f32x2){bf_lo((unsigned)st), rs * rs};
      }
      LOADB(0); LOADB(1); LOADB(2);
      SCHED();
      WAIT_V(18);
      WAIT_L(0);
#undef STAGE
      __builtin_amdgcn_s_barrier();
      const char* bsrc = ws + WS_BF + ct * 48 * 7168 + 2 * lw * 896;
      const unsigned bl = g * 224 + min(i16, 13) * 16;
#define STAGEB(j0)                                                                                                    \
  do {                                                                                                                \
    _Pragma("unroll") for (int i_ = (j0); i_ < (j0) + 6; ++i_)                                                        \
      asm volatile("s_mov_b32 m0, %3\n\ts_nop 0\n\tglobal_load_lds_dwordx4 %0, %2 sc1\n\tglobal_load_lds_dwordx4 %1, %2 offset:1024 sc1" \
                   ::"v"(bl + 128), "v"(bl), "s"(bsrc - 128 + i_ * 14336), "s"(smb + i_ * 8192 + lw * 2048) : "memory"); \
  } while (0)
      STAGEB(6);
      LOADB(3); LOADB(4);
      SCHED();
      WAIT_V(22);
      __builtin_amdgcn_s_barrier();
      STAGEB(12);
      LOADB(5); LOADB(6);
      SCHED();
      __builtin_amdgcn_s_barrier();
      STAGEB(0);
#undef STAGEB
#pragma unroll
      for (int j = 7; j < 12; ++j) LOADB(j);
      SCHED();
      WAIT_V(10);
      __builtin_amdgcn_s_barrier();
#pragma unroll
      for (int j = 12; j < 18; ++j) LOADB(j);
    } else {
    if (w == 3) {
      asm volatile("s_mov_b32 m0, %2\n\ts_nop 3\n\tglobal_load_lds_dword %0, %1" ::"v"(min((gi % 5) * 64 + l, CODE_LINES - 1) * 128), "s"(pc0),
                   "s"(smb + G_CS) : "memory");
    }
    const int wr = w >> 1, fh = w & 1;
    {
    const int q = (l & 15) >> 2;
    const int xb0 = (i16 >> 3) * 16384 + (wr * 16 + (l & 7)) * 128 + ((g ^ (((l & 7) >> 1) | ((i16 >> 3) << 2))) << 4);
    const int wb0 = (32768 + (8 * g + q) * 128 + (((fh * 2) ^ (((q >> 1) & 1) | ((g & 1) << 1))) << 5) + (l & 3) * 8) ^ (wr << 13);
    const char* xr[2];
    const char* wp[4];
#define SET_BUF(off)                                                                                       \
  do {                                                                                                     \
    xr[0] = sm + (off) + xb0; xr[1] = sm + (off) + (xb0 ^ 64);                                             \
    _Pragma("unroll") for (int i_ = 0; i_ < 4; ++i_) wp[i_] = sm + (off) + (wb0 ^ ((i_ & 1) << 5) ^ ((i_ >> 1) << 13)); \
  } while (0)
    f32x4 acc[4][8];
#pragma unroll
    for (int i = 0; i < 4; ++i)
#pragma unroll
      for (int r = 0; r < 8; ++r) acc[i][r] = (f32x4){0.f, 0.f, 0.f, 0.f};
    f32x4 cs[2] = {{0.f, 0.f, 0.f, 0.f}, {0.f, 0.f, 0.f, 0.f}};
    u32x4 ones = {0x3f803f80u, 0x3f803f80u, 0x3f803f80u, 0x3f803f80u};
    asm volatile("" : "+v"(ones));
    u32x4 xf[2][8];
    u32x2 wf[2][8];
#define RD(ks, n)                                                                                          \
  do {                                                                                                     \
    if ((n) >= 2 && (n) < 10) xf[ks][(n) - 2] = *(const u32x4*)(xr[ks] + (((n) - 2) & 3) * 4096 + (((n) - 2) >> 2) * 1024); \
    else { const int m_ = (n) < 2 ? (n) : (n) - 8; wf[ks][m_] = tr_read(wp[m_ >> 1] + (ks) * 4096 + (m_ & 1) * 512); } \
  } while (0)
#define WF(ks, i) ((u32x4){wf[ks][2 * (i)][0], wf[ks][2 * (i)][1], wf[ks][2 * (i) + 1][0], wf[ks][2 * (i) + 1][1]})
#define KSTEP(ks, READS, CSUM)                                                                             \
  do {                                                                                                     \
    _Pragma("unroll") for (int n_ = 0; n_ < 32; ++n_) {                                                    \
      acc[n_ >> 3][n_ & 7] = MFMA16_BF(WF(ks, n_ >> 3), xf[ks][n_ & 7], acc[n_ >> 3][n_ & 7]);             \
      if (READS && n_ < 16) RD((ks) ^ 1, n_);                                                              \
      if (CSUM && n_ >= 30) cs[n_ & 1] = MFMA16_BF(WF(ks, n_ & 1), ones, cs[n_ & 1]);                      \
      SCHED();                                                                                             \
    }                                                                                                      \
  } while (0)
    SET_BUF(0);
    TS_MARK();
    BARRIER();
    TS_MARK();
#pragma unroll
    for (int n = 0; n < 16; ++n) RD(0, n);
    SCHED();
    KSTEP(0, true, 1);
    int nxt = STAGE_B;
#pragma unroll 1
    for (int u = 0; u < 9; ++u) {
      BARRIER();
      SET_BUF(nxt);
      SCHED();
      KSTEP(1, true, 1);
      KSTEP(0, true, 1);
      nxt = nxt == 2 * STAGE_B ? 0 : nxt + STAGE_B;
    }
    TS_MARK();
#pragma unroll
    for (int i = 0; i < 2; ++i) {
      cs[i] = MFMA16_BF(WF(1, i), ones, cs[i]);
      if ((l & 15) == 0) *(f32x4*)(sm + G_CS + w * 128 + i * 64 + g * 16) = cs[i];
    }
    BARRIER();
    f32x4 co[2];
    f32x4 st[4];
#pragma unroll
    for (int i = 0; i < 2; ++i) co[i] = *(const f32x4*)(sm + G_CS + (w ^ 2) * 128 + i * 64 + g * 16);
#pragma unroll
    for (int r = 0; r < 4; ++r) st[r] = *(const f32x4*)(sm + G_ST + (wr * 128 + (r >> 1) * 64 + 8 * (l & 7) + 4 * (i16 >> 3) + (r & 1) * 2) * 8);
    SCHED();
    TS_MARK();
    const unsigned ao = WS_AF + ((panel * 4 + wr * 2) * 48 + ft * 2 + fh) * 4096 + l * 16;
#pragma unroll
    for (int r = 0; r < 10; ++r) {
      if (r < 8) {
#pragma unroll
        for (int i = 0; i < 4; ++i) acc[i][r] = MFMA16_BF(WF(1, i), xf[1][r], acc[i][r]);
      }
      if (r >= 2) {
        const int e = r - 2;
        const float mb = st[e >> 1][(e & 1) * 2], r2 = st[e >> 1][(e & 1) * 2 + 1];
        u32x4 o;
#pragma unroll
        for (int i = 0; i < 2; ++i) {
          const f32x4 hv = ((acc[i][e] - mb * cs[i]) * r2) * (acc[2 + i][e] - mb * co[i]);
          o[2 * i] = pk_bf(hv[0], hv[1]); o[2 * i + 1] = pk_bf(hv[2], hv[3]);
        }
        ST128(o, rw, ao + (e >> 2) * 48 * 4096 + (e & 3) * 1024);
      }
      SCHED();
    }
#undef SET_BUF
#undef RD
#undef WF
#undef KSTEP
    }
    SCHED();
    TS_MARK();
    WAIT_V(0);
    __builtin_amdgcn_s_barrier();
    asm volatile("" ::: "memory");
    TS_MARK();
    const u64 tag = next_epoch(*(const u64*)(sm + G_TAG));
    if (tid == 0) st_dev(ws + WS_FH + (panel * 32 + ft) * 8, tag);
    SCHED();
#pragma unroll
    for (int j = 0; j < 18; ++j)
#pragma unroll
      for (int i = 0; i < 2; ++i) B[j][i] = *(const u32x4*)(sm + j * 8192 + (2 * w + i) * 1024 + l * 16);
    SCHED();
    if (w == 0) wait_flags(ws + WS_FH + (xcd * 32 + (l < 24 ? l : 0)) * 8, tag);
    }
#pragma unroll
    for (int j = 0; j < NB; ++j) asm volatile("" : "+v"(B[j][0]), "+v"(B[j][1]));
    SCHED();
    TS_SET(7);
    BARRIER();
    asm volatile("" ::: "memory");
    TS_MARK();
    const char* asrc = ws + WS_AF + p3 * 48 * 4096 + w * 4096;
#define LOADA(ch)                                                                                                     \
  asm volatile("s_mov_b32 m0, %2\n\ts_nop 3\n\tglobal_load_lds_dwordx4 %0, %1 sc1\n\tglobal_load_lds_dwordx4 %0, %1 offset:1024 sc1\n\t" \
               "global_load_lds_dwordx4 %0, %1 offset:2048 sc1\n\tglobal_load_lds_dwordx4 %0, %1 offset:3072 sc1" ::"v"(l * 16), \
               "s"(asrc + (ch) * 32768), "s"(smb + ((ch) & 3) * 32768 + w * 4096) : "memory")
    LOADA(0); LOADA(1);
    const u64 tag = next_epoch(*(const u64*)(sm + G_TAG));
    f32x4 acc[4][2];
#pragma unroll
    for (int m = 0; m < 4; ++m) { acc[m][0] = (f32x4){0.f, 0.f, 0.f, 0.f}; acc[m][1] = acc[m][0]; }
    const LDS_AS char* ap[2];
#pragma unroll
    for (int i = 0; i < 2; ++i) { ap[i] = (const LDS_AS char*)sm + i * 65536 + kh * 4096 + l * 16; asm volatile("" : "+v"(ap[i])); }
    u32x4 A[3][4], V[11], xres[2];
    u32x2 mi[5];
    unsigned orow[2];
    u64 fv = 0;
    const unsigned vo = WS_VT + (c0 * 41 + 2 * g) * VP + (p3 >> 3) * 2048 + ((l >> 3) & 1) * 1024 + ((p3 & 7) * 8 + (l & 7)) * 16;
#define LOADV()                                                                                                       \
  do {                                                                                                                \
    _Pragma("unroll") for (int u = 0; u < 10; ++u) V[u] = LD128_DEV2(rw, vo, ((u >> 1) * 8 + (u & 1)) * VP);         \
    V[10] = LD128_DEV2(rw, vo - 2 * g * VP, 40 * VP);                                                                 \
  } while (0)
    bool early = false;
    WAIT_V(4);
    BARRIER();
    TS_MARK();
#define ZPART()                                                                                                       \
  do {                                                                                                                \
    if (g == 0) {                                                                                                     \
      _Pragma("unroll") for (int m = 0; m < 4; ++m) V[0][m] = (V[0][m] & 0xffffu) | (V[10][m] << 16);     \
    }                                                                                                                 \
    _Pragma("unroll") for (int ks = 0; ks < 5; ++ks)                                                                  \
      _Pragma("unroll") for (int m = 0; m < 4; ++m)                                                                   \
        acc[m][0] = MFMA16K16_BF(((u32x2){V[2 * ks][m], V[2 * ks + 1][m]}), mi[ks], acc[m][0]);                       \
  } while (0)
#pragma unroll
    for (int m = 0; m < 8; ++m) A[m >> 2][m & 3] = *(const LDS_AS u32x4*)(ap[0] + (m >> 2) * 8192 + (m & 3) * 1024);
    SCHED();
#pragma unroll
    for (int j = 0; j < 24; ++j) {
      if ((j & 3) == 2 && j < 22) {
        if (j == 2) WAIT_V(8); else if (j == 6) WAIT_V(14); else if (j == 10) WAIT_V(21); else if (j == 14) WAIT_V(7);
        BARRIER();
      }
#pragma unroll
      for (int n = 0; n < 8; ++n) {
        acc[n & 3][n >> 2] = MFMA16_BF(A[j % 3][n & 3], B[j % NB][n >> 2], acc[n & 3][n >> 2]);
        if (n < 4 && j < 22) A[(j + 2) % 3][n] = *(const LDS_AS u32x4*)(ap[((j + 2) >> 3) & 1] + ((j + 2) & 7) * 8192 + n * 1024);
        if (n == 4) {
          if (j < 3) LOADA(2 + j);
          if (j == 6) LOADA(5);
          if (j >= 3 && j < 6) LOADB(j + 15);
          if (j >= 7 && j < 10) LOADB(j + 14);
          if (j == 9) fv = ld_dev(ws + WS_FV + (l < 41 ? l : 0) * 8);
          if (j == 12) {
#pragma unroll
            for (int ks = 0; ks < 5; ++ks) mi[ks] = *(const u32x2*)&g_T80.i[ct][ks][l][0];
            const rsrc_t rx = mk_rsrc(x, 2048 * 640 * 2);
#pragma unroll
            for (int r = 0; r < 2; ++r) {
              orow[r] = s_of_rho(p3 * 64 + 8 * ((4 * g + 2 * kh + r) & 7) + 4 * (g >> 1) + (w & 3)) * 1280 + min(d1s + i16, 79) * 16;
              xres[r] = __builtin_amdgcn_raw_buffer_load_b128(rx, orow[r], 0, 0);
            }
          }
        }
        SCHED();
      }
      if (j == 14) {
        early = all_lanes(fv == tag);
        asm volatile("" ::: "memory");
        LOADV();
      }
      if (j == 21) {
        if (!early) {
          wait_flags(ws + WS_FV + (l < 41 ? l : 0) * 8, tag);
          LOADV();
        }
#pragma unroll
        for (int u = 0; u < 11; ++u) asm volatile("" : "+v"(V[u]));
        ZPART();
      }
      SCHED();
      if (j == 11) TS_MARK();
    }
#undef LOADA
#undef LOADB
    TS_MARK();
#undef LOADV
#undef ZPART
    char* tp = sm + 65536 + kh * 16384 + l * 8;
#pragma unroll
    for (int m = 0; m < 4; ++m)
#pragma unroll
      for (int i = 0; i < 2; ++i)
        *(u32x2*)(tp + (m * 8 + (c0 ^ i)) * 512) = (u32x2){pk_bf(acc[m][i][0], acc[m][i][1]), pk_bf(acc[m][i][2], acc[m][i][3])};
    BARRIER();
    TS_MARK();
    unsigned pp[2][8];
#pragma unroll
    for (int k = 0; k < 2; ++k)
#pragma unroll
      for (int c = 0; c < 8; ++c) pp[k][c] = *(const unsigned*)(sm + 65536 + k * 16384 + ((w & 3) * 8 + c) * 512 + l * 8 + kh * 4);
    SCHED();
    const rsrc_t ro = mk_rsrc(out, 2048 * 640 * 2);
#pragma unroll
    for (int r = 0; r < 2; ++r) {
      u32x4 o;
#pragma unroll
      for (int c = 0; c < 4; ++c) {
        const float a = r ? bf_hi(pp[0][2 * c]) + bf_hi(pp[1][2 * c]) : bf_lo(pp[0][2 * c]) + bf_lo(pp[1][2 * c]);
        const float b2 = r ? bf_hi(pp[0][2 * c + 1]) + bf_hi(pp[1][2 * c + 1]) : bf_lo(pp[0][2 * c + 1]) + bf_lo(pp[1][2 * c + 1]);
        o[c] = pk_bf(a + bf_lo(xres[r][c]), b2 + bf_hi(xres[r][c]));
      }
      if (i16 < nd1) ST128(o, ro, orow[r]);
    }
    TS_MARK();
    TS_END(0, 0);
    return;
  }
  const int aux = idx * 8 + xcd;
  u64 tag;
  f16x2 tc[8], tsn[8];
  u32x4 G1[8], G3[8];
  u32x4 wo_[3];
  u16x2 m2 = {0, 0};
  const u16* wl;
  unsigned wdst;
  int wst;
  {
    const int row = 4 * w + g, i16 = l & 15;
    dma1(l * 16, (const char*)&g_T80 + w * 2048, smb + A_T80 + w * 2048);
    dma1(l * 16, (const char*)&g_T80 + w * 2048 + 1024, smb + A_T80 + w * 2048 + 1024);
    const u32x4* xp = (const u32x4*)(x + s_of_rho(aux * 32 + row) * 640) + i16;
    u32x4 xv[5];
#pragma unroll
    for (int c = 0; c < 5; ++c) xv[c] = xp[c * 16];
    const u64 ep = ld_dev(ws + WS_EP);
    SCHED();
    {
      const float a0 = (float)(aux * w) * (1.f / 640.f), a1 = (float)(l & 31) * (1.f / 2048.f);
#pragma unroll
      for (int e = 0; e < 8; ++e) {
        const float a = a0 + a1 * (float)(2 * (e & 1) + 8 * (e >> 1) + 4 * (l >> 5)), a2 = a + a1;
        tc[e] = as_h2(pk_h(__builtin_amdgcn_cosf(a), __builtin_amdgcn_cosf(a2)));
        tsn[e] = as_h2(pk_h(__builtin_amdgcn_sinf(a), __builtin_amdgcn_sinf(a2)));
        asm volatile("" : "+v"(tc[e]), "+v"(tsn[e]));
      }
    }
    SCHED();
    WAIT_V(6);
    BARRIER();
    u32x4 fm[15];
#pragma unroll
    for (int u = 0; u < 15; ++u) fm[u] = *(const u32x4*)(sm + A_T80 + u * 1024 + l * 16);
    SCHED();
    TS_MARK();
    float f[40];
#pragma unroll
    for (int c = 0; c < 5; ++c)
#pragma unroll
      for (int j = 0; j < 4; ++j) { f[c * 8 + 2 * j] = bf_lo(xv[c][j]); f[c * 8 + 2 * j + 1] = bf_hi(xv[c][j]); }
    tag = next_epoch(ep);
    asm volatile("" ::"v"(tag), "v"(f[39]));
    SCHED();
    {
      const char* wsrc = (const char*)(w < 4 ? w_r : w_i) + (64 * (4 * (aux >> 3) + (w & 3)) + 8 * (aux & 7)) * 642;
#pragma unroll
      for (int i = 0; i < 5; ++i) DMAW(l * 16, wsrc + i * 1024, smb + A_W + w * 5152 + i * 1024);
      if (l == 0) DMAW(0, wsrc + 5120, smb + A_W + w * 5152 + 5120);
      dma1(l * 16, (const char*)&g_TF + w * 2048, smb + A_TF + w * 2048);
      dma1(l * 16, (const char*)&g_TF + w * 2048 + 1024, smb + A_TF + w * 2048 + 1024);
    }
    SCHED();
    float s4[4] = {0.f, 0.f, 0.f, 0.f};
#pragma unroll
    for (int e = 0; e < 40; ++e) s4[e & 3] += f[e];
    const float mean = sum16((s4[0] + s4[1]) + (s4[2] + s4[3])) * (1.f / 640.f);
    float q4[4] = {0.f, 0.f, 0.f, 0.f};
#pragma unroll
    for (int e = 0; e < 40; ++e) { f[e] -= mean; q4[e & 3] += f[e] * f[e]; }
    const float mb = rnd_bf(mean), vb = rnd_bf(sum16((q4[0] + q4[1]) + (q4[2] + q4[3])) * (1.f / 640.f));
    const float sd = rnd_bf(__builtin_amdgcn_sqrtf(rnd_bf(vb + 1e-5f)));
    const float rs = __builtin_amdgcn_rcpf(sd), off = (mean - mb) * rs;
    if (i16 == 0) st_dev(ws + WS_ST + (aux * 32 + row) * 8, (tag << 32) | (f2u(mb) >> 16) | f2u(sd));
#pragma unroll
    for (int c = 0; c < 5; ++c) {
      u32x4 oh;
#pragma unroll
      for (int j = 0; j < 4; ++j) oh[j] = pk_h(f[c * 8 + 2 * j] * rs + off, f[c * 8 + 2 * j + 1] * rs + off);
      *(u32x4*)(sm + A_XH + row * 1344 + (c * 16 + i16) * 16) = oh;
    }
    TS_MARK();
    {
      const int h = l >> 5, p = l & 3;
      const char* rd = sm + A_XH + (4 * w + 2 * (g & 1) + (p >> 1)) * 1344 + (8 * h + ((l & 15) >> 2)) * 16 + (p & 1) * 8;
      u32x4 ax[5];
#pragma unroll
      for (int ks = 0; ks < 5; ++ks) {
        const u32x2 lo = tr_read(rd + ks * 256), hi = tr_read(rd + ks * 256 + 64);
        ax[ks] = (u32x4){lo[0], lo[1], hi[0], hi[1]};
      }
      SCHED();
      const int ol = l & 31;
      char* ut = sm + A_UT + (w >> 1) * 10304 + (ol >> 1) * 256 + (w & 1) * 8;
      const int ub = (8 * h + (ol & 1)) ^ (ol & 14);
      f32x16 acc[3];
#pragma unroll
      for (int nt = 0; nt < 3; ++nt) {
#pragma unroll
        for (int e = 0; e < 16; ++e) acc[nt][e] = 0.f;
#pragma unroll
        for (int ks = 0; ks < 5; ++ks) acc[nt] = MFMA32_H(ax[ks], fm[nt * 5 + ks], acc[nt]);
      }
#pragma unroll
      for (int nt = 0; nt < 3; ++nt)
        if (nt < 2 || ol < 16) {
#pragma unroll
          for (int dl = 0; dl < 4; ++dl)
            *(u32x2*)(ut + nt * 4096 + ((ub ^ (2 * dl)) << 4)) = (u32x2){pk_h(acc[nt][dl], acc[nt][4 + dl]), pk_h(acc[nt][8 + dl], acc[nt][12 + dl])};
        }
    }
    WAIT_V(1);
    BARRIER();
    TS_MARK();
    {
      u32x4 uv[5];
#pragma unroll
      for (int i = 0; i < 5; ++i) {
        const int seg = i * 128 + (tid >> 2);
        uv[i] = *(const u32x4*)(sm + A_UT + (tid & 3) * 10304 + ((seg ^ ((seg >> 3) & 14)) << 4));
      }
      SCHED();
#pragma unroll
      for (int i = 0; i < 5; ++i) ST128(uv[i], rw, WS_UB + (i * 8 + w) * CP + aux * 1024 + l * 16);
    }
    SCHED();
    const int eq = l & 3, kb = (l >> 2) & 7, cl = 2 * (w & 3) + (l >> 5), half = w >> 2;
    int fr = 64 * (4 * (aux >> 3) + eq) + 8 * (aux & 7) + kb;
    if (half) fr = (2048 - fr) & 2047;
    wl = (const u16*)(sm + A_W) + eq * 2576 + kb * 321 + (half ? 80 - cl : cl);
    wdst = WS_WT + cl * CP + (half * 2048 + (fr >> 8) * 256 + (fr & 63) * 4 + ((fr >> 6) & 3)) * 16;
    wst = half ? -8 : 8;
#define WT_READ(i0)                                                                                                   \
  do {                                                                                                                \
    _Pragma("unroll") for (int i_ = 0; i_ < 3; ++i_)                                                                  \
      _Pragma("unroll") for (int j_ = 0; j_ < 4; ++j_)                                                                \
        wo_[i_][j_] = __builtin_bit_cast(unsigned, ((u16x2){(wl + wst * ((i0) + i_))[80 * j_], (wl + wst * ((i0) + i_))[10304 + 80 * j_]})); \
    _Pragma("unroll") for (int i_ = 0; i_ < 3; ++i_) {                                                                \
      _Pragma("unroll") for (int j_ = 0; j_ < 4; ++j_) m2 = __builtin_elementwise_max(m2, __builtin_bit_cast(u16x2, wo_[i_][j_] & 0x7fff7fffu)); \
      if (w >= 4) wo_[i_] = (u32x4){wo_[i_][3], wo_[i_][2], wo_[i_][1], wo_[i_][0]} ^ 0x80000000u;                    \
      asm volatile("" : "+v"(wo_[i_]));                                                                               \
    }                                                                                                                 \
  } while (0)
#define WT_STORE(i0)                                                                                                  \
  do {                                                                                                                \
    _Pragma("unroll") for (int i_ = 0; i_ < 3; ++i_)                                                                  \
      if ((i0) + i_ < 5 || ((w & 3) == 0 && l < 32)) ST128(wo_[i_], rw, wdst + ((i0) + i_) * 8 * CP);                 \
  } while (0)
    WT_READ(0);
    SCHED();
    TS_MARK();
    WAIT_V(0);
    __builtin_amdgcn_s_barrier();
    if (tid == 0) st_dev(ws + WS_F1 + aux * 8, tag);
    TS_MARK();
    WT_STORE(0);
    SCHED();
    WT_READ(3);
    WT_STORE(3);
    const unsigned mx = max64(max((unsigned)m2[0], (unsigned)m2[1]));
    if (l == 0) ((unsigned*)(sm + A_MX))[w] = mx;
    SCHED();
    TS_MARK();
  }
#define PUBLISH_F2()                                                                                                  \
  do {                                                                                                                \
    if (tid == 0) {                                                                                                   \
      const u32x4 m0_ = *(const u32x4*)(sm + A_MX), m1_ = *(const u32x4*)(sm + A_MX + 16);                            \
      const unsigned mx_ = max(max(max(m0_[0], m0_[1]), max(m0_[2], m0_[3])), max(max(m1_[0], m1_[1]), max(m1_[2], m1_[3]))); \
      st_dev(ws + WS_F2 + aux * 8, (tag & ~0xffffull) | mx_);                                                         \
    }                                                                                                                 \
  } while (0)
  WAIT_V(0);
  BARRIER();
  PUBLISH_F2();
  TS_MARK();
  if (aux < 41) {
    const int c1 = aux, c1s = c1 == 40 ? 0 : c1;
    const bool real_cls = c1s == 0;
    char* eb = sm + A_E;
    const u32x4 g8 = *(const u32x4*)&g_TF.g8[l][0][0];
#pragma unroll
    for (int i = 0; i < 8; ++i) { G1[i] = *(const u32x4*)(sm + A_TF + i * 1024 + l * 16); G3[i] = *(const u32x4*)(sm + A_TF + 8192 + i * 1024 + l * 16); }
    SCHED();
    wait_flags(ws + WS_F1 + l * 8, tag);
    TS_MARK();
    u32x4 ar[4], ai[4];
    {
      const unsigned uo = WS_UB + c1s * CP + (l >> 2) * 1024 + w * 128 + (l & 3) * 16;
#pragma unroll
      for (int i = 0; i < 4; ++i) {
        ar[i] = LD128_DEV(rw, uo + (c1 == 40 ? 64 : 0) + i * 16384);
        ai[i] = LD128_DEV(rw, uo + 64 + i * 16384);
      }
    }
    u64 f2 = ld_dev(ws + WS_F2 + l * 8);
#pragma unroll
    for (int i = 0; i < 8; ++i) asm volatile("" : "+v"(G1[i]), "+v"(G3[i]));
    SCHED();
    if (real_cls) {
#pragma unroll
      for (int i = 0; i < 4; ++i) ai[i] = (u32x4){0u, 0u, 0u, 0u};
    }
    char* erow = eb + (2 * w) * 4096 + ((l ^ e_key(w)) << 4);
    f32x16 zero;
#pragma unroll
    for (int e = 0; e < 16; ++e) zero[e] = 0.f;
    u32x2 wv[16];
    const unsigned wbase = WS_WT + c1 * CP + (g >> 1) * 32768 + (g & 1) * 8 + (l & 15) * 16 + ((4 * ((w >> 1) & 1) + (w & 1)) * 64 + 32 * (w >> 2)) * 64;
    bool early;
    {
      f32x16 dr[2], di[2];
#pragma unroll
      for (int t = 0; t < 2; ++t) {
        dr[t] = zero; di[t] = zero;
#pragma unroll
        for (int ks = 0; ks < 2; ++ks) {
          const u32x4 C = G1[(t * 2 + ks) * 2], S = G1[(t * 2 + ks) * 2 + 1];
          dr[t] = MFMA32_H(ar[t * 2 + ks], C, dr[t]);
          di[t] = MFMA32_H(ai[t * 2 + ks], C, di[t]);
          dr[t] = MFMA32_H(ai[t * 2 + ks], S, dr[t]);
          di[t] = MFMA32_H(neg_h(ar[t * 2 + ks]), S, di[t]);
        }
      }
      SCHED();
      TS_MARK();
      early = all_lanes((f2 >> 16) == (tag >> 16));
      asm volatile("" ::: "memory");
      if (early) {
#pragma unroll
        for (int i = 0; i < 16; ++i) wv[i] = LD64_DEV(rw, wbase + (i & 1) * 8192 + (i >> 1) * 256);
      }
      SCHED();
      u32x4 br[2][2], bi[2][2];
      bfly_tw<false>(dr, di, tc, tsn, br, bi);
#pragma unroll
      for (int t2 = 0; t2 < 2; ++t2) {
        f32x16 xr_ = zero, xi_ = zero;
#pragma unroll
        for (int ks = 0; ks < 2; ++ks) {
          const u32x4 C = G3[(t2 * 2 + ks) * 2], S = G3[(t2 * 2 + ks) * 2 + 1];
          xr_ = MFMA32_H(C, br[t2][ks], xr_);
          xi_ = MFMA32_H(C, bi[t2][ks], xi_);
          xr_ = MFMA32_H(S, bi[t2][ks], xr_);
          xi_ = MFMA32_H(S, neg_h(br[t2][ks]), xi_);
        }
        u32x4 o0[2], o1[2];
        pack_h(xr_, o0);
        pack_h(xi_, o1);
#pragma unroll
        for (int s2 = 0; s2 < 2; ++s2) {
          *(u32x4*)(erow + (t2 * 2 + s2) * 1024) = o0[s2];
          *(u32x4*)(erow + 4096 + (t2 * 2 + s2) * 1024) = swap_halves(o1[s2]);
        }
      }
    }
    SCHED();
    TS_MARK();
    if (!early) {
      f2 = wait_flags(ws + WS_F2 + l * 8, tag);
#pragma unroll
      for (int i = 0; i < 16; ++i) wv[i] = LD64_DEV(rw, wbase + (i & 1) * 8192 + (i >> 1) * 256);
    }
    float osc;
    u32x2 gi;
    {
      int ex = (int)(max64((unsigned)f2 & 0xffffu) >> 7);
      ex = ex < 8 ? 8 : (ex > 240 ? 240 : ex);
      const float up = u2f((unsigned)(250 - ex) << 23);
      gi = (u32x2){pk_bf(bf_lo(g8[2]) * up, bf_hi(g8[2]) * up), pk_bf(bf_lo(g8[3]) * up, bf_hi(g8[3]) * up)};
      osc = u2f((unsigned)(ex + 2) << 23) * ((real_cls ? 2.f : 4.f) * 256.f / (239.f * 640.f));
    }
    BARRIER();
    TS_MARK();
    {
      const int rr = 4 * g + ((l & 15) >> 2), wrw = l & 15;
      const int rdo = rr * 4096 + w * 512 + (((l & 3) ^ e_key(rr >> 1)) << 4) + (rr & 1) * 8;
      const int wro = wrw * 4096 + w * 512 + ((g ^ e_key(wrw >> 1)) << 4) + (wrw & 1) * 8;
      const u32x2 gf = {g8[0], g8[1]};
      const f32x4 z4 = {0.f, 0.f, 0.f, 0.f};
      u32x2 ev[16];
#pragma unroll
      for (int i = 0; i < 16; ++i) ev[i] = tr_read(eb + (rdo ^ (((i >> 1) & 3) * 64 + (i & 1) * 8)) + (i >> 3) * 256);
      SCHED();
      f32x4 xx[16], vv[16];
      xx[0] = MFMA16K16_H(gf, ev[0], z4);
      xx[1] = MFMA16K16_H(gf, ev[1], z4);
#pragma unroll
      for (int i = 0; i < 18; ++i) {
        if (i + 2 < 16) xx[i + 2] = MFMA16K16_H(gf, ev[i + 2], z4);
        if (i < 16) {
          const f32x4 xs = xx[i];
          const float r0 = bf_lo(wv[i][0]), i0 = bf_hi(wv[i][0]), r1 = bf_lo(wv[i][1]), i1 = bf_hi(wv[i][1]);
          const u32x2 y = {pk_bf(xs[0] * r0 - xs[1] * i0, xs[0] * i0 + xs[1] * r0), pk_bf(xs[2] * r1 - xs[3] * i1, xs[2] * i1 + xs[3] * r1)};
          vv[i] = MFMA16K16_BF(y, gi, z4);
        }
        if (i >= 2) {
          const int k = i - 2;
          *(u32x2*)(eb + (wro ^ (((k >> 1) & 3) * 64 + (k & 1) * 8)) + (k >> 3) * 256) = (u32x2){pk_h(vv[k][0], vv[k][1]), pk_h(vv[k][2], vv[k][3])};
        }
        SCHED();
      }
    }
    BARRIER();
    TS_MARK();
    {
      f32x16 dr[2], di[2];
      u32x4 yr[4], yi[4];
#pragma unroll
      for (int i = 0; i < 4; ++i) { yr[i] = *(const u32x4*)(erow + i * 1024); yi[i] = *(const u32x4*)(erow + 4096 + i * 1024); }
      SCHED();
#pragma unroll
      for (int t2 = 0; t2 < 2; ++t2) {
        dr[t2] = zero; di[t2] = zero;
#pragma unroll
        for (int s2 = 0; s2 < 2; ++s2) {
          const u32x4 pr_ = yr[t2 * 2 + s2], pi_ = swap_halves(yi[t2 * 2 + s2]);
          const u32x4 C = G1[(t2 * 2 + s2) * 2], S = G1[(t2 * 2 + s2) * 2 + 1];
          dr[t2] = MFMA32_H(pr_, C, dr[t2]);
          di[t2] = MFMA32_H(pi_, C, di[t2]);
          dr[t2] = MFMA32_H(neg_h(pi_), S, dr[t2]);
          di[t2] = MFMA32_H(pr_, S, di[t2]);
        }
      }
      u32x4 qr[2][2], qi[2][2];
      bfly_tw<true>(dr, di, tc, tsn, qr, qi);
      const unsigned vo = WS_VT + (w * 41 + c1) * VP + l * 16;
#pragma unroll
      for (int t = 0; t < 2; ++t) {
        f32x16 zr = zero, zq = zero;
#pragma unroll
        for (int ks = 0; ks < 2; ++ks) {
          const u32x4 C = G3[(t * 2 + ks) * 2], S = G3[(t * 2 + ks) * 2 + 1];
          zr = MFMA32_H(C, qr[t][ks], zr);
          zq = MFMA32_H(C, qi[t][ks], zq);
          zr = MFMA32_H(S, neg_h(qi[t][ks]), zr);
          zq = MFMA32_H(S, qr[t][ks], zq);
        }
#pragma unroll
        for (int u = 0; u < 4; ++u) {
          u32x4 o;
#pragma unroll
          for (int j = 0; j < 4; ++j) o[j] = pk_bf(zr[4 * u + j] * osc, zq[4 * u + j] * osc);
          ST128(o, rw, vo + (t * 4 + u) * 1024);
        }
      }
    }
    WAIT_V(0);
    __builtin_amdgcn_s_barrier();
    if (tid == 0) st_dev(ws + WS_FV + c1 * 8, tag);
    TS_END(0, 1);
    return;
  }
  if (aux == 63 && w == 0) {
    wait_flags(ws + WS_F1 + l * 8, tag);
#pragma unroll 1
    for (int i = 0; i < 3; ++i) wait_flags(ws + WS_FA + i * 1024 + l * 16, tag);
    if (l == 0) st_dev(ws + WS_EP, tag);
    while (__builtin_amdgcn_s_memrealtime() - tstart_ < 2800) __builtin_amdgcn_s_sleep(1);
  }
  TS_END(0, 2);
}

#if defined(__HIP_DEVICE_COMPILE__)
#pragma clang attribute pop
#endif

extern "C" void kernel_launch(void* const* d_in, const int* in_sizes, int n_in, void* d_out, int out_size, void* d_ws, size_t ws_size,
                              hipStream_t stream) {
  const u16* x = (const u16*)d_in[0];
  const u16* w_r = (const u16*)d_in[1];
  const u16* w_i = (const u16*)d_in[2];
  const u16* wg = (const u16*)d_in[4];
  const u16* wu = (const u16*)d_in[5];
  const u16* wd = (const u16*)d_in[6];
  char* ws = (char*)d_ws;
  ka<<<256, 512, 0, stream>>>(x, w_r, w_i, wg, wu, wd, ws, (u16*)d_out);
}
```

```cpp
#include <hip/hip_runtime.h>
#include <hip/hip_bf16.h>

typedef unsigned short u16;
typedef __attribute__((ext_vector_type(8))) __bf16 bf16x8;
typedef __attribute__((ext_vector_type(2))) __bf16 bf16x2;
typedef __attribute__((ext_vector_type(8))) _Float16 f16x8;
typedef __attribute__((ext_vector_type(4))) _Float16 f16x4;
typedef __attribute__((ext_vector_type(2))) _Float16 f16x2;
typedef __attribute__((ext_vector_type(4))) short s16x4;
typedef __attribute__((ext_vector_type(2))) float f32x2;
typedef __attribute__((ext_vector_type(4))) float f32x4;
typedef __attribute__((ext_vector_type(16))) float f32x16;
typedef __attribute__((ext_vector_type(4))) unsigned u32x4;
typedef __attribute__((ext_vector_type(2))) unsigned u32x2;
typedef __attribute__((ext_vector_type(2))) u16 u16x2;
typedef __amdgpu_buffer_rsrc_t rsrc_t;
#define DI __device__ __forceinline__
#define LDS_AS __attribute__((address_space(3)))

namespace tb {
constexpr double kPi = 3.14159265358979323846264338327950288;
constexpr double sinT(double x) { double x2 = x * x, t = x, s = x; for (int i = 1; i < 12; ++i) { t *= -x2 / ((2 * i) * (2 * i + 1)); s += t; } return s; }
constexpr double cosT(double x) { double x2 = x * x, t = 1, s = 1; for (int i = 1; i < 12; ++i) { t *= -x2 / ((2 * i - 1) * (2 * i)); s += t; } return s; }
constexpr double cos2pi(int q, int den) {
  q %= den; if (2 * q > den) q = den - q;
  double sg = 1; if (4 * q > den) { q = den / 2 - q; sg = -1; }
  if (8 * q > den) return sg * sinT(2 * kPi * (den / 4 - q) / den);
  return sg * cosT(2 * kPi * q / den);
}
constexpr u16 bfbits(double d) { unsigned u = __builtin_bit_cast(unsigned, (float)d); u += 0x7fffu + ((u >> 16) & 1u); return (u16)(u >> 16); }
constexpr u16 hbits(double d) { return __builtin_bit_cast(u16, (_Float16)d); }
constexpr int krow(int s, int h, int j) { return 16 * s + 8 * (j >> 2) + 4 * h + (j & 3); }
template <int N> struct CT { double c[N]; };
template <int N> constexpr CT<N> mkC() { CT<N> c{}; for (int i = 0; i < N; ++i) c.c[i] = cos2pi(i, N); return c; }
constexpr CT<64> c64 = mkC<64>();
constexpr CT<80> c80 = mkC<80>();
constexpr double m80(int o, int d) { return o == 1 ? c80.c[(40 * d) % 80] : c80.c[((o >> 1) * d + ((o & 1) ? 20 : 0)) % 80]; }
struct alignas(16) TabF {
  u16 g1[2][2][2][64][8];
  u16 g3[2][2][2][64][8];
  u16 g8[64][2][4];
};
constexpr TabF mkTabF() {
  TabF T{};
  for (int t = 0; t < 2; ++t) for (int ks = 0; ks < 2; ++ks) for (int l = 0; l < 64; ++l) for (int j = 0; j < 8; ++j) {
    const int r = l & 31, k = krow(ks, l >> 5, j);
    const int q1 = (r * (2 * k + t)) & 63, q3 = (k * (2 * r + t)) & 63;
    T.g1[t][ks][0][l][j] = hbits(c64.c[q1] * 0.25); T.g1[t][ks][1][l][j] = hbits(c64.c[(q1 + 48) & 63] * 0.25);
    T.g3[t][ks][0][l][j] = hbits(c64.c[q3] * 0.25); T.g3[t][ks][1][l][j] = hbits(c64.c[(q3 + 48) & 63] * 0.25);
  }
  for (int l = 0; l < 64; ++l) for (int e = 0; e < 4; ++e) {
    const int m = l & 15, k = 4 * (l >> 4) + e, q = (8 * (m >> 1) * (k >> 1)) & 63;
    const double C = c64.c[q], S = c64.c[(q + 48) & 63];
    T.g8[l][0][e] = hbits(0.25 * ((m & 1) == (k & 1) ? C : ((m & 1) ? -S : S)));
    T.g8[l][1][e] = bfbits(239. / 256. * ((m & 1) == (k & 1) ? C : ((k & 1) ? -S : S)));
  }
  return T;
}
struct alignas(16) Tab80 {
  u16 f[3][5][64][8];
  u16 pad[512];
  u16 i[6][5][64][4];
};
constexpr Tab80 mkTab80() {
  Tab80 T{};
  for (int n = 0; n < 3; ++n) for (int ks = 0; ks < 5; ++ks) for (int l = 0; l < 64; ++l) for (int j = 0; j < 8; ++j)
    T.f[n][ks][l][j] = 32 * n + (l & 31) < 80 ? hbits(m80(32 * n + (l & 31), 16 * ks + 8 * (l >> 5) + j)) : (u16)0;
  for (int n = 0; n < 6; ++n) for (int ks = 0; ks < 5; ++ks) for (int l = 0; l < 64; ++l) for (int e = 0; e < 4; ++e)
    T.i[n][ks][l][e] = bfbits(m80(16 * ks + 4 * (l >> 4) + e, 13 * n + (n < 2 ? n : 2) + (l & 15)));
  return T;
}
}
static __device__ const tb::TabF g_TF = tb::mkTabF();
static __device__ const tb::Tab80 g_T80 = tb::mkTab80();

constexpr unsigned WS_EP = 0;
constexpr unsigned WS_F1 = 256;
constexpr unsigned WS_F2 = WS_F1 + 512;
constexpr unsigned WS_FV = WS_F2 + 512;
constexpr unsigned WS_FH = WS_FV + 512;
constexpr unsigned WS_FA = 4096;
constexpr unsigned WS_ST = 8192;
constexpr unsigned CP = 65536 + 256;
constexpr unsigned WS_UB = WS_ST + 2048 * 8;
constexpr unsigned WS_WT = WS_UB + 40 * CP;
constexpr unsigned WS_AF = WS_WT + 41 * CP;
constexpr unsigned WS_BF = WS_AF + 32 * 48 * 4096;
constexpr unsigned VP = 8192 + 256;
constexpr unsigned WS_VT = WS_BF + 6 * 48 * 7168;
constexpr unsigned WS_END = WS_VT + 328 * VP;

#if defined(__HIP_DEVICE_COMPILE__)
#pragma clang attribute push(__attribute__((target("no-packed-fp32-ops"))), apply_to = function)
#endif
DI float u2f(unsigned u) { return __builtin_bit_cast(float, u); }
DI unsigned f2u(float f) { return __builtin_bit_cast(unsigned, f); }
DI bool all_lanes(bool p) { return __builtin_amdgcn_ballot_w64(!p) == 0; }
DI int s_of_rho(int rho) {
  const int t = rho >> 10, ks = (rho >> 9) & 1, h = (rho >> 8) & 1, r = (rho >> 3) & 31, jh = (rho >> 2) & 1, jl = rho & 3;
  return 64 * (16 * ks + 8 * jh + 4 * h + jl) + 32 * t + r;
}
DI float bf_lo(unsigned u) { return u2f(u << 16); }
DI float bf_hi(unsigned u) { return u2f(u & 0xffff0000u); }
DI unsigned pk_bf(float a, float b) { f32x2 f = {a, b}; return __builtin_bit_cast(unsigned, __builtin_convertvector(f, bf16x2)); }
DI unsigned pk_h(float a, float b) { f32x2 f = {a, b}; return __builtin_bit_cast(unsigned, __builtin_convertvector(f, f16x2)); }
DI float rnd_bf(float a) { return bf_lo(pk_bf(a, 0.f)); }
#define MFMA16_BF(a, b, c) __builtin_amdgcn_mfma_f32_16x16x32_bf16(__builtin_bit_cast(bf16x8, a), __builtin_bit_cast(bf16x8, b), c, 0, 0, 0)
#define MFMA32_BF(a, b, c) __builtin_amdgcn_mfma_f32_32x32x16_bf16(__builtin_bit_cast(bf16x8, a), __builtin_bit_cast(bf16x8, b), c, 0, 0, 0)
#define MFMA32_H(a, b, c) __builtin_amdgcn_mfma_f32_32x32x16_f16(__builtin_bit_cast(f16x8, a), __builtin_bit_cast(f16x8, b), c, 0, 0, 0)
#define MFMA16K16_H(a, b, c) __builtin_amdgcn_mfma_f32_16x16x16f16(__builtin_bit_cast(f16x4, a), __builtin_bit_cast(f16x4, b), c, 0, 0, 0)
#define MFMA16K16_BF(a, b, c) __builtin_amdgcn_mfma_f32_16x16x16bf16_1k(__builtin_bit_cast(s16x4, a), __builtin_bit_cast(s16x4, b), c, 0, 0, 0)
DI u32x2 tr_read(const char* p) {
  s16x4 v = __builtin_amdgcn_ds_read_tr16_b64_v4i16((LDS_AS s16x4*)(p));
  return __builtin_bit_cast(u32x2, v);
}
DI rsrc_t mk_rsrc(const void* p, unsigned bytes) { return __builtin_amdgcn_make_buffer_rsrc((void*)p, 0, bytes, 0x00020000); }
#define ST128(v, r, off) __builtin_amdgcn_raw_buffer_store_b128(v, r, off, 0, 16)
#define ST64(v, r, off) __builtin_amdgcn_raw_buffer_store_b64(v, r, off, 0, 16)
#define WAIT_V(n) asm volatile("s_waitcnt vmcnt(%0)" ::"n"(n) : "memory")
#define WAIT_L(n) asm volatile("s_waitcnt lgkmcnt(%0)" ::"n"(n) : "memory")
#define SCHED() __builtin_amdgcn_sched_barrier(0)
#define BARRIER() do { WAIT_L(0); __builtin_amdgcn_s_barrier(); asm volatile("" ::: "memory"); } while (0)
DI void dma1(unsigned voff, const void* sbase, unsigned lds) {
  asm volatile("s_mov_b32 m0, %2\n\ts_nop 3\n\tglobal_load_lds_dwordx4 %0, %1" ::"v"(voff), "s"(sbase), "s"(lds) : "memory");
}
DI void dma1nt(unsigned voff, const void* sbase, unsigned lds) {
  asm volatile("s_mov_b32 m0, %2\n\ts_nop 3\n\tglobal_load_lds_dwordx4 %0, %1 nt" ::"v"(voff), "s"(sbase), "s"(lds) : "memory");
}
template <int OFF> DI void dma1q(unsigned voff, const void* sbase, unsigned lds) {
  asm volatile("s_add_u32 m0, %2, %3\n\ts_nop 0\n\tglobal_load_lds_dwordx4 %0, %1" ::"v"(voff), "s"(sbase), "s"(lds), "n"(OFF) : "memory", "scc");
}
template <int CTRL> DI unsigned dpp_u(unsigned x) { return (unsigned)__builtin_amdgcn_mov_dpp((int)x, CTRL, 0xf, 0xf, true); }
template <int CTRL> DI float dpp_f(float x) { return u2f(dpp_u<CTRL>(f2u(x))); }
DI float sum16(float x) {
  x += dpp_f<0xB1>(x); x += dpp_f<0x4E>(x); x += dpp_f<0x141>(x); x += dpp_f<0x128>(x);
  return x;
}
DI float sum_rows(float x) {
  const auto s = __builtin_amdgcn_permlane16_swap(f2u(x), f2u(x), false, false);
  x = u2f(s[0]) + u2f(s[1]);
  const auto t = __builtin_amdgcn_permlane32_swap(f2u(x), f2u(x), false, false);
  return u2f(t[0]) + u2f(t[1]);
}
typedef unsigned long long u64;
typedef __attribute__((address_space(1))) u64 gu64;
#define RLX_AGENT __ATOMIC_RELAXED, __HIP_MEMORY_SCOPE_AGENT
DI u64 ld_dev(const char* p) { return __hip_atomic_load((gu64*)p, RLX_AGENT); }
DI void st_dev(char* p, u64 v) { __hip_atomic_store((gu64*)p, v, RLX_AGENT); }
DI u64 next_epoch(u64 e) { return e * 0x9E3779B97F4A7C15ull + 0x632BE59BD9B4E019ull; }
constexpr unsigned SPIN_CAP = 1u << 18;
DI u64 wait_flags(const char* p, u64 tag) {
  u64 v = 0;
#pragma unroll 1
  for (unsigned n = 0; n < SPIN_CAP; ++n) {
    v = ld_dev(p);
    if (all_lanes((v >> 16) == (tag >> 16))) break;
    __builtin_amdgcn_s_sleep(1);
  }
  asm volatile("" ::: "memory");
  return v;
}
#define LD128_DEV(r, off) __builtin_amdgcn_raw_buffer_load_b128(r, off, 0, 16)
#define LD128_DEV2(r, off, soff) __builtin_amdgcn_raw_buffer_load_b128(r, off, soff, 16)
#define LD64_DEV(r, off) __builtin_amdgcn_raw_buffer_load_b64(r, off, 0, 16)
DI unsigned max64(unsigned x) {
  x = max(x, dpp_u<0xB1>(x)); x = max(x, dpp_u<0x4E>(x)); x = max(x, dpp_u<0x141>(x)); x = max(x, dpp_u<0x128>(x));
  const auto s = __builtin_amdgcn_permlane16_swap(x, x, false, false);
  x = max(s[0], s[1]);
  const auto t = __builtin_amdgcn_permlane32_swap(x, x, false, false);
  return max(t[0], t[1]);
}
DI unsigned lds_addr(const char* p) { return (unsigned)(size_t)(LDS_AS const char*)p; }

#define TS_DECL
#define TS_SET(n)
#define TS_MARK()
#define TS_END(k, role)

#define MAPV 0
#define PFPOL "nt"
#define WPF 0
#define DMAW dma1
constexpr int CODE_LINES = 290;
constexpr int STAGE_B = 49152;
constexpr int G_CS = 3 * STAGE_B;
constexpr int G_ST = G_CS + 512;
constexpr int G_PF = G_ST + 2048;
constexpr int G_TAG = G_PF + 1024;
constexpr int G_SN = G_TAG + 1024;
constexpr int A_TF = 0;
constexpr int A_T80 = 16384;
constexpr int A_XH = 32768;
constexpr int A_UT = A_XH + 32 * 1344;
constexpr int A_W = A_UT + 4 * 10304;
constexpr int A_MX = A_W + 8 * 5152;
constexpr int A_E = 16384;
constexpr int KA_LDS = A_MX + 64;
static_assert(G_SN + 6144 <= KA_LDS && KA_LDS <= 163840, "lds");

DI void pack_h(const f32x16& v, u32x4 (&o)[2]) {
#pragma unroll
  for (int s = 0; s < 2; ++s)
#pragma unroll
    for (int j = 0; j < 4; ++j) o[s][j] = pk_h(v[8 * s + 2 * j], v[8 * s + 2 * j + 1]);
}
DI f16x2 as_h2(unsigned u) { return __builtin_bit_cast(f16x2, u); }
DI unsigned as_u(f16x2 v) { return __builtin_bit_cast(unsigned, v); }
template <bool INV> DI void bfly_tw(const f32x16 (&dr)[2], const f32x16 (&di)[2], const f16x2 (&tc)[8], const f16x2 (&ts)[8],
                                    u32x4 (&er)[2][2], u32x4 (&ei)[2][2]) {
#pragma unroll
  for (int e = 0; e < 8; ++e) {
    const f16x2 r0 = as_h2(pk_h(dr[0][2 * e], dr[0][2 * e + 1])), r1 = as_h2(pk_h(dr[1][2 * e], dr[1][2 * e + 1]));
    const f16x2 i0 = as_h2(pk_h(di[0][2 * e], di[0][2 * e + 1])), i1 = as_h2(pk_h(di[1][2 * e], di[1][2 * e + 1]));
#pragma unroll
    for (int t = 0; t < 2; ++t) {
      const f16x2 sr = t ? r0 - r1 : r0 + r1, si = t ? i0 - i1 : i0 + i1;
      const f16x2 a = sr * tc[e], c = si * tc[e];
      er[t][e >> 2][e & 3] = as_u(__builtin_elementwise_fma(INV ? -si : si, ts[e], a));
      ei[t][e >> 2][e & 3] = as_u(__builtin_elementwise_fma(INV ? sr : -sr, ts[e], c));
    }
  }
}
DI u32x4 neg_h(u32x4 v) { return v ^ 0x80008000u; }
DI u32x4 swap_halves(u32x4 v) { return (u32x4){v[2], v[3], v[0], v[1]}; }
DI int e_key(int d2) { return ((d2 & 2) << 2) | ((d2 & 1) << 2) | ((d2 & 4) >> 1) | ((d2 & 2) >> 1); }

__global__ __launch_bounds__(512, 2) void ka(const u16* __restrict__ x, const u16* __restrict__ w_r, const u16* __restrict__ w_i,
                                             const u16* __restrict__ wg, const u16* __restrict__ wu, const u16* __restrict__ wd,
                                             char* __restrict__ ws, u16* __restrict__ out) {
  __shared__ __attribute__((aligned(1024))) char sm[KA_LDS];
  if (blockIdx.x == 0 && threadIdx.x == 0) *(volatile u64*)(ws + (32u << 20)) = __builtin_amdgcn_s_memrealtime();
  u64 pc0;
  asm volatile("s_getpc_b64 %0" : "=s"(pc0));
  pc0 &= ~255ull;
  const int b = blockIdx.x, tid = threadIdx.x, l = tid & 63;
  const int w = __builtin_amdgcn_readfirstlane(tid >> 6);
  const int xcd = b & 7, idx = b >> 3;
  const unsigned smb = lds_addr(sm);
  const rsrc_t rw = mk_rsrc(ws, WS_END);
  const int g = l >> 4;
  TS_DECL TS_MARK();
  if (idx >= 8) {
    const int gi = idx - 8;
    const int panel = (xcd >> 2) * 4 + (gi & 3), ft = (xcd & 3) * 6 + (gi >> 2);
    const int gwg = gi * 8 + xcd;
    const int p3 = xcd * 4 + (gi & 3), ct = gi >> 2, d1s = 13 * ct + (ct < 2 ? ct : 2), nd1 = ct < 2 ? 14 : 13;
    const int i16 = l & 15, kh = w >> 2, c0 = 2 * (w & 3) + kh;
    constexpr int NB = 18;
    u32x4 B[NB][2];
    const unsigned bo = WS_BF + (ct * 48 + kh) * 7168 + g * 224 + min(i16, 13) * 16;
#define LOADB(j) do { B[(j) % NB][0] = LD128_DEV2(rw, bo + c0 * 896, (j) * 14336); B[(j) % NB][1] = LD128_DEV2(rw, bo + (c0 ^ 1) * 896, (j) * 14336); } while (0)
    if (w >= 4) {
      const int lw = w - 4, r8 = l >> 3, c8 = l & 7;
      asm volatile("s_mov_b32 m0, %2\n\ts_nop 3\n\tglobal_load_lds_dword %0, %1 sc1" ::"v"(WS_EP + (l & 1) * 4), "s"(ws), "s"(smb + G_TAG + lw * 256) : "memory");
      unsigned xo[2], woff[2];
#pragma unroll
      for (int v = 0; v < 2; ++v) {
        xo[v] = (s_of_rho(panel * 256 + 2 * lw + v) + r8) * 1280 + ((c8 ^ ((r8 >> 1) | ((lw >> 1) << 2))) << 4);
        woff[v] = ((16 * lw + 8 * v + r8) * 1536 + ft * 64) * 2 + ((c8 ^ ((((r8 >> 1) & 1) | (v << 1)) << 1)) << 4);
      }
      const char *xs = (const char*)x, *gs = (const char*)wg, *us = (const char*)wu;
      const unsigned dx = smb + lw * 8192, dw = smb + 32768 + lw * 2048;
#define STAGE(off)                                                                                   \
  do {                                                                                               \
    _Pragma("unroll") for (int v_ = 0; v_ < 2; ++v_) {                                               \
      dma1(woff[v_], gs, dw + (off) + v_ * 1024);                                                    \
      dma1(woff[v_], us, dw + (off) + 8192 + v_ * 1024);                                             \
    }                                                                                                \
    _Pragma("unroll") for (int i_ = 0; i_ < 8; ++i_) dma1(xo[i_ >> 2], xs + (i_ & 3) * 10240, dx + (off) + i_ * 1024); \
    xs += 128; gs += 64 * 3072; us += 64 * 3072;                                                     \
  } while (0)
      STAGE(0);
      STAGE(STAGE_B);
      WAIT_V(12);
      __builtin_amdgcn_s_barrier();
      const u64 tag = next_epoch(*(const u64*)(sm + G_TAG + lw * 256));
      const unsigned fao = WS_FA + ((ct * 48 + (l < 48 ? l : 0)) % 192) * 16;
      const int job = gwg + 192 * lw;
      if (job < 288) {
        const int jc = job / 48, F = job - jc * 48, lg = min(l / 14, 3), d1 = min(13 * jc + (jc < 2 ? jc : 2) + min(l - lg * 14, 13), 79);
        const unsigned d0 = (32 * F + 4 * lg) * 1280 + d1 * 16, d16 = d0 + 16 * 1280;
        u32x4 dv[8];
        asm volatile("global_load_dwordx4 %0, %8, %10\n\tglobal_load_dwordx4 %1, %8, %10 offset:1280\n\tglobal_load_dwordx4 %2, %8, %10 offset:2560\n\t"
                     "global_load_dwordx4 %3, %8, %10 offset:3840\n\tglobal_load_dwordx4 %4, %9, %10\n\tglobal_load_dwordx4 %5, %9, %10 offset:1280\n\t"
                     "global_load_dwordx4 %6, %9, %10 offset:2560\n\tglobal_load_dwordx4 %7, %9, %10 offset:3840"
                     : "=&v"(dv[0]), "=&v"(dv[1]), "=&v"(dv[2]), "=&v"(dv[3]), "=&v"(dv[4]), "=&v"(dv[5]), "=&v"(dv[6]), "=&v"(dv[7])
                     : "v"(d0), "v"(d16), "s"(wd) : "memory");
        STAGE(2 * STAGE_B);
        WAIT_V(20);
        __builtin_amdgcn_s_barrier();
        STAGE(0);
        asm volatile("s_waitcnt vmcnt(12)" : "+v"(dv[0]), "+v"(dv[1]), "+v"(dv[2]), "+v"(dv[3]), "+v"(dv[4]), "+v"(dv[5]), "+v"(dv[6]), "+v"(dv[7])::"memory");
        if (l < 56) {
#pragma unroll
          for (int c = 0; c < 8; ++c) {
            u32x4 o;
#pragma unroll
            for (int j = 0; j < 4; ++j) o[j] = __builtin_amdgcn_perm(dv[2 * j + 1][c >> 1], dv[2 * j][c >> 1], (c & 1) ? 0x07060302u : 0x05040100u);
            ST128(o, rw, WS_BF + job * 7168 + c * 896 + l * 16);
          }
        }
      } else {
        STAGE(2 * STAGE_B);
        WAIT_V(12);
        __builtin_amdgcn_s_barrier();
        STAGE(0);
        WAIT_V(12);
      }
      __builtin_amdgcn_s_barrier();
      STAGE(STAGE_B);
      if (job < 288) WAIT_V(20); else WAIT_V(12);
      __builtin_amdgcn_s_barrier();
      STAGE(2 * STAGE_B);
      WAIT_V(12);
      __builtin_amdgcn_s_barrier();
      if (tid == 256) st_dev(ws + WS_FA + gwg * 16, tag);
      STAGE(0);
      WAIT_V(12);
      __builtin_amdgcn_s_barrier();
      STAGE(STAGE_B);
      WAIT_V(12);
      __builtin_amdgcn_s_barrier();
      asm volatile("s_mov_b32 m0, %2\n\ts_nop 3\n\tglobal_load_lds_dwordx4 %0, %1 sc1" ::"v"(fao), "s"(ws), "s"(smb + G_SN + lw * 1536) : "memory");
      if (l < 32)
        asm volatile("s_mov_b32 m0, %2\n\ts_nop 3\n\tglobal_load_lds_dwordx4 %0, %1 sc1" ::"v"(WS_ST + (panel * 256 + lw * 64) * 8 + l * 16), "s"(ws),
                     "s"(smb + G_SN + lw * 1536 + 1024) : "memory");
      STAGE(2 * STAGE_B);
      WAIT_V(12);
      __builtin_amdgcn_s_barrier();
      STAGE(0);
      {
        u64 st = *(const u64*)(sm + G_SN + lw * 1536 + 1024 + l * 8);
        const u64 fa = *(const u64*)(sm + G_SN + lw * 1536 + l * 16);
        if (!all_lanes(fa == tag && (unsigned)(st >> 32) == (unsigned)tag)) {
          wait_flags(ws + fao, tag);
#pragma unroll 1
          for (unsigned n = 0; n < SPIN_CAP; ++n) {
            st = ld_dev(ws + WS_ST + (panel * 256 + tid - 256) * 8);
            if (all_lanes((unsigned)(st >> 32) == (unsigned)tag)) break;
            __builtin_amdgcn_s_sleep(4);
          }
        }
        asm volatile("" ::: "memory");
        const float rs = __builtin_amdgcn_rcpf(bf_hi((unsigned)st));
        *(f32x2*)(sm + G_ST + (tid - 256) * 8) = (f32x2){bf_lo((unsigned)st), rs * rs};
      }
      LOADB(0); LOADB(1); LOADB(2);
      SCHED();
      WAIT_V(18);
      WAIT_L(0);
#undef STAGE
      __builtin_amdgcn_s_barrier();
      const char* bsrc = ws + WS_BF + ct * 48 * 7168 + 2 * lw * 896;
      const unsigned bl = g * 224 + min(i16, 13) * 16;
#define STAGEB(j0)                                                                                                    \
  do {                                                                                                                \
    _Pragma("unroll") for (int i_ = (j0); i_ < (j0) + 6; ++i_)                                                        \
      asm volatile("s_mov_b32 m0, %3\n\ts_nop 0\n\tglobal_load_lds_dwordx4 %0, %2 sc1\n\tglobal_load_lds_dwordx4 %1, %2 offset:1024 sc1" \
                   ::"v"(bl + 128), "v"(bl), "s"(bsrc - 128 + i_ * 14336), "s"(smb + i_ * 8192 + lw * 2048) : "memory"); \
  } while (0)
      STAGEB(6);
      LOADB(3); LOADB(4);
      SCHED();
      WAIT_V(22);
      __builtin_amdgcn_s_barrier();
      STAGEB(12);
      LOADB(5); LOADB(6);
      SCHED();
      __builtin_amdgcn_s_barrier();
      STAGEB(0);
#undef STAGEB
#pragma unroll
      for (int j = 7; j < 12; ++j) LOADB(j);
      SCHED();
      WAIT_V(10);
      __builtin_amdgcn_s_barrier();
#pragma unroll
      for (int j = 12; j < 18; ++j) LOADB(j);
    } else {
    const int wr = w >> 1, fh = w & 1;
    {
    const int q = (l & 15) >> 2;
    const int xb0 = (i16 >> 3) * 16384 + (wr * 16 + (l & 7)) * 128 + ((g ^ (((l & 7) >> 1) | ((i16 >> 3) << 2))) << 4);
    const int wb0 = (32768 + (8 * g + q) * 128 + (((fh * 2) ^ (((q >> 1) & 1) | ((g & 1) << 1))) << 5) + (l & 3) * 8) ^ (wr << 13);
    const char* xr[2];
    const char* wp[4];
#define SET_BUF(off)                                                                                       \
  do {                                                                                                     \
    xr[0] = sm + (off) + xb0; xr[1] = sm + (off) + (xb0 ^ 64);                                             \
    _Pragma("unroll") for (int i_ = 0; i_ < 4; ++i_) wp[i_] = sm + (off) + (wb0 ^ ((i_ & 1) << 5) ^ ((i_ >> 1) << 13)); \
  } while (0)
    f32x4 acc[4][8];
#pragma unroll
    for (int i = 0; i < 4; ++i)
#pragma unroll
      for (int r = 0; r < 8; ++r) acc[i][r] = (f32x4){0.f, 0.f, 0.f, 0.f};
    f32x4 cs[2] = {{0.f, 0.f, 0.f, 0.f}, {0.f, 0.f, 0.f, 0.f}};
    u32x4 ones = {0x3f803f80u, 0x3f803f80u, 0x3f803f80u, 0x3f803f80u};
    asm volatile("" : "+v"(ones));
    u32x4 xf[2][8];
    u32x2 wf[2][8];
#define RD(ks, n)                                                                                          \
  do {                                                                                                     \
    if ((n) >= 2 && (n) < 10) xf[ks][(n) - 2] = *(const u32x4*)(xr[ks] + (((n) - 2) & 3) * 4096 + (((n) - 2) >> 2) * 1024); \
    else { const int m_ = (n) < 2 ? (n) : (n) - 8; wf[ks][m_] = tr_read(wp[m_ >> 1] + (ks) * 4096 + (m_ & 1) * 512); } \
  } while (0)
#define WF(ks, i) ((u32x4){wf[ks][2 * (i)][0], wf[ks][2 * (i)][1], wf[ks][2 * (i) + 1][0], wf[ks][2 * (i) + 1][1]})
#define KSTEP(ks, READS, CSUM)                                                                             \
  do {                                                                                                     \
    _Pragma("unroll") for (int n_ = 0; n_ < 32; ++n_) {                                                    \
      acc[n_ >> 3][n_ & 7] = MFMA16_BF(WF(ks, n_ >> 3), xf[ks][n_ & 7], acc[n_ >> 3][n_ & 7]);             \
      if (READS && n_ < 16) RD((ks) ^ 1, n_);                                                              \
      if (CSUM && n_ >= 30) cs[n_ & 1] = MFMA16_BF(WF(ks, n_ & 1), ones, cs[n_ & 1]);                      \
      SCHED();                                                                                             \
    }                                                                                                      \
  } while (0)
    SET_BUF(0);
    TS_MARK();
    BARRIER();
    TS_MARK();
#pragma unroll
    for (int n = 0; n < 16; ++n) RD(0, n);
    SCHED();
    KSTEP(0, true, 1);
    int nxt = STAGE_B;
#pragma unroll 1
    for (int u = 0; u < 9; ++u) {
      BARRIER();
      SET_BUF(nxt);
      SCHED();
      KSTEP(1, true, 1);
      KSTEP(0, true, 1);
      nxt = nxt == 2 * STAGE_B ? 0 : nxt + STAGE_B;
    }
    TS_MARK();
#pragma unroll
    for (int i = 0; i < 2; ++i) {
      cs[i] = MFMA16_BF(WF(1, i), ones, cs[i]);
      if ((l & 15) == 0) *(f32x4*)(sm + G_CS + w * 128 + i * 64 + g * 16) = cs[i];
    }
    BARRIER();
    f32x4 co[2];
    f32x4 st[4];
#pragma unroll
    for (int i = 0; i < 2; ++i) co[i] = *(const f32x4*)(sm + G_CS + (w ^ 2) * 128 + i * 64 + g * 16);
#pragma unroll
    for (int r = 0; r < 4; ++r) st[r] = *(const f32x4*)(sm + G_ST + (wr * 128 + (r >> 1) * 64 + 8 * (l & 7) + 4 * (i16 >> 3) + (r & 1) * 2) * 8);
    SCHED();
    TS_MARK();
    const unsigned ao = WS_AF + ((panel * 4 + wr * 2) * 48 + ft * 2 + fh) * 4096 + l * 16;
#pragma unroll
    for (int r = 0; r < 10; ++r) {
      if (r < 8) {
#pragma unroll
        for (int i = 0; i < 4; ++i) acc[i][r] = MFMA16_BF(WF(1, i), xf[1][r], acc[i][r]);
      }
      if (r >= 2) {
        const int e = r - 2;
        const float mb = st[e >> 1][(e & 1) * 2], r2 = st[e >> 1][(e & 1) * 2 + 1];
        u32x4 o;
#pragma unroll
        for (int i = 0; i < 2; ++i) {
          const f32x4 hv = ((acc[i][e] - mb * cs[i]) * r2) * (acc[2 + i][e] - mb * co[i]);
          o[2 * i] = pk_bf(hv[0], hv[1]); o[2 * i + 1] = pk_bf(hv[2], hv[3]);
        }
        ST128(o, rw, ao + (e >> 2) * 48 * 4096 + (e & 3) * 1024);
      }
      SCHED();
    }
#undef SET_BUF
#undef RD
#undef WF
#undef KSTEP
    }
    SCHED();
    TS_MARK();
    WAIT_V(0);
    __builtin_amdgcn_s_barrier();
    asm volatile("" ::: "memory");
    TS_MARK();
    const u64 tag = next_epoch(*(const u64*)(sm + G_TAG));
    if (tid == 0) st_dev(ws + WS_FH + (panel * 32 + ft) * 8, tag);
    SCHED();
#pragma unroll
    for (int j = 0; j < 18; ++j)
#pragma unroll
      for (int i = 0; i < 2; ++i) B[j][i] = *(const u32x4*)(sm + j * 8192 + (2 * w + i) * 1024 + l * 16);
    SCHED();
    if (w == 0) wait_flags(ws + WS_FH + (xcd * 32 + (l < 24 ? l : 0)) * 8, tag);
    }
#pragma unroll
    for (int j = 0; j < NB; ++j) asm volatile("" : "+v"(B[j][0]), "+v"(B[j][1]));
    SCHED();
    TS_SET(7);
    BARRIER();
    asm volatile("" ::: "memory");
    TS_MARK();
    const char* asrc = ws + WS_AF + p3 * 48 * 4096 + w * 4096;
#define LOADA(ch)                                                                                                     \
  asm volatile("s_mov_b32 m0, %2\n\ts_nop 3\n\tglobal_load_lds_dwordx4 %0, %1 sc1\n\tglobal_load_lds_dwordx4 %0, %1 offset:1024 sc1\n\t" \
               "global_load_lds_dwordx4 %0, %1 offset:2048 sc1\n\tglobal_load_lds_dwordx4 %0, %1 offset:3072 sc1" ::"v"(l * 16), \
               "s"(asrc + (ch) * 32768), "s"(smb + ((ch) & 3) * 32768 + w * 4096) : "memory")
    LOADA(0); LOADA(1);
    const u64 tag = next_epoch(*(const u64*)(sm + G_TAG));
    f32x4 acc[4][2];
#pragma unroll
    for (int m = 0; m < 4; ++m) { acc[m][0] = (f32x4){0.f, 0.f, 0.f, 0.f}; acc[m][1] = acc[m][0]; }
    const LDS_AS char* ap[2];
#pragma unroll
    for (int i = 0; i < 2; ++i) { ap[i] = (const LDS_AS char*)sm + i * 65536 + kh * 4096 + l * 16; asm volatile("" : "+v"(ap[i])); }
    u32x4 A[3][4], V[11], xres[2];
    u32x2 mi[5];
    unsigned orow[2];
    u64 fv = 0;
    const unsigned vo = WS_VT + (c0 * 41 + 2 * g) * VP + (p3 >> 3) * 2048 + ((l >> 3) & 1) * 1024 + ((p3 & 7) * 8 + (l & 7)) * 16;
#define LOADV()                                                                                                       \
  do {                                                                                                                \
    _Pragma("unroll") for (int u = 0; u < 10; ++u) V[u] = LD128_DEV2(rw, vo, ((u >> 1) * 8 + (u & 1)) * VP);         \
    V[10] = LD128_DEV2(rw, vo - 2 * g * VP, 40 * VP);                                                                 \
  } while (0)
    bool early = false;
    WAIT_V(4);
    BARRIER();
    TS_MARK();
#define ZPART()                                                                                                       \
  do {                                                                                                                \
    if (g == 0) {                                                                                                     \
      _Pragma("unroll") for (int m = 0; m < 4; ++m) V[0][m] = (V[0][m] & 0xffffu) | (V[10][m] << 16);     \
    }                                                                                                                 \
    _Pragma("unroll") for (int ks = 0; ks < 5; ++ks)                                                                  \
      _Pragma("unroll") for (int m = 0; m < 4; ++m)                                                                   \
        acc[m][0] = MFMA16K16_BF(((u32x2){V[2 * ks][m], V[2 * ks + 1][m]}), mi[ks], acc[m][0]);                       \
  } while (0)
#pragma unroll
    for (int m = 0; m < 8; ++m) A[m >> 2][m & 3] = *(const LDS_AS u32x4*)(ap[0] + (m >> 2) * 8192 + (m & 3) * 1024);
    SCHED();
#pragma unroll
    for (int j = 0; j < 24; ++j) {
      if ((j & 3) == 2 && j < 22) {
        if (j == 2) WAIT_V(8); else if (j == 6) WAIT_V(14); else if (j == 10) WAIT_V(21); else if (j == 14) WAIT_V(7);
        BARRIER();
      }
#pragma unroll
      for (int n = 0; n < 8; ++n) {
        acc[n & 3][n >> 2] = MFMA16_BF(A[j % 3][n & 3], B[j % NB][n >> 2], acc[n & 3][n >> 2]);
        if (n < 4 && j < 22) A[(j + 2) % 3][n] = *(const LDS_AS u32x4*)(ap[((j + 2) >> 3) & 1] + ((j + 2) & 7) * 8192 + n * 1024);
        if (n == 4) {
          if (j < 3) LOADA(2 + j);
          if (j == 6) LOADA(5);
          if (j >= 3 && j < 6) LOADB(j + 15);
          if (j >= 7 && j < 10) LOADB(j + 14);
          if (j == 9) fv = ld_dev(ws + WS_FV + (l < 41 ? l : 0) * 8);
          if (j == 12) {
#pragma unroll
            for (int ks = 0; ks < 5; ++ks) mi[ks] = *(const u32x2*)&g_T80.i[ct][ks][l][0];
            const rsrc_t rx = mk_rsrc(x, 2048 * 640 * 2);
#pragma unroll
            for (int r = 0; r < 2; ++r) {
              orow[r] = s_of_rho(p3 * 64 + 8 * ((4 * g + 2 * kh + r) & 7) + 4 * (g >> 1) + (w & 3)) * 1280 + min(d1s + i16, 79) * 16;
              xres[r] = __builtin_amdgcn_raw_buffer_load_b128(rx, orow[r], 0, 0);
            }
          }
        }
        SCHED();
      }
      if (j == 14) {
        early = all_lanes(fv == tag);
        asm volatile("" ::: "memory");
        LOADV();
      }
      if (j == 21) {
        if (!early) {
          wait_flags(ws + WS_FV + (l < 41 ? l : 0) * 8, tag);
          LOADV();
        }
#pragma unroll
        for (int u = 0; u < 11; ++u) asm volatile("" : "+v"(V[u]));
        ZPART();
      }
      SCHED();
      if (j == 11) TS_MARK();
    }
#undef LOADA
#undef LOADB
    TS_MARK();
#undef LOADV
#undef ZPART
    char* tp = sm + 65536 + kh * 16384 + l * 8;
#pragma unroll
    for (int m = 0; m < 4; ++m)
#pragma unroll
      for (int i = 0; i < 2; ++i)
        *(u32x2*)(tp + (m * 8 + (c0 ^ i)) * 512) = (u32x2){pk_bf(acc[m][i][0], acc[m][i][1]), pk_bf(acc[m][i][2], acc[m][i][3])};
    BARRIER();
    TS_MARK();
    unsigned pp[2][8];
#pragma unroll
    for (int k = 0; k < 2; ++k)
#pragma unroll
      for (int c = 0; c < 8; ++c) pp[k][c] = *(const unsigned*)(sm + 65536 + k * 16384 + ((w & 3) * 8 + c) * 512 + l * 8 + kh * 4);
    SCHED();
    const rsrc_t ro = mk_rsrc(out, 2048 * 640 * 2);
#pragma unroll
    for (int r = 0; r < 2; ++r) {
      u32x4 o;
#pragma unroll
      for (int c = 0; c < 4; ++c) {
        const float a = r ? bf_hi(pp[0][2 * c]) + bf_hi(pp[1][2 * c]) : bf_lo(pp[0][2 * c]) + bf_lo(pp[1][2 * c]);
        const float b2 = r ? bf_hi(pp[0][2 * c + 1]) + bf_hi(pp[1][2 * c + 1]) : bf_lo(pp[0][2 * c + 1]) + bf_lo(pp[1][2 * c + 1]);
        o[c] = pk_bf(a + bf_lo(xres[r][c]), b2 + bf_hi(xres[r][c]));
      }
      if (i16 < nd1) ST128(o, ro, orow[r]);
    }
    TS_MARK();
    TS_END(0, 0);
    return;
  }
  const int aux = idx * 8 + xcd;
  u64 tag;
  f16x2 tc[8], tsn[8];
  u32x4 G1[8], G3[8];
  u32x4 wo_[3];
  u16x2 m2 = {0, 0};
  const u16* wl;
  unsigned wdst;
  int wst;
  {
    const int row = 4 * w + g, i16 = l & 15;
    dma1(l * 16, (const char*)&g_T80 + w * 2048, smb + A_T80 + w * 2048);
    dma1(l * 16, (const char*)&g_T80 + w * 2048 + 1024, smb + A_T80 + w * 2048 + 1024);
    const u32x4* xp = (const u32x4*)(x + s_of_rho(aux * 32 + row) * 640) + i16;
    u32x4 xv[5];
#pragma unroll
    for (int c = 0; c < 5; ++c) xv[c] = xp[c * 16];
    const u64 ep = ld_dev(ws + WS_EP);
    SCHED();
    {
      const float a0 = (float)(aux * w) * (1.f / 640.f), a1 = (float)(l & 31) * (1.f / 2048.f);
#pragma unroll
      for (int e = 0; e < 8; ++e) {
        const float a = a0 + a1 * (float)(2 * (e & 1) + 8 * (e >> 1) + 4 * (l >> 5)), a2 = a + a1;
        tc[e] = as_h2(pk_h(__builtin_amdgcn_cosf(a), __builtin_amdgcn_cosf(a2)));
        tsn[e] = as_h2(pk_h(__builtin_amdgcn_sinf(a), __builtin_amdgcn_sinf(a2)));
        asm volatile("" : "+v"(tc[e]), "+v"(tsn[e]));
      }
    }
    SCHED();
    WAIT_V(6);
    BARRIER();
    u32x4 fm[15];
#pragma unroll
    for (int u = 0; u < 15; ++u) fm[u] = *(const u32x4*)(sm + A_T80 + u * 1024 + l * 16);
    SCHED();
    TS_MARK();
    float f[40];
#pragma unroll
    for (int c = 0; c < 5; ++c)
#pragma unroll
      for (int j = 0; j < 4; ++j) { f[c * 8 + 2 * j] = bf_lo(xv[c][j]); f[c * 8 + 2 * j + 1] = bf_hi(xv[c][j]); }
    tag = next_epoch(ep);
    asm volatile("" ::"v"(tag), "v"(f[39]));
    SCHED();
    {
      const char* wsrc = (const char*)(w < 4 ? w_r : w_i) + (64 * (4 * (aux >> 3) + (w & 3)) + 8 * (aux & 7)) * 642;
#pragma unroll
      for (int i = 0; i < 5; ++i) DMAW(l * 16, wsrc + i * 1024, smb + A_W + w * 5152 + i * 1024);
      if (l == 0) DMAW(0, wsrc + 5120, smb + A_W + w * 5152 + 5120);
      dma1(l * 16, (const char*)&g_TF + w * 2048, smb + A_TF + w * 2048);
      dma1(l * 16, (const char*)&g_TF + w * 2048 + 1024, smb + A_TF + w * 2048 + 1024);
    }
    SCHED();
    float s4[4] = {0.f, 0.f, 0.f, 0.f};
#pragma unroll
    for (int e = 0; e < 40; ++e) s4[e & 3] += f[e];
    const float mean = sum16((s4[0] + s4[1]) + (s4[2] + s4[3])) * (1.f / 640.f);
    float q4[4] = {0.f, 0.f, 0.f, 0.f};
#pragma unroll
    for (int e = 0; e < 40; ++e) { f[e] -= mean; q4[e & 3] += f[e] * f[e]; }
    const float mb = rnd_bf(mean), vb = rnd_bf(sum16((q4[0] + q4[1]) + (q4[2] + q4[3])) * (1.f / 640.f));
    const float sd = rnd_bf(__builtin_amdgcn_sqrtf(rnd_bf(vb + 1e-5f)));
    const float rs = __builtin_amdgcn_rcpf(sd), off = (mean - mb) * rs;
    if (i16 == 0) st_dev(ws + WS_ST + (aux * 32 + row) * 8, (tag << 32) | (f2u(mb) >> 16) | f2u(sd));
#pragma unroll
    for (int c = 0; c < 5; ++c) {
      u32x4 oh;
#pragma unroll
      for (int j = 0; j < 4; ++j) oh[j] = pk_h(f[c * 8 + 2 * j] * rs + off, f[c * 8 + 2 * j + 1] * rs + off);
      *(u32x4*)(sm + A_XH + row * 1344 + (c * 16 + i16) * 16) = oh;
    }
    TS_MARK();
    {
      const int h = l >> 5, p = l & 3;
      const char* rd = sm + A_XH + (4 * w + 2 * (g & 1) + (p >> 1)) * 1344 + (8 * h + ((l & 15) >> 2)) * 16 + (p & 1) * 8;
      u32x4 ax[5];
#pragma unroll
      for (int ks = 0; ks < 5; ++ks) {
        const u32x2 lo = tr_read(rd + ks * 256), hi = tr_read(rd + ks * 256 + 64);
        ax[ks] = (u32x4){lo[0], lo[1], hi[0], hi[1]};
      }
      SCHED();
      const int ol = l & 31;
      char* ut = sm + A_UT + (w >> 1) * 10304 + (ol >> 1) * 256 + (w & 1) * 8;
      const int ub = (8 * h + (ol & 1)) ^ (ol & 14);
      f32x16 acc[3];
#pragma unroll
      for (int nt = 0; nt < 3; ++nt) {
#pragma unroll
        for (int e = 0; e < 16; ++e) acc[nt][e] = 0.f;
#pragma unroll
        for (int ks = 0; ks < 5; ++ks) acc[nt] = MFMA32_H(ax[ks], fm[nt * 5 + ks], acc[nt]);
      }
#pragma unroll
      for (int nt = 0; nt < 3; ++nt)
        if (nt < 2 || ol < 16) {
#pragma unroll
          for (int dl = 0; dl < 4; ++dl)
            *(u32x2*)(ut + nt * 4096 + ((ub ^ (2 * dl)) << 4)) = (u32x2){pk_h(acc[nt][dl], acc[nt][4 + dl]), pk_h(acc[nt][8 + dl], acc[nt][12 + dl])};
        }
    }
    WAIT_V(1);
    BARRIER();
    TS_MARK();
    {
      u32x4 uv[5];
#pragma unroll
      for (int i = 0; i < 5; ++i) {
        const int seg = i * 128 + (tid >> 2);
        uv[i] = *(const u32x4*)(sm + A_UT + (tid & 3) * 10304 + ((seg ^ ((seg >> 3) & 14)) << 4));
      }
      SCHED();
#pragma unroll
      for (int i = 0; i < 5; ++i) ST128(uv[i], rw, WS_UB + (i * 8 + w) * CP + aux * 1024 + l * 16);
    }
    SCHED();
    const int eq = l & 3, kb = (l >> 2) & 7, cl = 2 * (w & 3) + (l >> 5), half = w >> 2;
    int fr = 64 * (4 * (aux >> 3) + eq) + 8 * (aux & 7) + kb;
    if (half) fr = (2048 - fr) & 2047;
    wl = (const u16*)(sm + A_W) + eq * 2576 + kb * 321 + (half ? 80 - cl : cl);
    wdst = WS_WT + cl * CP + (half * 2048 + (fr >> 8) * 256 + (fr & 63) * 4 + ((fr >> 6) & 3)) * 16;
    wst = half ? -8 : 8;
#define WT_READ(i0)                                                                                                   \
  do {                                                                                                                \
    _Pragma("unroll") for (int i_ = 0; i_ < 3; ++i_)                                                                  \
      _Pragma("unroll") for (int j_ = 0; j_ < 4; ++j_)                                                                \
        wo_[i_][j_] = __builtin_bit_cast(unsigned, ((u16x2){(wl + wst * ((i0) + i_))[80 * j_], (wl + wst * ((i0) + i_))[10304 + 80 * j_]})); \
    _Pragma("unroll") for (int i_ = 0; i_ < 3; ++i_) {                                                                \
      _Pragma("unroll") for (int j_ = 0; j_ < 4; ++j_) m2 = __builtin_elementwise_max(m2, __builtin_bit_cast(u16x2, wo_[i_][j_] & 0x7fff7fffu)); \
      if (w >= 4) wo_[i_] = (u32x4){wo_[i_][3], wo_[i_][2], wo_[i_][1], wo_[i_][0]} ^ 0x80000000u;                    \
      asm volatile("" : "+v"(wo_[i_]));                                                                               \
    }                                                                                                                 \
  } while (0)
#define WT_STORE(i0)                                                                                                  \
  do {                                                                                                                \
    _Pragma("unroll") for (int i_ = 0; i_ < 3; ++i_)                                                                  \
      if ((i0) + i_ < 5 || ((w & 3) == 0 && l < 32)) ST128(wo_[i_], rw, wdst + ((i0) + i_) * 8 * CP);                 \
  } while (0)
    WT_READ(0);
    SCHED();
    TS_MARK();
    WAIT_V(0);
    __builtin_amdgcn_s_barrier();
    if (tid == 0) st_dev(ws + WS_F1 + aux * 8, tag);
    TS_MARK();
    WT_STORE(0);
    SCHED();
    WT_READ(3);
    WT_STORE(3);
    const unsigned mx = max64(max((unsigned)m2[0], (unsigned)m2[1]));
    if (l == 0) ((unsigned*)(sm + A_MX))[w] = mx;
    SCHED();
    TS_MARK();
  }
#define PUBLISH_F2()                                                                                                  \
  do {                                                                                                                \
    if (tid == 0) {                                                                                                   \
      const u32x4 m0_ = *(const u32x4*)(sm + A_MX), m1_ = *(const u32x4*)(sm + A_MX + 16);                            \
      const unsigned mx_ = max(max(max(m0_[0], m0_[1]), max(m0_[2], m0_[3])), max(max(m1_[0], m1_[1]), max(m1_[2], m1_[3]))); \
      st_dev(ws + WS_F2 + aux * 8, (tag & ~0xffffull) | mx_);                                                         \
    }                                                                                                                 \
  } while (0)
  WAIT_V(0);
  BARRIER();
  PUBLISH_F2();
  TS_MARK();
  if (aux < 41) {
    const int c1 = aux, c1s = c1 == 40 ? 0 : c1;
    const bool real_cls = c1s == 0;
    char* eb = sm + A_E;
    const u32x4 g8 = *(const u32x4*)&g_TF.g8[l][0][0];
#pragma unroll
    for (int i = 0; i < 8; ++i) { G1[i] = *(const u32x4*)(sm + A_TF + i * 1024 + l * 16); G3[i] = *(const u32x4*)(sm + A_TF + 8192 + i * 1024 + l * 16); }
    SCHED();
    wait_flags(ws + WS_F1 + l * 8, tag);
    TS_MARK();
    u32x4 ar[4], ai[4];
    {
      const unsigned uo = WS_UB + c1s * CP + (l >> 2) * 1024 + w * 128 + (l & 3) * 16;
#pragma unroll
      for (int i = 0; i < 4; ++i) {
        ar[i] = LD128_DEV(rw, uo + (c1 == 40 ? 64 : 0) + i * 16384);
        ai[i] = LD128_DEV(rw, uo + 64 + i * 16384);
      }
    }
    u64 f2 = ld_dev(ws + WS_F2 + l * 8);
#pragma unroll
    for (int i = 0; i < 8; ++i) asm volatile("" : "+v"(G1[i]), "+v"(G3[i]));
    SCHED();
    if (real_cls) {
#pragma unroll
      for (int i = 0; i < 4; ++i) ai[i] = (u32x4){0u, 0u, 0u, 0u};
    }
    char* erow = eb + (2 * w) * 4096 + ((l ^ e_key(w)) << 4);
    f32x16 zero;
#pragma unroll
    for (int e = 0; e < 16; ++e) zero[e] = 0.f;
    u32x2 wv[16];
    const unsigned wbase = WS_WT + c1 * CP + (g >> 1) * 32768 + (g & 1) * 8 + (l & 15) * 16 + ((4 * ((w >> 1) & 1) + (w & 1)) * 64 + 32 * (w >> 2)) * 64;
    bool early;
    {
      f32x16 dr[2], di[2];
#pragma unroll
      for (int t = 0; t < 2; ++t) {
        dr[t] = zero; di[t] = zero;
#pragma unroll
        for (int ks = 0; ks < 2; ++ks) {
          const u32x4 C = G1[(t * 2 + ks) * 2], S = G1[(t * 2 + ks) * 2 + 1];
          dr[t] = MFMA32_H(ar[t * 2 + ks], C, dr[t]);
          di[t] = MFMA32_H(ai[t * 2 + ks], C, di[t]);
          dr[t] = MFMA32_H(ai[t * 2 + ks], S, dr[t]);
          di[t] = MFMA32_H(neg_h(ar[t * 2 + ks]), S, di[t]);
        }
      }
      SCHED();
      TS_MARK();
      early = all_lanes((f2 >> 16) == (tag >> 16));
      asm volatile("" ::: "memory");
      if (early) {
#pragma unroll
        for (int i = 0; i < 16; ++i) wv[i] = LD64_DEV(rw, wbase + (i & 1) * 8192 + (i >> 1) * 256);
      }
      SCHED();
      u32x4 br[2][2], bi[2][2];
      bfly_tw<false>(dr, di, tc, tsn, br, bi);
#pragma unroll
      for (int t2 = 0; t2 < 2; ++t2) {
        f32x16 xr_ = zero, xi_ = zero;
#pragma unroll
        for (int ks = 0; ks < 2; ++ks) {
          const u32x4 C = G3[(t2 * 2 + ks) * 2], S = G3[(t2 * 2 + ks) * 2 + 1];
          xr_ = MFMA32_H(C, br[t2][ks], xr_);
          xi_ = MFMA32_H(C, bi[t2][ks], xi_);
          xr_ = MFMA32_H(S, bi[t2][ks], xr_);
          xi_ = MFMA32_H(S, neg_h(br[t2][ks]), xi_);
        }
        u32x4 o0[2], o1[2];
        pack_h(xr_, o0);
        pack_h(xi_, o1);
#pragma unroll
        for (int s2 = 0; s2 < 2; ++s2) {
          *(u32x4*)(erow + (t2 * 2 + s2) * 1024) = o0[s2];
          *(u32x4*)(erow + 4096 + (t2 * 2 + s2) * 1024) = swap_halves(o1[s2]);
        }
      }
    }
    SCHED();
    TS_MARK();
    if (!early) {
      f2 = wait_flags(ws + WS_F2 + l * 8, tag);
#pragma unroll
      for (int i = 0; i < 16; ++i) wv[i] = LD64_DEV(rw, wbase + (i & 1) * 8192 + (i >> 1) * 256);
    }
    float osc;
    u32x2 gi;
    {
      int ex = (int)(max64((unsigned)f2 & 0xffffu) >> 7);
      ex = ex < 8 ? 8 : (ex > 240 ? 240 : ex);
      const float up = u2f((unsigned)(250 - ex) << 23);
      gi = (u32x2){pk_bf(bf_lo(g8[2]) * up, bf_hi(g8[2]) * up), pk_bf(bf_lo(g8[3]) * up, bf_hi(g8[3]) * up)};
      osc = u2f((unsigned)(ex + 2) << 23) * ((real_cls ? 2.f : 4.f) * 256.f / (239.f * 640.f));
    }
    BARRIER();
    TS_MARK();
    {
      const int rr = 4 * g + ((l & 15) >> 2), wrw = l & 15;
      const int rdo = rr * 4096 + w * 512 + (((l & 3) ^ e_key(rr >> 1)) << 4) + (rr & 1) * 8;
      const int wro = wrw * 4096 + w * 512 + ((g ^ e_key(wrw >> 1)) << 4) + (wrw & 1) * 8;
      const u32x2 gf = {g8[0], g8[1]};
      const f32x4 z4 = {0.f, 0.f, 0.f, 0.f};
      u32x2 ev[16];
#pragma unroll
      for (int i = 0; i < 16; ++i) ev[i] = tr_read(eb + (rdo ^ (((i >> 1) & 3) * 64 + (i & 1) * 8)) + (i >> 3) * 256);
      SCHED();
      f32x4 xx[16], vv[16];
      xx[0] = MFMA16K16_H(gf, ev[0], z4);
      xx[1] = MFMA16K16_H(gf, ev[1], z4);
#pragma unroll
      for (int i = 0; i < 18; ++i) {
        if (i + 2 < 16) xx[i + 2] = MFMA16K16_H(gf, ev[i + 2], z4);
        if (i < 16) {
          const f32x4 xs = xx[i];
          const float r0 = bf_lo(wv[i][0]), i0 = bf_hi(wv[i][0]), r1 = bf_lo(wv[i][1]), i1 = bf_hi(wv[i][1]);
          const u32x2 y = {pk_bf(xs[0] * r0 - xs[1] * i0, xs[0] * i0 + xs[1] * r0), pk_bf(xs[2] * r1 - xs[3] * i1, xs[2] * i1 + xs[3] * r1)};
          vv[i] = MFMA16K16_BF(y, gi, z4);
        }
        if (i >= 2) {
          const int k = i - 2;
          *(u32x2*)(eb + (wro ^ (((k >> 1) & 3) * 64 + (k & 1) * 8)) + (k >> 3) * 256) = (u32x2){pk_h(vv[k][0], vv[k][1]), pk_h(vv[k][2], vv[k][3])};
        }
        SCHED();
      }
    }
    BARRIER();
    TS_MARK();
    {
      f32x16 dr[2], di[2];
      u32x4 yr[4], yi[4];
#pragma unroll
      for (int i = 0; i < 4; ++i) { yr[i] = *(const u32x4*)(erow + i * 1024); yi[i] = *(const u32x4*)(erow + 4096 + i * 1024); }
      SCHED();
#pragma unroll
      for (int t2 = 0; t2 < 2; ++t2) {
        dr[t2] = zero; di[t2] = zero;
#pragma unroll
        for (int s2 = 0; s2 < 2; ++s2) {
          const u32x4 pr_ = yr[t2 * 2 + s2], pi_ = swap_halves(yi[t2 * 2 + s2]);
          const u32x4 C = G1[(t2 * 2 + s2) * 2], S = G1[(t2 * 2 + s2) * 2 + 1];
          dr[t2] = MFMA32_H(pr_, C, dr[t2]);
          di[t2] = MFMA32_H(pi_, C, di[t2]);
          dr[t2] = MFMA32_H(neg_h(pi_), S, dr[t2]);
          di[t2] = MFMA32_H(pr_, S, di[t2]);
        }
      }
      u32x4 qr[2][2], qi[2][2];
      bfly_tw<true>(dr, di, tc, tsn, qr, qi);
      const unsigned vo = WS_VT + (w * 41 + c1) * VP + l * 16;
#pragma unroll
      for (int t = 0; t < 2; ++t) {
        f32x16 zr = zero, zq = zero;
#pragma unroll
        for (int ks = 0; ks < 2; ++ks) {
          const u32x4 C = G3[(t * 2 + ks) * 2], S = G3[(t * 2 + ks) * 2 + 1];
          zr = MFMA32_H(C, qr[t][ks], zr);
          zq = MFMA32_H(C, qi[t][ks], zq);
          zr = MFMA32_H(S, neg_h(qi[t][ks]), zr);
          zq = MFMA32_H(S, qr[t][ks], zq);
        }
#pragma unroll
        for (int u = 0; u < 4; ++u) {
          u32x4 o;
#pragma unroll
          for (int j = 0; j < 4; ++j) o[j] = pk_bf(zr[4 * u + j] * osc, zq[4 * u + j] * osc);
          ST128(o, rw, vo + (t * 4 + u) * 1024);
        }
      }
    }
    WAIT_V(0);
    __builtin_amdgcn_s_barrier();
    if (tid == 0) st_dev(ws + WS_FV + c1 * 8, tag);
    TS_END(0, 1);
    return;
  }
  if (aux == 63 && w == 0) {
    wait_flags(ws + WS_F1 + l * 8, tag);
#pragma unroll 1
    for (int i = 0; i < 3; ++i) wait_flags(ws + WS_FA + i * 1024 + l * 16, tag);
    if (l == 0) st_dev(ws + WS_EP, tag);
  }
  TS_END(0, 2);
}

#if defined(__HIP_DEVICE_COMPILE__)
#pragma clang attribute pop
#endif

__global__ void kfirst(char* ws) { *(volatile u64*)(ws + (32u << 20)) = __builtin_amdgcn_s_memrealtime(); }
__global__ void klast(char* ws) { const u64 t0 = *(volatile u64*)(ws + (32u << 20)); for (int i = 0; i < 100000 && __builtin_amdgcn_s_memrealtime() - t0 < 4000; ++i) __builtin_amdgcn_s_sleep(1); }
extern "C" void kernel_launch(void* const* d_in, const int* in_sizes, int n_in, void* d_out, int out_size, void* d_ws, size_t ws_size,
                              hipStream_t stream) {
  const u16* x = (const u16*)d_in[0];
  const u16* w_r = (const u16*)d_in[1];
  const u16* w_i = (const u16*)d_in[2];
  const u16* wg = (const u16*)d_in[4];
  const u16* wu = (const u16*)d_in[5];
  const u16* wd = (const u16*)d_in[6];
  char* ws = (char*)d_ws;
  ka<<<256, 512, 0, stream>>>(x, w_r, w_i, wg, wu, wd, ws, (u16*)d_out);
  klast<<<1, 64, 0, stream>>>(ws);
}
```

```cpp
#include <hip/hip_runtime.h>
#include <hip/hip_bf16.h>

typedef unsigned short u16;
typedef __attribute__((ext_vector_type(8))) __bf16 bf16x8;
typedef __attribute__((ext_vector_type(2))) __bf16 bf16x2;
typedef __attribute__((ext_vector_type(8))) _Float16 f16x8;
typedef __attribute__((ext_vector_type(4))) _Float16 f16x4;
typedef __attribute__((ext_vector_type(2))) _Float16 f16x2;
typedef __attribute__((ext_vector_type(4))) short s16x4;
typedef __attribute__((ext_vector_type(2))) float f32x2;
typedef __attribute__((ext_vector_type(4))) float f32x4;
typedef __attribute__((ext_vector_type(16))) float f32x16;
typedef __attribute__((ext_vector_type(4))) unsigned u32x4;
typedef __attribute__((ext_vector_type(2))) unsigned u32x2;
typedef __attribute__((ext_vector_type(2))) u16 u16x2;
typedef __amdgpu_buffer_rsrc_t rsrc_t;
#define DI __device__ __forceinline__
#define LDS_AS __attribute__((address_space(3)))

namespace tb {
constexpr double kPi = 3.14159265358979323846264338327950288;
constexpr double sinT(double x) { double x2 = x * x, t = x, s = x; for (int i = 1; i < 12; ++i) { t *= -x2 / ((2 * i) * (2 * i + 1)); s += t; } return s; }
constexpr double cosT(double x) { double x2 = x * x, t = 1, s = 1; for (int i = 1; i < 12; ++i) { t *= -x2 / ((2 * i - 1) * (2 * i)); s += t; } return s; }
constexpr double cos2pi(int q, int den) {
  q %= den; if (2 * q > den) q = den - q;
  double sg = 1; if (4 * q > den) { q = den / 2 - q; sg = -1; }
  if (8 * q > den) return sg * sinT(2 * kPi * (den / 4 - q) / den);
  return sg * cosT(2 * kPi * q / den);
}
constexpr u16 bfbits(double d) { unsigned u = __builtin_bit_cast(unsigned, (float)d); u += 0x7fffu + ((u >> 16) & 1u); return (u16)(u >> 16); }
constexpr u16 hbits(double d) { return __builtin_bit_cast(u16, (_Float16)d); }
constexpr int krow(int s, int h, int j) { return 16 * s + 8 * (j >> 2) + 4 * h + (j & 3); }
template <int N> struct CT { double c[N]; };
template <int N> constexpr CT<N> mkC() { CT<N> c{}; for (int i = 0; i < N; ++i) c.c[i] = cos2pi(i, N); return c; }
constexpr CT<64> c64 = mkC<64>();
constexpr CT<80> c80 = mkC<80>();
constexpr double m80(int o, int d) { return o == 1 ? c80.c[(40 * d) % 80] : c80.c[((o >> 1) * d + ((o & 1) ? 20 : 0)) % 80]; }
struct alignas(16) TabF {
  u16 g1[2][2][2][64][8];
  u16 g3[2][2][2][64][8];
  u16 g8[64][2][4];
};
constexpr TabF mkTabF() {
  TabF T{};
  for (int t = 0; t < 2; ++t) for (int ks = 0; ks < 2; ++ks) for (int l = 0; l < 64; ++l) for (int j = 0; j < 8; ++j) {
    const int r = l & 31, k = krow(ks, l >> 5, j);
    const int q1 = (r * (2 * k + t)) & 63, q3 = (k * (2 * r + t)) & 63;
    T.g1[t][ks][0][l][j] = hbits(c64.c[q1] * 0.25); T.g1[t][ks][1][l][j] = hbits(c64.c[(q1 + 48) & 63] * 0.25);
    T.g3[t][ks][0][l][j] = hbits(c64.c[q3] * 0.25); T.g3[t][ks][1][l][j] = hbits(c64.c[(q3 + 48) & 63] * 0.25);
  }
  for (int l = 0; l < 64; ++l) for (int e = 0; e < 4; ++e) {
    const int m = l & 15, k = 4 * (l >> 4) + e, q = (8 * (m >> 1) * (k >> 1)) & 63;
    const double C = c64.c[q], S = c64.c[(q + 48) & 63];
    T.g8[l][0][e] = hbits(0.25 * ((m & 1) == (k & 1) ? C : ((m & 1) ? -S : S)));
    T.g8[l][1][e] = bfbits(239. / 256. * ((m & 1) == (k & 1) ? C : ((k & 1) ? -S : S)));
  }
  return T;
}
struct alignas(16) Tab80 {
  u16 f[3][5][64][8];
  u16 pad[512];
  u16 i[6][5][64][4];
};
constexpr Tab80 mkTab80() {
  Tab80 T{};
  for (int n = 0; n < 3; ++n) for (int ks = 0; ks < 5; ++ks) for (int l = 0; l < 64; ++l) for (int j = 0; j < 8; ++j)
    T.f[n][ks][l][j] = 32 * n + (l & 31) < 80 ? hbits(m80(32 * n + (l & 31), 16 * ks + 8 * (l >> 5) + j)) : (u16)0;
  for (int n = 0; n < 6; ++n) for (int ks = 0; ks < 5; ++ks) for (int l = 0; l < 64; ++l) for (int e = 0; e < 4; ++e)
    T.i[n][ks][l][e] = bfbits(m80(16 * ks + 4 * (l >> 4) + e, 13 * n + (n < 2 ? n : 2) + (l & 15)));
  return T;
}
}
static __device__ const tb::TabF g_TF = tb::mkTabF();
static __device__ const tb::Tab80 g_T80 = tb::mkTab80();

constexpr unsigned WS_EP = 0;
constexpr unsigned WS_F1 = 256;
constexpr unsigned WS_F2 = WS_F1 + 512;
constexpr unsigned WS_FV = WS_F2 + 512;
constexpr unsigned WS_FH = WS_FV + 512;
constexpr unsigned WS_FA = 4096;
constexpr unsigned WS_ST = 8192;
constexpr unsigned CP = 65536 + 256;
constexpr unsigned WS_UB = WS_ST + 2048 * 8;
constexpr unsigned WS_WT = WS_UB + 40 * CP;
constexpr unsigned WS_AF = WS_WT + 41 * CP;
constexpr unsigned WS_BF = WS_AF + 32 * 48 * 4096;
constexpr unsigned VP = 8192 + 256;
constexpr unsigned WS_VT = WS_BF + 6 * 48 * 7168;
constexpr unsigned WS_END = WS_VT + 328 * VP;

#if defined(__HIP_DEVICE_COMPILE__)
#pragma clang attribute push(__attribute__((target("no-packed-fp32-ops"))), apply_to = function)
#endif
DI float u2f(unsigned u) { return __builtin_bit_cast(float, u); }
DI unsigned f2u(float f) { return __builtin_bit_cast(unsigned, f); }
DI bool all_lanes(bool p) { return __builtin_amdgcn_ballot_w64(!p) == 0; }
DI int s_of_rho(int rho) {
  const int t = rho >> 10, ks = (rho >> 9) & 1, h = (rho >> 8) & 1, r = (rho >> 3) & 31, jh = (rho >> 2) & 1, jl = rho & 3;
  return 64 * (16 * ks + 8 * jh + 4 * h + jl) + 32 * t + r;
}
DI float bf_lo(unsigned u) { return u2f(u << 16); }
DI float bf_hi(unsigned u) { return u2f(u & 0xffff0000u); }
DI unsigned pk_bf(float a, float b) { f32x2 f = {a, b}; return __builtin_bit_cast(unsigned, __builtin_convertvector(f, bf16x2)); }
DI unsigned pk_h(float a, float b) { f32x2 f = {a, b}; return __builtin_bit_cast(unsigned, __builtin_convertvector(f, f16x2)); }
DI float rnd_bf(float a) { return bf_lo(pk_bf(a, 0.f)); }
#define MFMA16_BF(a, b, c) __builtin_amdgcn_mfma_f32_16x16x32_bf16(__builtin_bit_cast(bf16x8, a), __builtin_bit_cast(bf16x8, b), c, 0, 0, 0)
#define MFMA32_BF(a, b, c) __builtin_amdgcn_mfma_f32_32x32x16_bf16(__builtin_bit_cast(bf16x8, a), __builtin_bit_cast(bf16x8, b), c, 0, 0, 0)
#define MFMA32_H(a, b, c) __builtin_amdgcn_mfma_f32_32x32x16_f16(__builtin_bit_cast(f16x8, a), __builtin_bit_cast(f16x8, b), c, 0, 0, 0)
#define MFMA16K16_H(a, b, c) __builtin_amdgcn_mfma_f32_16x16x16f16(__builtin_bit_cast(f16x4, a), __builtin_bit_cast(f16x4, b), c, 0, 0, 0)
#define MFMA16K16_BF(a, b, c) __builtin_amdgcn_mfma_f32_16x16x16bf16_1k(__builtin_bit_cast(s16x4, a), __builtin_bit_cast(s16x4, b), c, 0, 0, 0)
DI u32x2 tr_read(const char* p) {
  s16x4 v = __builtin_amdgcn_ds_read_tr16_b64_v4i16((LDS_AS s16x4*)(p));
  return __builtin_bit_cast(u32x2, v);
}
DI rsrc_t mk_rsrc(const void* p, unsigned bytes) { return __builtin_amdgcn_make_buffer_rsrc((void*)p, 0, bytes, 0x00020000); }
#define ST128(v, r, off) __builtin_amdgcn_raw_buffer_store_b128(v, r, off, 0, 16)
#define ST64(v, r, off) __builtin_amdgcn_raw_buffer_store_b64(v, r, off, 0, 16)
#define WAIT_V(n) asm volatile("s_waitcnt vmcnt(%0)" ::"n"(n) : "memory")
#define WAIT_L(n) asm volatile("s_waitcnt lgkmcnt(%0)" ::"n"(n) : "memory")
#define SCHED() __builtin_amdgcn_sched_barrier(0)
#define BARRIER() do { WAIT_L(0); __builtin_amdgcn_s_barrier(); asm volatile("" ::: "memory"); } while (0)
DI void dma1(unsigned voff, const void* sbase, unsigned lds) {
  asm volatile("s_mov_b32 m0, %2\n\ts_nop 3\n\tglobal_load_lds_dwordx4 %0, %1" ::"v"(voff), "s"(sbase), "s"(lds) : "memory");
}
template <int CTRL> DI unsigned dpp_u(unsigned x) { return (unsigned)__builtin_amdgcn_mov_dpp((int)x, CTRL, 0xf, 0xf, true); }
template <int CTRL> DI float dpp_f(float x) { return u2f(dpp_u<CTRL>(f2u(x))); }
DI float sum16(float x) {
  x += dpp_f<0xB1>(x); x += dpp_f<0x4E>(x); x += dpp_f<0x141>(x); x += dpp_f<0x128>(x);
  return x;
}
DI float sum_rows(float x) {
  const auto s = __builtin_amdgcn_permlane16_swap(f2u(x), f2u(x), false, false);
  x = u2f(s[0]) + u2f(s[1]);
  const auto t = __builtin_amdgcn_permlane32_swap(f2u(x), f2u(x), false, false);
  return u2f(t[0]) + u2f(t[1]);
}
typedef unsigned long long u64;
typedef __attribute__((address_space(1))) u64 gu64;
#define RLX_AGENT __ATOMIC_RELAXED, __HIP_MEMORY_SCOPE_AGENT
DI u64 ld_dev(const char* p) { return __hip_atomic_load((gu64*)p, RLX_AGENT); }
DI void st_dev(char* p, u64 v) { __hip_atomic_store((gu64*)p, v, RLX_AGENT); }
DI u64 next_epoch(u64 e) { return e * 0x9E3779B97F4A7C15ull + 0x632BE59BD9B4E019ull; }
#define SLP 1
constexpr unsigned SPIN_CAP = 1u << 18;
DI u64 wait_flags(const char* p, u64 tag) {
  u64 v = 0;
#pragma unroll 1
  for (unsigned n = 0; n < SPIN_CAP; ++n) {
    v = ld_dev(p);
    if (all_lanes((v >> 16) == (tag >> 16))) break;
    __builtin_amdgcn_s_sleep(SLP);
  }
  asm volatile("" ::: "memory");
  return v;
}
#define LD128_DEV(r, off) __builtin_amdgcn_raw_buffer_load_b128(r, off, 0, 16)
#define LD128_DEV2(r, off, soff) __builtin_amdgcn_raw_buffer_load_b128(r, off, soff, 16)
#define LD64_DEV(r, off) __builtin_amdgcn_raw_buffer_load_b64(r, off, 0, 16)
DI unsigned max64(unsigned x) {
  x = max(x, dpp_u<0xB1>(x)); x = max(x, dpp_u<0x4E>(x)); x = max(x, dpp_u<0x141>(x)); x = max(x, dpp_u<0x128>(x));
  const auto s = __builtin_amdgcn_permlane16_swap(x, x, false, false);
  x = max(s[0], s[1]);
  const auto t = __builtin_amdgcn_permlane32_swap(x, x, false, false);
  return max(t[0], t[1]);
}
DI unsigned lds_addr(const char* p) { return (unsigned)(size_t)(LDS_AS const char*)p; }

#define TS_DECL
#define TS_SET(n)
#define TS_MARK()
#define TS_END(k, role)

#define ZJ 22
#define OUTAUX 16
#define N5 3
#define N8 2
#define NPIN 8
#define CPF 290
#define PFD 0
constexpr int STAGE_B = 49152;
constexpr int G_CS = 3 * STAGE_B;
constexpr int G_ST = G_CS + 512;
constexpr int G_PF = G_ST + 2048;
constexpr int G_TAG = G_PF + 1024;
constexpr int G_SN = G_TAG + 1024;
constexpr int A_TF = 0;
constexpr int A_T80 = 16384;
constexpr int A_XH = 32768;
constexpr int A_UT = A_XH + 32 * 1344;
constexpr int A_W = A_UT + 4 * 10304;
constexpr int A_MX = A_W + 8 * 5152;
constexpr int A_E = 16384;
constexpr int KA_LDS = A_MX + 64;
static_assert(G_SN + 6144 <= KA_LDS && KA_LDS <= 163840, "lds");

DI void pack_h(const f32x16& v, u32x4 (&o)[2]) {
#pragma unroll
  for (int s = 0; s < 2; ++s)
#pragma unroll
    for (int j = 0; j < 4; ++j) o[s][j] = pk_h(v[8 * s + 2 * j], v[8 * s + 2 * j + 1]);
}
DI f16x2 as_h2(unsigned u) { return __builtin_bit_cast(f16x2, u); }
DI unsigned as_u(f16x2 v) { return __builtin_bit_cast(unsigned, v); }
template <bool INV> DI void bfly_tw(const f32x16 (&dr)[2], const f32x16 (&di)[2], const f16x2 (&tc)[8], const f16x2 (&ts)[8],
                                    u32x4 (&er)[2][2], u32x4 (&ei)[2][2]) {
#pragma unroll
  for (int e = 0; e < 8; ++e) {
    const f16x2 r0 = as_h2(pk_h(dr[0][2 * e], dr[0][2 * e + 1])), r1 = as_h2(pk_h(dr[1][2 * e], dr[1][2 * e + 1]));
    const f16x2 i0 = as_h2(pk_h(di[0][2 * e], di[0][2 * e + 1])), i1 = as_h2(pk_h(di[1][2 * e], di[1][2 * e + 1]));
#pragma unroll
    for (int t = 0; t < 2; ++t) {
      const f16x2 sr = t ? r0 - r1 : r0 + r1, si = t ? i0 - i1 : i0 + i1;
      const f16x2 a = sr * tc[e], c = si * tc[e];
      er[t][e >> 2][e & 3] = as_u(__builtin_elementwise_fma(INV ? -si : si, ts[e], a));
      ei[t][e >> 2][e & 3] = as_u(__builtin_elementwise_fma(INV ? sr : -sr, ts[e], c));
    }
  }
}
DI u32x4 neg_h(u32x4 v) { return v ^ 0x80008000u; }
DI u32x4 swap_halves(u32x4 v) { return (u32x4){v[2], v[3], v[0], v[1]}; }
DI int e_key(int d2) { return ((d2 & 2) << 2) | ((d2 & 1) << 2) | ((d2 & 4) >> 1) | ((d2 & 2) >> 1); }

__global__ __launch_bounds__(512, 2) void ka(const u16* __restrict__ x, const u16* __restrict__ w_r, const u16* __restrict__ w_i,
                                             const u16* __restrict__ wg, const u16* __restrict__ wu, const u16* __restrict__ wd,
                                             char* __restrict__ ws, u16* __restrict__ out) {
  __shared__ __attribute__((aligned(1024))) char sm[KA_LDS];
  u64 pc0;
  asm volatile("s_getpc_b64 %0" : "=s"(pc0));
  pc0 &= ~255ull;
  const int b = blockIdx.x, tid = threadIdx.x, l = tid & 63;
  const int w = __builtin_amdgcn_readfirstlane(tid >> 6);
  const int xcd = b & 7, idx = b >> 3;
  const unsigned smb = lds_addr(sm);
  const rsrc_t rw = mk_rsrc(ws, WS_END);
  const int g = l >> 4;
  TS_DECL
  if (idx >= 8) {
    const int gi = idx - 8;
    const int panel = (xcd >> 2) * 4 + (gi & 3), ft = (xcd & 3) * 6 + (gi >> 2);
    const int gwg = gi * 8 + xcd;
    const int p3 = xcd * 4 + (gi & 3), ct = gi >> 2, d1s = 13 * ct + (ct < 2 ? ct : 2), nd1 = ct < 2 ? 14 : 13;
    const int i16 = l & 15, kh = w >> 2, c0 = 2 * (w & 3) + kh;
    constexpr int NB = 18;
    u32x4 B[NB][2];
    const unsigned bl = i16 < 12 ? g * 192 + i16 * 16 : 768 + g * 32 + (i16 & 1) * 16;
    const unsigned bo = WS_BF + (ct * 48 + kh) * 7168 + bl;
#define LOADB(j) do { B[(j) % NB][0] = LD128_DEV2(rw, bo + c0 * 896, (j) * 14336); B[(j) % NB][1] = LD128_DEV2(rw, bo + (c0 ^ 1) * 896, (j) * 14336); } while (0)
    if (w >= 4) {
      const int lw = w - 4, r8 = l >> 3, c8 = l & 7;
      asm volatile("s_mov_b32 m0, %2\n\ts_nop 3\n\tglobal_load_lds_dword %0, %1 sc1" ::"v"(WS_EP + (l & 1) * 4), "s"(ws), "s"(smb + G_TAG + lw * 256) : "memory");
      unsigned xo[2], woff[2];
#pragma unroll
      for (int v = 0; v < 2; ++v) {
        xo[v] = (s_of_rho(panel * 256 + 2 * lw + v) + r8) * 1280 + ((c8 ^ ((r8 >> 1) | ((lw >> 1) << 2))) << 4);
        woff[v] = ((16 * lw + 8 * v + r8) * 1536 + ft * 64) * 2 + ((c8 ^ ((((r8 >> 1) & 1) | (v << 1)) << 1)) << 4);
      }
      const char *xs = (const char*)x, *gs = (const char*)wg, *us = (const char*)wu;
      const unsigned dx = smb + lw * 8192, dw = smb + 32768 + lw * 2048;
#define STAGE(off)                                                                                   \
  do {                                                                                               \
    _Pragma("unroll") for (int v_ = 0; v_ < 2; ++v_) {                                               \
      dma1(woff[v_], gs, dw + (off) + v_ * 1024);                                                    \
      dma1(woff[v_], us, dw + (off) + 8192 + v_ * 1024);                                             \
    }                                                                                                \
    _Pragma("unroll") for (int i_ = 0; i_ < 8; ++i_) dma1(xo[i_ >> 2], xs + (i_ & 3) * 10240, dx + (off) + i_ * 1024); \
    xs += 128; gs += 64 * 3072; us += 64 * 3072;                                                     \
  } while (0)
      STAGE(0);
      STAGE(STAGE_B);
      WAIT_V(12);
      __builtin_amdgcn_s_barrier();
      const u64 tag = next_epoch(*(const u64*)(sm + G_TAG + lw * 256));
      const unsigned fao = WS_FA + ((ct * 48 + (l < 48 ? l : 0)) % 192) * 16;
      const int job = gwg + 192 * lw;
      if (job < 288) {
        const int jc = job / 48, F = job - jc * 48, lg = l < 48 ? l / 12 : (l >> 1) & 3;
        const int d1 = min(13 * jc + (jc < 2 ? jc : 2) + (l < 48 ? l - lg * 12 : 12 + (l & 1)), 79);
        const unsigned d0 = (32 * F + 4 * lg) * 1280 + d1 * 16, d16 = d0 + 16 * 1280;
        u32x4 dv[8];
        asm volatile("global_load_dwordx4 %0, %8, %10\n\tglobal_load_dwordx4 %1, %8, %10 offset:1280\n\tglobal_load_dwordx4 %2, %8, %10 offset:2560\n\t"
                     "global_load_dwordx4 %3, %8, %10 offset:3840\n\tglobal_load_dwordx4 %4, %9, %10\n\tglobal_load_dwordx4 %5, %9, %10 offset:1280\n\t"
                     "global_load_dwordx4 %6, %9, %10 offset:2560\n\tglobal_load_dwordx4 %7, %9, %10 offset:3840"
                     : "=&v"(dv[0]), "=&v"(dv[1]), "=&v"(dv[2]), "=&v"(dv[3]), "=&v"(dv[4]), "=&v"(dv[5]), "=&v"(dv[6]), "=&v"(dv[7])
                     : "v"(d0), "v"(d16), "s"(wd) : "memory");
        STAGE(2 * STAGE_B);
        WAIT_V(20);
        __builtin_amdgcn_s_barrier();
        STAGE(0);
        asm volatile("s_waitcnt vmcnt(12)" : "+v"(dv[0]), "+v"(dv[1]), "+v"(dv[2]), "+v"(dv[3]), "+v"(dv[4]), "+v"(dv[5]), "+v"(dv[6]), "+v"(dv[7])::"memory");
        if (l < 56) {
#pragma unroll
          for (int c = 0; c < 8; ++c) {
            u32x4 o;
#pragma unroll
            for (int j = 0; j < 4; ++j) o[j] = __builtin_amdgcn_perm(dv[2 * j + 1][c >> 1], dv[2 * j][c >> 1], (c & 1) ? 0x07060302u : 0x05040100u);
            ST128(o, rw, WS_BF + job * 7168 + c * 896 + l * 16);
          }
        }
      } else {
        STAGE(2 * STAGE_B);
        WAIT_V(12);
        __builtin_amdgcn_s_barrier();
        STAGE(0);
        WAIT_V(12);
      }
      __builtin_amdgcn_s_barrier();
      STAGE(STAGE_B);
      WAIT_V(12);
      __builtin_amdgcn_s_barrier();
      if (tid == 256) st_dev(ws + WS_FA + gwg * 16, tag);
      STAGE(2 * STAGE_B);
      WAIT_V(12);
      __builtin_amdgcn_s_barrier();
      STAGE(0);
      WAIT_V(12);
      __builtin_amdgcn_s_barrier();
      asm volatile("s_mov_b32 m0, %2\n\ts_nop 3\n\tglobal_load_lds_dwordx4 %0, %1 sc1" ::"v"(fao), "s"(ws), "s"(smb + G_SN + lw * 1536) : "memory");
      if (l < 32)
        asm volatile("s_mov_b32 m0, %2\n\ts_nop 3\n\tglobal_load_lds_dwordx4 %0, %1 sc1" ::"v"(WS_ST + (panel * 256 + lw * 64) * 8 + l * 16), "s"(ws),
                     "s"(smb + G_SN + lw * 1536 + 1024) : "memory");
      STAGE(STAGE_B);
      WAIT_V(12);
      __builtin_amdgcn_s_barrier();
      STAGE(2 * STAGE_B);
      {
        u64 st = *(const u64*)(sm + G_SN + lw * 1536 + 1024 + l * 8);
        const u64 fa = *(const u64*)(sm + G_SN + lw * 1536 + l * 16);
        if (!all_lanes(fa == tag && (unsigned)(st >> 32) == (unsigned)tag)) {
          wait_flags(ws + fao, tag);
#pragma unroll 1
          for (unsigned n = 0; n < SPIN_CAP; ++n) {
            st = ld_dev(ws + WS_ST + (panel * 256 + tid - 256) * 8);
            if (all_lanes((unsigned)(st >> 32) == (unsigned)tag)) break;
            __builtin_amdgcn_s_sleep(4);
          }
        }
        asm volatile("" ::: "memory");
        const float rs = __builtin_amdgcn_rcpf(bf_hi((unsigned)st));
        *(f32x2*)(sm + G_ST + (tid - 256) * 8) = (f32x2){u2f((((unsigned)st & 0xffffu) ^ 0x8000u) * 0x10001u), rs * rs};
      }
#pragma unroll
      for (int j = 0; j < N5; ++j) LOADB(j);
      SCHED();
      WAIT_V(12 + 2 * N5);
      BARRIER();
      STAGE(0);
      SCHED();
#pragma unroll
      for (int j = N5; j < N5 + 3; ++j) LOADB(j);
      SCHED();
      WAIT_V(18 + 2 * N5);
#define JB (N5 + 3)
#undef STAGE
      __builtin_amdgcn_s_barrier();
      const char* bsrc = ws + WS_BF + ct * 48 * 7168 + 2 * lw * 896;
#define STAGEB(j0)                                                                                                    \
  do {                                                                                                                \
    _Pragma("unroll") for (int i_ = (j0); i_ < (j0) + 6; ++i_)                                                        \
      asm volatile("s_mov_b32 m0, %3\n\ts_nop 0\n\tglobal_load_lds_dwordx4 %0, %2 sc1\n\tglobal_load_lds_dwordx4 %1, %2 offset:1024 sc1" \
                   ::"v"(bl + 128), "v"(bl), "s"(bsrc - 128 + i_ * 14336), "s"(smb + i_ * 8192 + lw * 2048) : "memory"); \
  } while (0)
      STAGEB(6);
      LOADB(JB); LOADB(JB + 1); LOADB(JB + 2);
      SCHED();
      WAIT_V(24);
      __builtin_amdgcn_s_barrier();
      STAGEB(12);
#pragma unroll
      for (int j = JB + 3; j < JB + 3 + N8; ++j) LOADB(j);
      SCHED();
      __builtin_amdgcn_s_barrier();
      STAGEB(0);
#undef STAGEB
      WAIT_V(0);
      __builtin_amdgcn_s_barrier();
#pragma unroll
      for (int j = JB + 3 + N8; j < 18; ++j) LOADB(j);
    } else {
    if (w < 2) {
      __builtin_amdgcn_s_sleep(PFD);
      const int n = tid * 8 + panel;
      asm volatile("s_mov_b32 m0, %2\n\ts_nop 3\n\tglobal_load_lds_dword %0, %1" ::"v"((unsigned)((128 + (n >> 1)) * 3072 + ft * 128)),
                   "s"((panel & 1) ? wu : wg), "s"(smb + G_PF + w * 256) : "memory");
    }
    if (w == 3 && gi < 5) {
      asm volatile("s_mov_b32 m0, %2\n\ts_nop 3\n\tglobal_load_lds_dword %0, %1" ::"v"(min(gi * 64 + l, CPF - 1) * 128), "s"(pc0),
                   "s"(smb + G_PF + 768) : "memory");
    }
    const int wr = w >> 1, fh = w & 1;
    {
    const int q = (l & 15) >> 2;
    const int xb0 = (i16 >> 3) * 16384 + (wr * 16 + (l & 7)) * 128 + ((g ^ (((l & 7) >> 1) | ((i16 >> 3) << 2))) << 4);
    const int wb0 = (32768 + (8 * g + q) * 128 + (((fh * 2) ^ (((q >> 1) & 1) | ((g & 1) << 1))) << 5) + (l & 3) * 8) ^ (wr << 13);
    const char* xr[2];
    const char* wp[4];
#define SET_BUF(off)                                                                                       \
  do {                                                                                                     \
    xr[0] = sm + (off) + xb0; xr[1] = sm + (off) + (xb0 ^ 64);                                             \
    _Pragma("unroll") for (int i_ = 0; i_ < 4; ++i_) wp[i_] = sm + (off) + (wb0 ^ ((i_ & 1) << 5) ^ ((i_ >> 1) << 13)); \
  } while (0)
    f32x4 acc[4][8];
#pragma unroll
    for (int i = 0; i < 4; ++i)
#pragma unroll
      for (int r = 0; r < 8; ++r) acc[i][r] = (f32x4){0.f, 0.f, 0.f, 0.f};
    f32x4 cs[2] = {{0.f, 0.f, 0.f, 0.f}, {0.f, 0.f, 0.f, 0.f}};
    u32x4 ones = {0x3f803f80u, 0x3f803f80u, 0x3f803f80u, 0x3f803f80u};
    asm volatile("" : "+v"(ones));
    u32x4 xf[2][8];
    u32x2 wf[2][8];
#define RD(ks, n)                                                                                          \
  do {                                                                                                     \
    if ((n) >= 2 && (n) < 10) xf[ks][(n) - 2] = *(const u32x4*)(xr[ks] + (((n) - 2) & 3) * 4096 + (((n) - 2) >> 2) * 1024); \
    else { const int m_ = (n) < 2 ? (n) : (n) - 8; wf[ks][m_] = tr_read(wp[m_ >> 1] + (ks) * 4096 + (m_ & 1) * 512); } \
  } while (0)
#define WF(ks, i) ((u32x4){wf[ks][2 * (i)][0], wf[ks][2 * (i)][1], wf[ks][2 * (i) + 1][0], wf[ks][2 * (i) + 1][1]})
#define KSTEP(ks, READS, CSUM)                                                                             \
  do {                                                                                                     \
    _Pragma("unroll") for (int n_ = 0; n_ < 32; ++n_) {                                                    \
      acc[n_ >> 3][n_ & 7] = MFMA16_BF(WF(ks, n_ >> 3), xf[ks][n_ & 7], acc[n_ >> 3][n_ & 7]);             \
      if (READS && n_ < 16) RD((ks) ^ 1, n_);                                                              \
      if (CSUM && n_ >= 30) cs[n_ & 1] = MFMA16_BF(WF(ks, n_ & 1), ones, cs[n_ & 1]);                      \
      SCHED();                                                                                             \
    }                                                                                                      \
  } while (0)
    SET_BUF(0);
    TS_MARK();
    BARRIER();
    TS_MARK();
#pragma unroll
    for (int n = 0; n < 16; ++n) RD(0, n);
    SCHED();
    KSTEP(0, true, 1);
    int nxt = STAGE_B;
#pragma unroll 1
    for (int u = 0; u < 9; ++u) {
      BARRIER();
      SET_BUF(nxt);
      SCHED();
      KSTEP(1, true, 1);
      KSTEP(0, true, 1);
      nxt = nxt == 2 * STAGE_B ? 0 : nxt + STAGE_B;
    }
    TS_MARK();
#pragma unroll
    for (int i = 0; i < 2; ++i) {
      cs[i] = MFMA16_BF(WF(1, i), ones, cs[i]);
      u32x4 hl;
#pragma unroll
      for (int e = 0; e < 4; ++e) { const float hi = rnd_bf(cs[i][e]); hl[e] = pk_bf(hi, cs[i][e] - hi); }
      if (i16 == 0) *(u32x4*)(sm + G_CS + w * 128 + i * 64 + g * 16) = hl;
    }
    BARRIER();
    unsigned ca[4];
    f32x4 st[4];
#pragma unroll
    for (int i = 0; i < 4; ++i) ca[i] = *(const unsigned*)(sm + G_CS + (w ^ (i & 2)) * 128 + (i & 1) * 64 + i16 * 4);
#pragma unroll
    for (int r = 0; r < 4; ++r) st[r] = *(const f32x4*)(sm + G_ST + (wr * 128 + (r >> 1) * 64 + 8 * (l & 7) + 4 * (i16 >> 3) + (r & 1) * 2) * 8);
    SCHED();
    TS_MARK();
    const unsigned ao = WS_AF + ((panel * 4 + wr * 2) * 48 + ft * 2 + fh) * 4096 + l * 16;
#pragma unroll
    for (int r = 0; r < 10; ++r) {
      if (r < 8) {
#pragma unroll
        for (int i = 0; i < 4; ++i) acc[i][r] = MFMA16_BF(WF(1, i), xf[1][r], acc[i][r]);
      }
      if (r >= 1 && r < 9) {
        const int e = r - 1;
        const u32x2 bm = {g == 0 ? f2u(st[e >> 1][(e & 1) * 2]) : 0u, 0u};
#pragma unroll
        for (int i = 0; i < 4; ++i) acc[i][e] = MFMA16K16_BF(((u32x2){ca[i], 0u}), bm, acc[i][e]);
      }
      if (r >= 2) {
        const int e = r - 2;
        const float r2 = st[e >> 1][(e & 1) * 2 + 1];
        u32x4 o;
#pragma unroll
        for (int i = 0; i < 2; ++i) {
          const f32x4 hv = (acc[i][e] * r2) * acc[2 + i][e];
          o[2 * i] = pk_bf(hv[0], hv[1]); o[2 * i + 1] = pk_bf(hv[2], hv[3]);
        }
        ST128(o, rw, ao + (e >> 2) * 48 * 4096 + (e & 3) * 1024);
      }
      SCHED();
    }
#undef SET_BUF
#undef RD
#undef WF
#undef KSTEP
    }
    SCHED();
    TS_MARK();
    WAIT_V(0);
    __builtin_amdgcn_s_barrier();
    asm volatile("" ::: "memory");
    TS_MARK();
    const u64 tag = next_epoch(*(const u64*)(sm + G_TAG));
    if (tid == 0) st_dev(ws + WS_FH + (panel * 32 + ft) * 8, tag);
    SCHED();
#pragma unroll
    for (int j = 0; j < 18; ++j)
#pragma unroll
      for (int i = 0; i < 2; ++i) B[j][i] = *(const u32x4*)(sm + j * 8192 + (2 * w + i) * 1024 + l * 16);
    SCHED();
    if (w == 0) wait_flags(ws + WS_FH + (xcd * 32 + (l < 24 ? l : 0)) * 8, tag);
    }
#pragma unroll
    for (int j = 0; j < NPIN; ++j) asm volatile("" : "+v"(B[j][0]), "+v"(B[j][1]));
    SCHED();
    TS_SET(7);
    BARRIER();
    asm volatile("" ::: "memory");
    TS_MARK();
    const char* asrc = ws + WS_AF + p3 * 48 * 4096;
#define LOADA(ch, st)                                                                                                 \
  asm volatile("s_mov_b32 m0, %2\n\ts_nop 3\n\tglobal_load_lds_dwordx4 %0, %1 sc1\n\tglobal_load_lds_dwordx4 %0, %1 offset:1024 sc1\n\t" \
               "global_load_lds_dwordx4 %0, %1 offset:2048 sc1\n\tglobal_load_lds_dwordx4 %0, %1 offset:3072 sc1" ::"v"(l * 16), \
               "s"(asrc + (ch) * 32768 + (st) * 4096), "s"(smb + ((ch) & 3) * 32768 + (st) * 4096) : "memory")
#define LOADA2(ch) do { if (kh == (((ch) & 1) ^ 1)) { LOADA(ch, w & 3); LOADA(ch, (w & 3) + 4); } } while (0)
    LOADA(0, w);
    LOADA(1, w);
    const u64 tag = next_epoch(*(const u64*)(sm + G_TAG));
    f32x4 acc[4][2];
#pragma unroll
    for (int m = 0; m < 4; ++m) { acc[m][0] = (f32x4){0.f, 0.f, 0.f, 0.f}; acc[m][1] = acc[m][0]; }
    const LDS_AS char* ap[2];
#pragma unroll
    for (int i = 0; i < 2; ++i) { ap[i] = (const LDS_AS char*)sm + i * 65536 + kh * 4096 + l * 16; asm volatile("" : "+v"(ap[i])); }
    u32x4 A[3][4], V[11], xres[2];
    u32x2 mi[5];
    unsigned orow[2];
    u64 fv = 0;
    const unsigned vo = WS_VT + (c0 * 41 + 2 * g) * VP + (p3 >> 3) * 2048 + ((l >> 3) & 1) * 1024 + ((p3 & 7) * 8 + (l & 7)) * 16;
#define LOADV()                                                                                                       \
  do {                                                                                                                \
    _Pragma("unroll") for (int u = 0; u < 10; ++u) V[u] = LD128_DEV2(rw, vo, ((u >> 1) * 8 + (u & 1)) * VP);         \
    V[10] = LD128_DEV2(rw, vo - 2 * g * VP, 40 * VP);                                                                 \
  } while (0)
    bool early = false;
    WAIT_V(4);
    BARRIER();
    TS_MARK();
#define ZPART()                                                                                                       \
  do {                                                                                                                \
    if (g == 0) {                                                                                                     \
      _Pragma("unroll") for (int m = 0; m < 4; ++m) V[0][m] = (V[0][m] & 0xffffu) | (V[10][m] << 16);     \
    }                                                                                                                 \
    _Pragma("unroll") for (int ks = 0; ks < 5; ++ks)                                                                  \
      _Pragma("unroll") for (int m = 0; m < 4; ++m)                                                                   \
        acc[m][0] = MFMA16K16_BF(((u32x2){V[2 * ks][m], V[2 * ks + 1][m]}), mi[ks], acc[m][0]);                       \
  } while (0)
#pragma unroll
    for (int m = 0; m < 8; ++m) A[m >> 2][m & 3] = *(const LDS_AS u32x4*)(ap[0] + (m >> 2) * 8192 + (m & 3) * 1024);
    SCHED();
    const rsrc_t rx = mk_rsrc(x, 2048 * 640 * 2);
#pragma unroll
    for (int j = 0; j < 24; ++j) {
      if ((j & 3) == 2 && j < 22) {
        if (j == 2) WAIT_V(4); else if (j == 6) WAIT_V(8); else if (j == 10) WAIT_V(14); else if (j == 14) WAIT_V(10);
        BARRIER();
        if (j == 14) { early = all_lanes(fv == tag); asm volatile("" ::: "memory"); }
      }
#pragma unroll
      for (int n = 0; n < 8; ++n) {
        acc[n & 3][n >> 2] = MFMA16_BF(A[j % 3][n & 3], B[j % NB][n >> 2], acc[n & 3][n >> 2]);
        if (n < 4 && j < 22) A[(j + 2) % 3][n] = *(const LDS_AS u32x4*)(ap[((j + 2) >> 3) & 1] + ((j + 2) & 7) * 8192 + n * 1024);
        if (n == 2 || n == 6) {
          const int q = 2 * j + (n >> 2);
          if (q < 16)
            asm volatile("s_mov_b32 m0, %2\n\ts_nop 0\n\tglobal_load_lds_dwordx4 %0, %1 offset:%3 sc1" ::"v"(l * 16), "s"(asrc + (2 + (q >> 2)) * 32768 + w * 4096),
                         "s"(smb + ((2 + (q >> 2)) & 3) * 32768 + w * 4096), "n"((q & 3) * 1024) : "memory");
          else if (q < 28) B[j - 8][q & 1] = LD128_DEV2(rw, bo + (c0 ^ (q & 1)) * 896, (j + 10) * 14336);
          else if (q < 38) V[q - 28] = LD128_DEV2(rw, vo, (((q - 28) >> 1) * 8 + (q & 1)) * VP);
          else if (q == 38) V[10] = LD128_DEV2(rw, vo - 2 * g * VP, 40 * VP);
        }
        if (n == 4) {
          if (j == 8 || j == 9) {
            orow[j - 8] = s_of_rho(p3 * 64 + 8 * ((4 * g + 2 * kh + j - 8) & 7) + 4 * (g >> 1) + (w & 3)) * 1280 + min(d1s + i16, 79) * 16;
            xres[j - 8] = __builtin_amdgcn_raw_buffer_load_b128(rx, orow[j - 8], 0, 0);
          }
          if (j == 10) fv = ld_dev(ws + WS_FV + (l < 41 ? l : 0) * 8);
          if (j >= 11 && j < 16) mi[j - 11] = *(const u32x2*)&g_T80.i[ct][j - 11][l][0];
        }
        SCHED();
      }
      if (j == ZJ) {
        if (!early) {
          wait_flags(ws + WS_FV + (l < 41 ? l : 0) * 8, tag);
          LOADV();
        }
#pragma unroll
        for (int u = 0; u < 11; ++u) asm volatile("" : "+v"(V[u]));
        ZPART();
      }
      SCHED();
      if (j == 11) TS_MARK();
    }
#undef LOADA
#undef LOADA2
#undef LOADB
    TS_MARK();
#undef LOADV
#undef ZPART
    char* tp = sm + 65536 + kh * 16384 + l * 8;
#pragma unroll
    for (int m = 0; m < 4; ++m)
#pragma unroll
      for (int i = 0; i < 2; ++i)
        *(u32x2*)(tp + (m * 8 + (c0 ^ i)) * 512) = (u32x2){pk_bf(acc[m][i][0], acc[m][i][1]), pk_bf(acc[m][i][2], acc[m][i][3])};
    BARRIER();
    TS_MARK();
    unsigned pp[2][8];
#pragma unroll
    for (int k = 0; k < 2; ++k)
#pragma unroll
      for (int c = 0; c < 8; ++c) pp[k][c] = *(const unsigned*)(sm + 65536 + k * 16384 + ((w & 3) * 8 + c) * 512 + l * 8 + kh * 4);
    SCHED();
    const rsrc_t ro = mk_rsrc(out, 2048 * 640 * 2);
#pragma unroll
    for (int r = 0; r < 2; ++r) {
      u32x4 o;
#pragma unroll
      for (int c = 0; c < 4; ++c) {
        const float a = r ? bf_hi(pp[0][2 * c]) + bf_hi(pp[1][2 * c]) : bf_lo(pp[0][2 * c]) + bf_lo(pp[1][2 * c]);
        const float b2 = r ? bf_hi(pp[0][2 * c + 1]) + bf_hi(pp[1][2 * c + 1]) : bf_lo(pp[0][2 * c + 1]) + bf_lo(pp[1][2 * c + 1]);
        o[c] = pk_bf(a + bf_lo(xres[r][c]), b2 + bf_hi(xres[r][c]));
      }
      if (i16 < nd1) __builtin_amdgcn_raw_buffer_store_b128(o, ro, orow[r], 0, OUTAUX);
    }
    TS_MARK();
    TS_END(0, 0);
    return;
  }
  const int aux = idx * 8 + xcd;
  u64 tag;
  f16x2 tc[8], tsn[8];
  u32x4 G1[8], G3[8];
  u32x4 wo_[3];
  u16x2 m2 = {0, 0};
  const u16* wl;
  unsigned wdst;
  int wst;
  {
    const int row = 4 * w + g, i16 = l & 15;
    dma1(l * 16, (const char*)&g_T80 + w * 2048, smb + A_T80 + w * 2048);
    dma1(l * 16, (const char*)&g_T80 + w * 2048 + 1024, smb + A_T80 + w * 2048 + 1024);
    const u32x4* xp = (const u32x4*)(x + s_of_rho(aux * 32 + row) * 640) + i16;
    u32x4 xv[5];
#pragma unroll
    for (int c = 0; c < 5; ++c) xv[c] = xp[c * 16];
    const u64 ep = ld_dev(ws + WS_EP);
    SCHED();
    {
      const float a0 = (float)(aux * w) * (1.f / 640.f), a1 = (float)(l & 31) * (1.f / 2048.f);
#pragma unroll
      for (int e = 0; e < 8; ++e) {
        const float a = a0 + a1 * (float)(2 * (e & 1) + 8 * (e >> 1) + 4 * (l >> 5)), a2 = a + a1;
        tc[e] = as_h2(pk_h(__builtin_amdgcn_cosf(a), __builtin_amdgcn_cosf(a2)));
        tsn[e] = as_h2(pk_h(__builtin_amdgcn_sinf(a), __builtin_amdgcn_sinf(a2)));
        asm volatile("" : "+v"(tc[e]), "+v"(tsn[e]));
      }
    }
    SCHED();
    WAIT_V(6);
    BARRIER();
    u32x4 fm[15];
#pragma unroll
    for (int u = 0; u < 15; ++u) fm[u] = *(const u32x4*)(sm + A_T80 + u * 1024 + l * 16);
    SCHED();
    TS_MARK();
    float f[40];
#pragma unroll
    for (int c = 0; c < 5; ++c)
#pragma unroll
      for (int j = 0; j < 4; ++j) { f[c * 8 + 2 * j] = bf_lo(xv[c][j]); f[c * 8 + 2 * j + 1] = bf_hi(xv[c][j]); }
    tag = next_epoch(ep);
    asm volatile("" ::"v"(tag), "v"(f[39]));
    SCHED();
    {
      const char* wsrc = (const char*)(w < 4 ? w_r : w_i) + (64 * (4 * (aux >> 3) + (w & 3)) + 8 * (aux & 7)) * 642;
#pragma unroll
      for (int i = 0; i < 5; ++i) dma1(l * 16, wsrc + i * 1024, smb + A_W + w * 5152 + i * 1024);
      if (l == 0) dma1(0, wsrc + 5120, smb + A_W + w * 5152 + 5120);
      dma1(l * 16, (const char*)&g_TF + w * 2048, smb + A_TF + w * 2048);
      dma1(l * 16, (const char*)&g_TF + w * 2048 + 1024, smb + A_TF + w * 2048 + 1024);
    }
    SCHED();
    float s4[4] = {0.f, 0.f, 0.f, 0.f};
#pragma unroll
    for (int e = 0; e < 40; ++e) s4[e & 3] += f[e];
    const float mean = sum16((s4[0] + s4[1]) + (s4[2] + s4[3])) * (1.f / 640.f);
    float q4[4] = {0.f, 0.f, 0.f, 0.f};
#pragma unroll
    for (int e = 0; e < 40; ++e) { f[e] -= mean; q4[e & 3] += f[e] * f[e]; }
    const float mb = rnd_bf(mean), vb = rnd_bf(sum16((q4[0] + q4[1]) + (q4[2] + q4[3])) * (1.f / 640.f));
    const float sd = rnd_bf(__builtin_amdgcn_sqrtf(rnd_bf(vb + 1e-5f)));
    const float rs = __builtin_amdgcn_rcpf(sd), off = (mean - mb) * rs;
    if (i16 == 0) st_dev(ws + WS_ST + (aux * 32 + row) * 8, (tag << 32) | (f2u(mb) >> 16) | f2u(sd));
#pragma unroll
    for (int c = 0; c < 5; ++c) {
      u32x4 oh;
#pragma unroll
      for (int j = 0; j < 4; ++j) oh[j] = pk_h(f[c * 8 + 2 * j] * rs + off, f[c * 8 + 2 * j + 1] * rs + off);
      *(u32x4*)(sm + A_XH + row * 1344 + (c * 16 + i16) * 16) = oh;
    }
    TS_MARK();
    {
      const int h = l >> 5, p = l & 3;
      const char* rd = sm + A_XH + (4 * w + 2 * (g & 1) + (p >> 1)) * 1344 + (8 * h + ((l & 15) >> 2)) * 16 + (p & 1) * 8;
      u32x4 ax[5];
#pragma unroll
      for (int ks = 0; ks < 5; ++ks) {
        const u32x2 lo = tr_read(rd + ks * 256), hi = tr_read(rd + ks * 256 + 64);
        ax[ks] = (u32x4){lo[0], lo[1], hi[0], hi[1]};
      }
      SCHED();
      const int ol = l & 31;
      char* ut = sm + A_UT + (w >> 1) * 10304 + (ol >> 1) * 256 + (w & 1) * 8;
      const int ub = (8 * h + (ol & 1)) ^ (ol & 14);
      f32x16 acc[3];
#pragma unroll
      for (int nt = 0; nt < 3; ++nt) {
#pragma unroll
        for (int e = 0; e < 16; ++e) acc[nt][e] = 0.f;
#pragma unroll
        for (int ks = 0; ks < 5; ++ks) acc[nt] = MFMA32_H(ax[ks], fm[nt * 5 + ks], acc[nt]);
      }
#pragma unroll
      for (int nt = 0; nt < 3; ++nt)
        if (nt < 2 || ol < 16) {
#pragma unroll
          for (int dl = 0; dl < 4; ++dl)
            *(u32x2*)(ut + nt * 4096 + ((ub ^ (2 * dl)) << 4)) = (u32x2){pk_h(acc[nt][dl], acc[nt][4 + dl]), pk_h(acc[nt][8 + dl], acc[nt][12 + dl])};
        }
    }
    WAIT_V(1);
    BARRIER();
    TS_MARK();
    {
      u32x4 uv[5];
#pragma unroll
      for (int i = 0; i < 5; ++i) {
        const int seg = i * 128 + (tid >> 2);
        uv[i] = *(const u32x4*)(sm + A_UT + (tid & 3) * 10304 + ((seg ^ ((seg >> 3) & 14)) << 4));
      }
      SCHED();
#pragma unroll
      for (int i = 0; i < 5; ++i) ST128(uv[i], rw, WS_UB + (i * 8 + w) * CP + aux * 1024 + l * 16);
    }
    SCHED();
    const int eq = l & 3, kb = (l >> 2) & 7, cl = 2 * (w & 3) + (l >> 5), half = w >> 2;
    int fr = 64 * (4 * (aux >> 3) + eq) + 8 * (aux & 7) + kb;
    if (half) fr = (2048 - fr) & 2047;
    wl = (const u16*)(sm + A_W) + eq * 2576 + kb * 321 + (half ? 80 - cl : cl);
    wdst = WS_WT + cl * CP + (half * 2048 + (fr >> 8) * 256 + (fr & 63) * 4 + ((fr >> 6) & 3)) * 16;
    wst = half ? -8 : 8;
#define WT_READ(i0)                                                                                                   \
  do {                                                                                                                \
    _Pragma("unroll") for (int i_ = 0; i_ < 3; ++i_)                                                                  \
      _Pragma("unroll") for (int j_ = 0; j_ < 4; ++j_)                                                                \
        wo_[i_][j_] = __builtin_bit_cast(unsigned, ((u16x2){(wl + wst * ((i0) + i_))[80 * j_], (wl + wst * ((i0) + i_))[10304 + 80 * j_]})); \
    _Pragma("unroll") for (int i_ = 0; i_ < 3; ++i_) {                                                                \
      _Pragma("unroll") for (int j_ = 0; j_ < 4; ++j_) m2 = __builtin_elementwise_max(m2, __builtin_bit_cast(u16x2, wo_[i_][j_] & 0x7fff7fffu)); \
      if (w >= 4) wo_[i_] = (u32x4){wo_[i_][3], wo_[i_][2], wo_[i_][1], wo_[i_][0]} ^ 0x80000000u;                    \
      asm volatile("" : "+v"(wo_[i_]));                                                                               \
    }                                                                                                                 \
  } while (0)
#define WT_STORE(i0)                                                                                                  \
  do {                                                                                                                \
    _Pragma("unroll") for (int i_ = 0; i_ < 3; ++i_)                                                                  \
      if ((i0) + i_ < 5 || ((w & 3) == 0 && l < 32)) ST128(wo_[i_], rw, wdst + ((i0) + i_) * 8 * CP);                 \
  } while (0)
    WT_READ(0);
    SCHED();
    TS_MARK();
    WAIT_V(0);
    __builtin_amdgcn_s_barrier();
    if (tid == 0) st_dev(ws + WS_F1 + aux * 8, tag);
    TS_MARK();
    WT_STORE(0);
    SCHED();
    WT_READ(3);
    WT_STORE(3);
    const unsigned mx = max64(max((unsigned)m2[0], (unsigned)m2[1]));
    if (l == 0) ((unsigned*)(sm + A_MX))[w] = mx;
    SCHED();
    TS_MARK();
  }
#define PUBLISH_F2()                                                                                                  \
  do {                                                                                                                \
    if (tid == 0) {                                                                                                   \
      const u32x4 m0_ = *(const u32x4*)(sm + A_MX), m1_ = *(const u32x4*)(sm + A_MX + 16);                            \
      const unsigned mx_ = max(max(max(m0_[0], m0_[1]), max(m0_[2], m0_[3])), max(max(m1_[0], m1_[1]), max(m1_[2], m1_[3]))); \
      st_dev(ws + WS_F2 + aux * 8, (tag & ~0xffffull) | mx_);                                                         \
    }                                                                                                                 \
  } while (0)
  WAIT_V(0);
  BARRIER();
  PUBLISH_F2();
  TS_MARK();
  if (aux < 41) {
    const int c1 = aux, c1s = c1 == 40 ? 0 : c1;
    const bool real_cls = c1s == 0;
    char* eb = sm + A_E;
    const u32x4 g8 = *(const u32x4*)&g_TF.g8[l][0][0];
#pragma unroll
    for (int i = 0; i < 8; ++i) { G1[i] = *(const u32x4*)(sm + A_TF + i * 1024 + l * 16); G3[i] = *(const u32x4*)(sm + A_TF + 8192 + i * 1024 + l * 16); }
    SCHED();
    wait_flags(ws + WS_F1 + l * 8, tag);
    TS_MARK();
    u32x4 ar[4], ai[4];
    {
      const unsigned uo = WS_UB + c1s * CP + (l >> 2) * 1024 + w * 128 + (l & 3) * 16;
#pragma unroll
      for (int i = 0; i < 4; ++i) {
        ar[i] = LD128_DEV(rw, uo + (c1 == 40 ? 64 : 0) + i * 16384);
        ai[i] = LD128_DEV(rw, uo + 64 + i * 16384);
      }
    }
    u64 f2 = ld_dev(ws + WS_F2 + l * 8);
#pragma unroll
    for (int i = 0; i < 8; ++i) asm volatile("" : "+v"(G1[i]), "+v"(G3[i]));
    SCHED();
    if (real_cls) {
#pragma unroll
      for (int i = 0; i < 4; ++i) ai[i] = (u32x4){0u, 0u, 0u, 0u};
    }
    char* erow = eb + (2 * w) * 4096 + ((l ^ e_key(w)) << 4);
    f32x16 zero;
#pragma unroll
    for (int e = 0; e < 16; ++e) zero[e] = 0.f;
    u32x2 wv[16];
    const unsigned wbase = WS_WT + c1 * CP + (g >> 1) * 32768 + (g & 1) * 8 + (l & 15) * 16 + ((4 * ((w >> 1) & 1) + (w & 1)) * 64 + 32 * (w >> 2)) * 64;
    bool early;
    {
      f32x16 dr[2], di[2];
#pragma unroll
      for (int t = 0; t < 2; ++t) {
        dr[t] = zero; di[t] = zero;
#pragma unroll
        for (int ks = 0; ks < 2; ++ks) {
          const u32x4 C = G1[(t * 2 + ks) * 2], S = G1[(t * 2 + ks) * 2 + 1];
          dr[t] = MFMA32_H(ar[t * 2 + ks], C, dr[t]);
          di[t] = MFMA32_H(ai[t * 2 + ks], C, di[t]);
          dr[t] = MFMA32_H(ai[t * 2 + ks], S, dr[t]);
          di[t] = MFMA32_H(neg_h(ar[t * 2 + ks]), S, di[t]);
        }
      }
      SCHED();
      TS_MARK();
      early = all_lanes((f2 >> 16) == (tag >> 16));
      asm volatile("" ::: "memory");
      if (early) {
#pragma unroll
        for (int i = 0; i < 16; ++i) wv[i] = LD64_DEV(rw, wbase + (i & 1) * 8192 + (i >> 1) * 256);
      }
      SCHED();
      u32x4 br[2][2], bi[2][2];
      bfly_tw<false>(dr, di, tc, tsn, br, bi);
#pragma unroll
      for (int t2 = 0; t2 < 2; ++t2) {
        f32x16 xr_ = zero, xi_ = zero;
#pragma unroll
        for (int ks = 0; ks < 2; ++ks) {
          const u32x4 C = G3[(t2 * 2 + ks) * 2], S = G3[(t2 * 2 + ks) * 2 + 1];
          xr_ = MFMA32_H(C, br[t2][ks], xr_);
          xi_ = MFMA32_H(C, bi[t2][ks], xi_);
          xr_ = MFMA32_H(S, bi[t2][ks], xr_);
          xi_ = MFMA32_H(S, neg_h(br[t2][ks]), xi_);
        }
        u32x4 o0[2], o1[2];
        pack_h(xr_, o0);
        pack_h(xi_, o1);
#pragma unroll
        for (int s2 = 0; s2 < 2; ++s2) {
          *(u32x4*)(erow + (t2 * 2 + s2) * 1024) = o0[s2];
          *(u32x4*)(erow + 4096 + (t2 * 2 + s2) * 1024) = swap_halves(o1[s2]);
        }
      }
    }
    SCHED();
    TS_MARK();
    if (!early) {
      f2 = wait_flags(ws + WS_F2 + l * 8, tag);
#pragma unroll
      for (int i = 0; i < 16; ++i) wv[i] = LD64_DEV(rw, wbase + (i & 1) * 8192 + (i >> 1) * 256);
    }
    float osc;
    u32x2 gi;
    {
      int ex = (int)(max64((unsigned)f2 & 0xffffu) >> 7);
      ex = ex < 8 ? 8 : (ex > 240 ? 240 : ex);
      const float up = u2f((unsigned)(250 - ex) << 23);
      gi = (u32x2){pk_bf(bf_lo(g8[2]) * up, bf_hi(g8[2]) * up), pk_bf(bf_lo(g8[3]) * up, bf_hi(g8[3]) * up)};
      osc = u2f((unsigned)(ex + 2) << 23) * ((real_cls ? 2.f : 4.f) * 256.f / (239.f * 640.f));
    }
    BARRIER();
    TS_MARK();
    {
      const int rr = 4 * g + ((l & 15) >> 2), wrw = l & 15;
      const int rdo = rr * 4096 + w * 512 + (((l & 3) ^ e_key(rr >> 1)) << 4) + (rr & 1) * 8;
      const int wro = wrw * 4096 + w * 512 + ((g ^ e_key(wrw >> 1)) << 4) + (wrw & 1) * 8;
      const u32x2 gf = {g8[0], g8[1]};
      const f32x4 z4 = {0.f, 0.f, 0.f, 0.f};
      u32x2 ev[16];
#pragma unroll
      for (int i = 0; i < 16; ++i) ev[i] = tr_read(eb + (rdo ^ (((i >> 1) & 3) * 64 + (i & 1) * 8)) + (i >> 3) * 256);
      SCHED();
      f32x4 xx[16], vv[16];
      xx[0] = MFMA16K16_H(gf, ev[0], z4);
      xx[1] = MFMA16K16_H(gf, ev[1], z4);
#pragma unroll
      for (int i = 0; i < 18; ++i) {
        if (i + 2 < 16) xx[i + 2] = MFMA16K16_H(gf, ev[i + 2], z4);
        if (i < 16) {
          const f32x4 xs = xx[i];
          const float r0 = bf_lo(wv[i][0]), i0 = bf_hi(wv[i][0]), r1 = bf_lo(wv[i][1]), i1 = bf_hi(wv[i][1]);
          const u32x2 y = {pk_bf(xs[0] * r0 - xs[1] * i0, xs[0] * i0 + xs[1] * r0), pk_bf(xs[2] * r1 - xs[3] * i1, xs[2] * i1 + xs[3] * r1)};
          vv[i] = MFMA16K16_BF(y, gi, z4);
        }
        if (i >= 2) {
          const int k = i - 2;
          *(u32x2*)(eb + (wro ^ (((k >> 1) & 3) * 64 + (k & 1) * 8)) + (k >> 3) * 256) = (u32x2){pk_h(vv[k][0], vv[k][1]), pk_h(vv[k][2], vv[k][3])};
        }
        SCHED();
      }
    }
    BARRIER();
    TS_MARK();
    {
      f32x16 dr[2], di[2];
      u32x4 yr[4], yi[4];
#pragma unroll
      for (int i = 0; i < 4; ++i) { yr[i] = *(const u32x4*)(erow + i * 1024); yi[i] = *(const u32x4*)(erow + 4096 + i * 1024); }
      SCHED();
#pragma unroll
      for (int t2 = 0; t2 < 2; ++t2) {
        dr[t2] = zero; di[t2] = zero;
#pragma unroll
        for (int s2 = 0; s2 < 2; ++s2) {
          const u32x4 pr_ = yr[t2 * 2 + s2], pi_ = swap_halves(yi[t2 * 2 + s2]);
          const u32x4 C = G1[(t2 * 2 + s2) * 2], S = G1[(t2 * 2 + s2) * 2 + 1];
          dr[t2] = MFMA32_H(pr_, C, dr[t2]);
          di[t2] = MFMA32_H(pi_, C, di[t2]);
          dr[t2] = MFMA32_H(neg_h(pi_), S, dr[t2]);
          di[t2] = MFMA32_H(pr_, S, di[t2]);
        }
      }
      u32x4 qr[2][2], qi[2][2];
      bfly_tw<true>(dr, di, tc, tsn, qr, qi);
      const unsigned vo = WS_VT + (w * 41 + c1) * VP + l * 16;
#pragma unroll
      for (int t = 0; t < 2; ++t) {
        f32x16 zr = zero, zq = zero;
#pragma unroll
        for (int ks = 0; ks < 2; ++ks) {
          const u32x4 C = G3[(t * 2 + ks) * 2], S = G3[(t * 2 + ks) * 2 + 1];
          zr = MFMA32_H(C, qr[t][ks], zr);
          zq = MFMA32_H(C, qi[t][ks], zq);
          zr = MFMA32_H(S, neg_h(qi[t][ks]), zr);
          zq = MFMA32_H(S, qr[t][ks], zq);
        }
#pragma unroll
        for (int u = 0; u < 4; ++u) {
          u32x4 o;
#pragma unroll
          for (int j = 0; j < 4; ++j) o[j] = pk_bf(zr[4 * u + j] * osc, zq[4 * u + j] * osc);
          ST128(o, rw, vo + (t * 4 + u) * 1024);
        }
      }
    }
    WAIT_V(0);
    __builtin_amdgcn_s_barrier();
    if (tid == 0) st_dev(ws + WS_FV + c1 * 8, tag);
    TS_END(0, 1);
    return;
  }
  if (aux == 63 && w == 0) {
    wait_flags(ws + WS_F1 + l * 8, tag);
#pragma unroll 1
    for (int i = 0; i < 3; ++i) wait_flags(ws + WS_FA + i * 1024 + l * 16, tag);
    if (l == 0) st_dev(ws + WS_EP, tag);
  }
  TS_END(0, 2);
}

#if defined(__HIP_DEVICE_COMPILE__)
#pragma clang attribute pop
#endif

extern "C" void kernel_launch(void* const* d_in, const int* in_sizes, int n_in, void* d_out, int out_size, void* d_ws, size_t ws_size,
                              hipStream_t stream) {
  const u16* x = (const u16*)d_in[0];
  const u16* w_r = (const u16*)d_in[1];
  const u16* w_i = (const u16*)d_in[2];
  const u16* wg = (const u16*)d_in[4];
  const u16* wu = (const u16*)d_in[5];
  const u16* wd = (const u16*)d_in[6];
  char* ws = (char*)d_ws;
  ka<<<256, 512, 0, stream>>>(x, w_r, w_i, wg, wu, wd, ws, (u16*)d_out);
}
```

```cpp
#define MR 0
#define MK 2
#include <hip/hip_runtime.h>
#include <hip/hip_bf16.h>

typedef unsigned short u16;
typedef __attribute__((ext_vector_type(8))) __bf16 bf16x8;
typedef __attribute__((ext_vector_type(2))) __bf16 bf16x2;
typedef __attribute__((ext_vector_type(8))) _Float16 f16x8;
typedef __attribute__((ext_vector_type(4))) _Float16 f16x4;
typedef __attribute__((ext_vector_type(2))) _Float16 f16x2;
typedef __attribute__((ext_vector_type(4))) short s16x4;
typedef __attribute__((ext_vector_type(2))) float f32x2;
typedef __attribute__((ext_vector_type(4))) float f32x4;
typedef __attribute__((ext_vector_type(16))) float f32x16;
typedef __attribute__((ext_vector_type(4))) unsigned u32x4;
typedef __attribute__((ext_vector_type(2))) unsigned u32x2;
typedef __attribute__((ext_vector_type(2))) u16 u16x2;
typedef __amdgpu_buffer_rsrc_t rsrc_t;
#define DI __device__ __forceinline__
#define LDS_AS __attribute__((address_space(3)))

namespace tb {
constexpr double kPi = 3.14159265358979323846264338327950288;
constexpr double sinT(double x) { double x2 = x * x, t = x, s = x; for (int i = 1; i < 12; ++i) { t *= -x2 / ((2 * i) * (2 * i + 1)); s += t; } return s; }
constexpr double cosT(double x) { double x2 = x * x, t = 1, s = 1; for (int i = 1; i < 12; ++i) { t *= -x2 / ((2 * i - 1) * (2 * i)); s += t; } return s; }
constexpr double cos2pi(int q, int den) {
  q %= den; if (2 * q > den) q = den - q;
  double sg = 1; if (4 * q > den) { q = den / 2 - q; sg = -1; }
  if (8 * q > den) return sg * sinT(2 * kPi * (den / 4 - q) / den);
  return sg * cosT(2 * kPi * q / den);
}
constexpr u16 bfbits(double d) { unsigned u = __builtin_bit_cast(unsigned, (float)d); u += 0x7fffu + ((u >> 16) & 1u); return (u16)(u >> 16); }
constexpr u16 hbits(double d) { return __builtin_bit_cast(u16, (_Float16)d); }
constexpr int krow(int s, int h, int j) { return 16 * s + 8 * (j >> 2) + 4 * h + (j & 3); }
template <int N> struct CT { double c[N]; };
template <int N> constexpr CT<N> mkC() { CT<N> c{}; for (int i = 0; i < N; ++i) c.c[i] = cos2pi(i, N); return c; }
constexpr CT<64> c64 = mkC<64>();
constexpr CT<80> c80 = mkC<80>();
constexpr double m80(int o, int d) { return o == 1 ? c80.c[(40 * d) % 80] : c80.c[((o >> 1) * d + ((o & 1) ? 20 : 0)) % 80]; }
struct alignas(16) TabF {
  u16 g1[2][2][2][64][8];
  u16 g3[2][2][2][64][8];
  u16 g8[64][2][4];
};
constexpr TabF mkTabF() {
  TabF T{};
  for (int t = 0; t < 2; ++t) for (int ks = 0; ks < 2; ++ks) for (int l = 0; l < 64; ++l) for (int j = 0; j < 8; ++j) {
    const int r = l & 31, k = krow(ks, l >> 5, j);
    const int q1 = (r * (2 * k + t)) & 63, q3 = (k * (2 * r + t)) & 63;
    T.g1[t][ks][0][l][j] = hbits(c64.c[q1] * 0.25); T.g1[t][ks][1][l][j] = hbits(c64.c[(q1 + 48) & 63] * 0.25);
    T.g3[t][ks][0][l][j] = hbits(c64.c[q3] * 0.25); T.g3[t][ks][1][l][j] = hbits(c64.c[(q3 + 48) & 63] * 0.25);
  }
  for (int l = 0; l < 64; ++l) for (int e = 0; e < 4; ++e) {
    const int m = l & 15, k = 4 * (l >> 4) + e, q = (8 * (m >> 1) * (k >> 1)) & 63;
    const double C = c64.c[q], S = c64.c[(q + 48) & 63];
    T.g8[l][0][e] = hbits(0.25 * ((m & 1) == (k & 1) ? C : ((m & 1) ? -S : S)));
    T.g8[l][1][e] = bfbits(239. / 256. * ((m & 1) == (k & 1) ? C : ((k & 1) ? -S : S)));
  }
  return T;
}
struct alignas(16) Tab80 {
  u16 f[3][5][64][8];
  u16 pad[512];
  u16 i[6][5][64][4];
};
constexpr Tab80 mkTab80() {
  Tab80 T{};
  for (int n = 0; n < 3; ++n) for (int ks = 0; ks < 5; ++ks) for (int l = 0; l < 64; ++l) for (int j = 0; j < 8; ++j)
    T.f[n][ks][l][j] = 32 * n + (l & 31) < 80 ? hbits(m80(32 * n + (l & 31), 16 * ks + 8 * (l >> 5) + j)) : (u16)0;
  for (int n = 0; n < 6; ++n) for (int ks = 0; ks < 5; ++ks) for (int l = 0; l < 64; ++l) for (int e = 0; e < 4; ++e)
    T.i[n][ks][l][e] = bfbits(m80(16 * ks + 4 * (l >> 4) + e, 13 * n + (n < 2 ? n : 2) + (l & 15)));
  return T;
}
}
static __device__ const tb::TabF g_TF = tb::mkTabF();
static __device__ const tb::Tab80 g_T80 = tb::mkTab80();

constexpr unsigned WS_EP = 0;
constexpr unsigned WS_F1 = 256;
constexpr unsigned WS_F2 = WS_F1 + 512;
constexpr unsigned WS_FV = WS_F2 + 512;
constexpr unsigned WS_FH = 8192;
constexpr unsigned WS_FA = 4096;
constexpr unsigned WS_ST = 16384;
constexpr unsigned CP = 65536 + 256;
constexpr unsigned WS_UB = WS_ST + 2048 * 8;
constexpr unsigned WS_WT = WS_UB + 40 * CP;
constexpr unsigned WS_AF = WS_WT + 41 * CP;
constexpr unsigned WS_BF = WS_AF + 32 * 48 * 4096;
constexpr unsigned VP = 8192 + 256;
constexpr unsigned WS_VT = WS_BF + 6 * 48 * 7168;
constexpr unsigned WS_END = WS_VT + 328 * VP;

#if defined(__HIP_DEVICE_COMPILE__)
#pragma clang attribute push(__attribute__((target("no-packed-fp32-ops"))), apply_to = function)
#endif
DI float u2f(unsigned u) { return __builtin_bit_cast(float, u); }
DI unsigned f2u(float f) { return __builtin_bit_cast(unsigned, f); }
DI bool all_lanes(bool p) { return __builtin_amdgcn_ballot_w64(!p) == 0; }
DI int s_of_rho(int rho) {
  const int t = rho >> 10, ks = (rho >> 9) & 1, h = (rho >> 8) & 1, r = (rho >> 3) & 31, jh = (rho >> 2) & 1, jl = rho & 3;
  return 64 * (16 * ks + 8 * jh + 4 * h + jl) + 32 * t + r;
}
DI float bf_lo(unsigned u) { return u2f(u << 16); }
DI float bf_hi(unsigned u) { return u2f(u & 0xffff0000u); }
DI unsigned pk_bf(float a, float b) { f32x2 f = {a, b}; return __builtin_bit_cast(unsigned, __builtin_convertvector(f, bf16x2)); }
DI unsigned pk_h(float a, float b) { f32x2 f = {a, b}; return __builtin_bit_cast(unsigned, __builtin_convertvector(f, f16x2)); }
DI float rnd_bf(float a) { return bf_lo(pk_bf(a, 0.f)); }
#define MFMA16_BF(a, b, c) __builtin_amdgcn_mfma_f32_16x16x32_bf16(__builtin_bit_cast(bf16x8, a), __builtin_bit_cast(bf16x8, b), c, 0, 0, 0)
#define MFMA32_BF(a, b, c) __builtin_amdgcn_mfma_f32_32x32x16_bf16(__builtin_bit_cast(bf16x8, a), __builtin_bit_cast(bf16x8, b), c, 0, 0, 0)
#define MFMA32_H(a, b, c) __builtin_amdgcn_mfma_f32_32x32x16_f16(__builtin_bit_cast(f16x8, a), __builtin_bit_cast(f16x8, b), c, 0, 0, 0)
#define MFMA16K16_H(a, b, c) __builtin_amdgcn_mfma_f32_16x16x16f16(__builtin_bit_cast(f16x4, a), __builtin_bit_cast(f16x4, b), c, 0, 0, 0)
#define MFMA16K16_BF(a, b, c) __builtin_amdgcn_mfma_f32_16x16x16bf16_1k(__builtin_bit_cast(s16x4, a), __builtin_bit_cast(s16x4, b), c, 0, 0, 0)
DI u32x2 tr_read(const char* p) {
  s16x4 v = __builtin_amdgcn_ds_read_tr16_b64_v4i16((LDS_AS s16x4*)(p));
  return __builtin_bit_cast(u32x2, v);
}
DI rsrc_t mk_rsrc(const void* p, unsigned bytes) { return __builtin_amdgcn_make_buffer_rsrc((void*)p, 0, bytes, 0x00020000); }
#define ST128(v, r, off) __builtin_amdgcn_raw_buffer_store_b128(v, r, off, 0, 16)
#define ST64(v, r, off) __builtin_amdgcn_raw_buffer_store_b64(v, r, off, 0, 16)
#define WAIT_V(n) asm volatile("s_waitcnt vmcnt(%0)" ::"n"(n) : "memory")
#define WAIT_L(n) asm volatile("s_waitcnt lgkmcnt(%0)" ::"n"(n) : "memory")
#define SCHED() __builtin_amdgcn_sched_barrier(0)
#define BARRIER() do { WAIT_L(0); __builtin_amdgcn_s_barrier(); asm volatile("" ::: "memory"); } while (0)
DI void dma1(unsigned voff, const void* sbase, unsigned lds) {
  asm volatile("s_mov_b32 m0, %2\n\ts_nop 3\n\tglobal_load_lds_dwordx4 %0, %1" ::"v"(voff), "s"(sbase), "s"(lds) : "memory");
}
template <int CTRL> DI unsigned dpp_u(unsigned x) { return (unsigned)__builtin_amdgcn_mov_dpp((int)x, CTRL, 0xf, 0xf, true); }
template <int CTRL> DI float dpp_f(float x) { return u2f(dpp_u<CTRL>(f2u(x))); }
DI float sum16(float x) {
  x += dpp_f<0xB1>(x); x += dpp_f<0x4E>(x); x += dpp_f<0x141>(x); x += dpp_f<0x128>(x);
  return x;
}
DI float sum_rows(float x) {
  const auto s = __builtin_amdgcn_permlane16_swap(f2u(x), f2u(x), false, false);
  x = u2f(s[0]) + u2f(s[1]);
  const auto t = __builtin_amdgcn_permlane32_swap(f2u(x), f2u(x), false, false);
  return u2f(t[0]) + u2f(t[1]);
}
typedef unsigned long long u64;
typedef __attribute__((address_space(1))) u64 gu64;
#define RLX_AGENT __ATOMIC_RELAXED, __HIP_MEMORY_SCOPE_AGENT
DI u64 ld_dev(const char* p) { return __hip_atomic_load((gu64*)p, RLX_AGENT); }
DI void st_dev(char* p, u64 v) { __hip_atomic_store((gu64*)p, v, RLX_AGENT); }
DI u64 next_epoch(u64 e) { return e * 0x9E3779B97F4A7C15ull + 0x632BE59BD9B4E019ull; }
#define SLP 1
constexpr unsigned SPIN_CAP = 1u << 18;
DI u64 wait_flags(const char* p, u64 tag) {
  u64 v = 0;
#pragma unroll 1
  for (unsigned n = 0; n < SPIN_CAP; ++n) {
    v = ld_dev(p);
    if (all_lanes((v >> 16) == (tag >> 16))) break;
    __builtin_amdgcn_s_sleep(SLP);
  }
  asm volatile("" ::: "memory");
  return v;
}
#define LD128_DEV(r, off) __builtin_amdgcn_raw_buffer_load_b128(r, off, 0, 16)
#define LD128_DEV2(r, off, soff) __builtin_amdgcn_raw_buffer_load_b128(r, off, soff, 16)
#define LD64_DEV(r, off) __builtin_amdgcn_raw_buffer_load_b64(r, off, 0, 16)
DI unsigned max64(unsigned x) {
  x = max(x, dpp_u<0xB1>(x)); x = max(x, dpp_u<0x4E>(x)); x = max(x, dpp_u<0x141>(x)); x = max(x, dpp_u<0x128>(x));
  const auto s = __builtin_amdgcn_permlane16_swap(x, x, false, false);
  x = max(s[0], s[1]);
  const auto t = __builtin_amdgcn_permlane32_swap(x, x, false, false);
  return max(t[0], t[1]);
}
DI unsigned lds_addr(const char* p) { return (unsigned)(size_t)(LDS_AS const char*)p; }

#define TS_DECL unsigned long long t0_ = __builtin_amdgcn_s_memrealtime(), tm_ = t0_; int tsn_ = 1;
#define TS_MARK() do { if (tsn_ == MK) { SCHED(); tm_ = __builtin_amdgcn_s_memrealtime(); __builtin_amdgcn_s_waitcnt(0xC07F); SCHED(); } ++tsn_; } while (0)
#define TS_SET(n) tsn_ = n
#define TS_END(k, role) do { if (role == MR) { WAIT_V(0); TS_MARK(); if (threadIdx.x < 64) { for (int i_ = 0; i_ < 100000 && __builtin_amdgcn_s_memrealtime() - tm_ < 3000; ++i_) __builtin_amdgcn_s_sleep(1); } } } while (0)

#define ZJ 22
#define OUTAUX 16
#define N5 3
#define N8 2
#define NPIN 8
#define CPF 290
#define EPF 1
#define EIL 3
#define PRIO 0
#define ENTRY 1
#define PF0 0
#define A1LATE
#define FEWB 0
#define PFD 0
constexpr int STAGE_B = 49152;
constexpr int G_CS = 3 * STAGE_B;
constexpr int G_ST = G_CS + 512;
constexpr int G_PF = G_ST + 2048;
constexpr int G_TAG = G_PF + 1024;
constexpr int G_SN = G_TAG + 1024;
constexpr int A_TF = 0;
constexpr int A_T80 = 16384;
constexpr int A_XH = 32768;
constexpr int A_UT = A_XH + 32 * 1344;
constexpr int A_W = A_UT + 4 * 10304;
constexpr int A_MX = A_W + 8 * 5152;
constexpr int A_E = 16384;
constexpr int KA_LDS = A_MX + 256;
static_assert(G_SN + 6144 <= KA_LDS && KA_LDS <= 163840, "lds");

DI void pack_h(const f32x16& v, u32x4 (&o)[2]) {
#pragma unroll
  for (int s = 0; s < 2; ++s)
#pragma unroll
    for (int j = 0; j < 4; ++j) o[s][j] = pk_h(v[8 * s + 2 * j], v[8 * s + 2 * j + 1]);
}
DI f16x2 as_h2(unsigned u) { return __builtin_bit_cast(f16x2, u); }
DI unsigned as_u(f16x2 v) { return __builtin_bit_cast(unsigned, v); }
template <bool INV> DI void bfly_tw(const f32x16 (&dr)[2], const f32x16 (&di)[2], const f16x2 (&tc)[8], const f16x2 (&ts)[8],
                                    u32x4 (&er)[2][2], u32x4 (&ei)[2][2]) {
#pragma unroll
  for (int e = 0; e < 8; ++e) {
    const f16x2 r0 = as_h2(pk_h(dr[0][2 * e], dr[0][2 * e + 1])), r1 = as_h2(pk_h(dr[1][2 * e], dr[1][2 * e + 1]));
    const f16x2 i0 = as_h2(pk_h(di[0][2 * e], di[0][2 * e + 1])), i1 = as_h2(pk_h(di[1][2 * e], di[1][2 * e + 1]));
#pragma unroll
    for (int t = 0; t < 2; ++t) {
      const f16x2 sr = t ? r0 - r1 : r0 + r1, si = t ? i0 - i1 : i0 + i1;
      const f16x2 a = sr * tc[e], c = si * tc[e];
      er[t][e >> 2][e & 3] = as_u(__builtin_elementwise_fma(INV ? -si : si, ts[e], a));
      ei[t][e >> 2][e & 3] = as_u(__builtin_elementwise_fma(INV ? sr : -sr, ts[e], c));
    }
  }
}
DI u32x4 neg_h(u32x4 v) { return v ^ 0x80008000u; }
DI u32x4 swap_halves(u32x4 v) { return (u32x4){v[2], v[3], v[0], v[1]}; }
DI int e_key(int d2) { return ((d2 & 2) << 2) | ((d2 & 1) << 2) | ((d2 & 4) >> 1) | ((d2 & 2) >> 1); }

__global__ __launch_bounds__(512, 2) void ka(const u16* __restrict__ x, const u16* __restrict__ w_r, const u16* __restrict__ w_i,
                                             const u16* __restrict__ wg, const u16* __restrict__ wu, const u16* __restrict__ wd,
                                             char* __restrict__ ws, u16* __restrict__ out) {
  __shared__ __attribute__((aligned(1024))) char sm[KA_LDS];
  u64 pc0;
  asm volatile("s_getpc_b64 %0" : "=s"(pc0));
  pc0 &= ~255ull;
  const int b = blockIdx.x, tid = threadIdx.x, l = tid & 63;
  const int w = __builtin_amdgcn_readfirstlane(tid >> 6);
  const int xcd = b & 7, idx = b >> 3;
  const unsigned smb = lds_addr(sm);
  const rsrc_t rw = mk_rsrc(ws, WS_END);
  const int g = l >> 4;
  TS_DECL
#define PREFETCH()                                                                                                    \
  do {                                                                                                                \
    constexpr int NP_ = (640 - PF0) * 6 / 768;                                                                        \
    const int p0_ = (((idx - 8) * 8 + xcd) * 2 + (w & 1)) * NP_;                                                      \
    _Pragma("unroll") for (int i_ = 0; i_ < NP_; ++i_)                                                                \
      dma1(l * 16 + (p0_ + i_) * 1024 + PF0 * 3072, (w & 2) ? wu : wg, smb + G_PF);                                   \
  } while (0)
  if (idx >= 8) {
    const int pn = (xcd >> 2) * 4 + (idx & 3), f0 = (xcd & 3) * 6 + ((idx - 8) >> 2), r8 = l >> 3, c8 = l & 7;
    const unsigned xo = (s_of_rho(pn * 256 + w) + r8) * 1280 + ((c8 ^ ((r8 >> 1) | (w & 4))) << 4);
    const unsigned wo = ((8 * w + r8) * 1536 + f0 * 64) * 2 + ((c8 ^ ((((r8 >> 1) & 1) | ((w & 1) << 1)) << 1)) << 4);
    dma1(wo, wg, smb + 32768 + w * 1024);
    dma1(wo, wu, smb + 40960 + w * 1024);
#pragma unroll
    for (int i = 0; i < 4; ++i) dma1(xo, (const char*)x + i * 10240, smb + w * 4096 + i * 1024);
    if (w < 4) PREFETCH();
  }
  asm volatile("" ::: "memory");
  if (idx >= 8) {
    const int gi = idx - 8;
    const int panel = (xcd >> 2) * 4 + (gi & 3), ft = (xcd & 3) * 6 + (gi >> 2);
    const int gwg = gi * 8 + xcd;
    const int p3 = xcd * 4 + (gi & 3), ct = gi >> 2, d1s = 13 * ct + (ct < 2 ? ct : 2), nd1 = ct < 2 ? 14 : 13;
    const int i16 = l & 15, kh = w >> 2, c0 = 2 * (w & 3) + kh;
    constexpr int NB = 18;
    u32x4 B[NB][2];
    const unsigned bl = i16 < 12 ? g * 192 + i16 * 16 : 768 + g * 32 + (i16 & 1) * 16;
    const unsigned bo = WS_BF + (ct * 48 + kh) * 7168 + bl;
#define LOADB(j) do { B[(j) % NB][0] = LD128_DEV2(rw, bo + c0 * 896, (j) * 14336); B[(j) % NB][1] = LD128_DEV2(rw, bo + (c0 ^ 1) * 896, (j) * 14336); } while (0)
    if (w >= 4) {
      const int lw = w - 4, r8 = l >> 3, c8 = l & 7;
      asm volatile("s_mov_b32 m0, %2\n\ts_nop 3\n\tglobal_load_lds_dword %0, %1 sc1" ::"v"(WS_EP + (l & 1) * 4), "s"(ws), "s"(smb + G_TAG + lw * 256) : "memory");
      unsigned xo[2], woff[2];
#pragma unroll
      for (int v = 0; v < 2; ++v) {
        xo[v] = (s_of_rho(panel * 256 + 2 * lw + v) + r8) * 1280 + ((c8 ^ ((r8 >> 1) | ((lw >> 1) << 2))) << 4);
        woff[v] = ((16 * lw + 8 * v + r8) * 1536 + ft * 64) * 2 + ((c8 ^ ((((r8 >> 1) & 1) | (v << 1)) << 1)) << 4);
      }
      const char *xs = (const char*)x + ENTRY * 128, *gs = (const char*)wg + ENTRY * 64 * 3072, *us = (const char*)wu + ENTRY * 64 * 3072;
      const unsigned dx = smb + lw * 8192, dw = smb + 32768 + lw * 2048;
#define STAGE(off)                                                                                   \
  do {                                                                                               \
    _Pragma("unroll") for (int v_ = 0; v_ < 2; ++v_) {                                               \
      dma1(woff[v_], gs, dw + (off) + v_ * 1024);                                                    \
      dma1(woff[v_], us, dw + (off) + 8192 + v_ * 1024);                                             \
    }                                                                                                \
    _Pragma("unroll") for (int i_ = 0; i_ < 8; ++i_) dma1(xo[i_ >> 2], xs + (i_ & 3) * 10240, dx + (off) + i_ * 1024); \
    xs += 128; gs += 64 * 3072; us += 64 * 3072;                                                     \
  } while (0)
      STAGE(STAGE_B);
      WAIT_V(12);
      __builtin_amdgcn_s_barrier();
      const u64 tag = next_epoch(*(const u64*)(sm + G_TAG + lw * 256));
      const unsigned fao = WS_FA + ((ct * 48 + (l < 48 ? l : 0)) % 192) * 16;
      const int job = gwg + 192 * lw;
      if (job < 288) {
        const int jc = job / 48, F = job - jc * 48, lg = l < 48 ? l / 12 : (l >> 1) & 3;
        const int d1 = min(13 * jc + (jc < 2 ? jc : 2) + (l < 48 ? l - lg * 12 : 12 + (l & 1)), 79);
        const unsigned d0 = (32 * F + 4 * lg) * 1280 + d1 * 16, d16 = d0 + 16 * 1280;
        u32x4 dv[8];
        asm volatile("global_load_dwordx4 %0, %8, %10\n\tglobal_load_dwordx4 %1, %8, %10 offset:1280\n\tglobal_load_dwordx4 %2, %8, %10 offset:2560\n\t"
                     "global_load_dwordx4 %3, %8, %10 offset:3840\n\tglobal_load_dwordx4 %4, %9, %10\n\tglobal_load_dwordx4 %5, %9, %10 offset:1280\n\t"
                     "global_load_dwordx4 %6, %9, %10 offset:2560\n\tglobal_load_dwordx4 %7, %9, %10 offset:3840"
                     : "=&v"(dv[0]), "=&v"(dv[1]), "=&v"(dv[2]), "=&v"(dv[3]), "=&v"(dv[4]), "=&v"(dv[5]), "=&v"(dv[6]), "=&v"(dv[7])
                     : "v"(d0), "v"(d16), "s"(wd) : "memory");
        STAGE(2 * STAGE_B);
        WAIT_V(20);
        __builtin_amdgcn_s_barrier();
        STAGE(0);
        asm volatile("s_waitcnt vmcnt(12)" : "+v"(dv[0]), "+v"(dv[1]), "+v"(dv[2]), "+v"(dv[3]), "+v"(dv[4]), "+v"(dv[5]), "+v"(dv[6]), "+v"(dv[7])::"memory");
        if (l < 56) {
#pragma unroll
          for (int c = 0; c < 8; ++c) {
            u32x4 o;
#pragma unroll
            for (int j = 0; j < 4; ++j) o[j] = __builtin_amdgcn_perm(dv[2 * j + 1][c >> 1], dv[2 * j][c >> 1], (c & 1) ? 0x07060302u : 0x05040100u);
            ST128(o, rw, WS_BF + job * 7168 + c * 896 + l * 16);
          }
        }
      } else {
        STAGE(2 * STAGE_B);
        WAIT_V(12);
        __builtin_amdgcn_s_barrier();
        STAGE(0);
        WAIT_V(12);
      }
      __builtin_amdgcn_s_barrier();
      STAGE(STAGE_B);
      WAIT_V(12);
      __builtin_amdgcn_s_barrier();
      if (tid == 256) st_dev(ws + WS_FA + gwg * 16, tag);
      STAGE(2 * STAGE_B);
      WAIT_V(12);
      __builtin_amdgcn_s_barrier();
      STAGE(0);
      WAIT_V(12);
      __builtin_amdgcn_s_barrier();
      asm volatile("s_mov_b32 m0, %2\n\ts_nop 3\n\tglobal_load_lds_dwordx4 %0, %1 sc1" ::"v"(fao), "s"(ws), "s"(smb + G_SN + lw * 1536) : "memory");
      if (l < 32)
        asm volatile("s_mov_b32 m0, %2\n\ts_nop 3\n\tglobal_load_lds_dwordx4 %0, %1 sc1" ::"v"(WS_ST + (panel * 256 + lw * 64) * 8 + l * 16), "s"(ws),
                     "s"(smb + G_SN + lw * 1536 + 1024) : "memory");
      STAGE(STAGE_B);
      WAIT_V(12);
      __builtin_amdgcn_s_barrier();
      STAGE(2 * STAGE_B);
      {
        u64 st = *(const u64*)(sm + G_SN + lw * 1536 + 1024 + l * 8);
        const u64 fa = *(const u64*)(sm + G_SN + lw * 1536 + l * 16);
        if (!all_lanes(fa == tag && (unsigned)(st >> 32) == (unsigned)tag)) {
          wait_flags(ws + fao, tag);
#pragma unroll 1
          for (unsigned n = 0; n < SPIN_CAP; ++n) {
            st = ld_dev(ws + WS_ST + (panel * 256 + tid - 256) * 8);
            if (all_lanes((unsigned)(st >> 32) == (unsigned)tag)) break;
            __builtin_amdgcn_s_sleep(4);
          }
        }
        asm volatile("" ::: "memory");
        const float rs = __builtin_amdgcn_rcpf(bf_hi((unsigned)st));
        *(f32x2*)(sm + G_ST + (tid - 256) * 8) = (f32x2){u2f((((unsigned)st & 0xffffu) ^ 0x8000u) * 0x10001u), rs * rs};
      }
#pragma unroll
      for (int j = 0; j < N5; ++j) LOADB(j);
      SCHED();
      WAIT_V(12 + 2 * N5);
      BARRIER();
      STAGE(0);
      SCHED();
#pragma unroll
      for (int j = N5; j < N5 + 3; ++j) LOADB(j);
      SCHED();
      WAIT_V(18 + 2 * N5);
#define JB (N5 + 3)
#undef STAGE
      __builtin_amdgcn_s_barrier();
      const char* bsrc = ws + WS_BF + ct * 48 * 7168 + 2 * lw * 896;
#define STAGEB(j0)                                                                                                    \
  do {                                                                                                                \
    _Pragma("unroll") for (int i_ = (j0); i_ < (j0) + 6; ++i_)                                                        \
      asm volatile("s_mov_b32 m0, %3\n\ts_nop 0\n\tglobal_load_lds_dwordx4 %0, %2 sc1\n\tglobal_load_lds_dwordx4 %1, %2 offset:1024 sc1" \
                   ::"v"(bl + 128), "v"(bl), "s"(bsrc - 128 + i_ * 14336), "s"(smb + i_ * 8192 + lw * 2048) : "memory"); \
  } while (0)
      STAGEB(6);
      LOADB(JB); LOADB(JB + 1); LOADB(JB + 2);
      SCHED();
      WAIT_V(24);
      __builtin_amdgcn_s_barrier();
      STAGEB(12);
#pragma unroll
      for (int j = JB + 3; j < JB + 3 + N8; ++j) LOADB(j);
      SCHED();
      __builtin_amdgcn_s_barrier();
      STAGEB(0);
#undef STAGEB
      WAIT_V(0);
      __builtin_amdgcn_s_barrier();
#pragma unroll
      for (int j = JB + 3 + N8; j < 18; ++j) LOADB(j);
    } else {
    if (w == 3 && gi < 5) {
      asm volatile("s_mov_b32 m0, %2\n\ts_nop 3\n\tglobal_load_lds_dword %0, %1" ::"v"(min(gi * 64 + l, CPF - 1) * 128), "s"(pc0),
                   "s"(smb + A_MX) : "memory");
    }
    const int wr = w >> 1, fh = w & 1;
    {
    const int q = (l & 15) >> 2;
    const int xb0 = (i16 >> 3) * 16384 + (wr * 16 + (l & 7)) * 128 + ((g ^ (((l & 7) >> 1) | ((i16 >> 3) << 2))) << 4);
    const int wb0 = (32768 + (8 * g + q) * 128 + (((fh * 2) ^ (((q >> 1) & 1) | ((g & 1) << 1))) << 5) + (l & 3) * 8) ^ (wr << 13);
    const char* xr[2];
    const char* wp[4];
#define SET_BUF(off)                                                                                       \
  do {                                                                                                     \
    xr[0] = sm + (off) + xb0; xr[1] = sm + (off) + (xb0 ^ 64);                                             \
    _Pragma("unroll") for (int i_ = 0; i_ < 4; ++i_) wp[i_] = sm + (off) + (wb0 ^ ((i_ & 1) << 5) ^ ((i_ >> 1) << 13)); \
  } while (0)
    f32x4 acc[4][8];
#pragma unroll
    for (int i = 0; i < 4; ++i)
#pragma unroll
      for (int r = 0; r < 8; ++r) acc[i][r] = (f32x4){0.f, 0.f, 0.f, 0.f};
    f32x4 cs[2] = {{0.f, 0.f, 0.f, 0.f}, {0.f, 0.f, 0.f, 0.f}};
    u32x4 ones = {0x3f803f80u, 0x3f803f80u, 0x3f803f80u, 0x3f803f80u};
    asm volatile("" : "+v"(ones));
    u32x4 xf[2][8];
    u32x2 wf[2][8];
#define RD(ks, n)                                                                                          \
  do {                                                                                                     \
    if ((n) >= 2 && (n) < 10) xf[ks][(n) - 2] = *(const u32x4*)(xr[ks] + (((n) - 2) & 3) * 4096 + (((n) - 2) >> 2) * 1024); \
    else { const int m_ = (n) < 2 ? (n) : (n) - 8; wf[ks][m_] = tr_read(wp[m_ >> 1] + (ks) * 4096 + (m_ & 1) * 512); } \
  } while (0)
#define WF(ks, i) ((u32x4){wf[ks][2 * (i)][0], wf[ks][2 * (i)][1], wf[ks][2 * (i) + 1][0], wf[ks][2 * (i) + 1][1]})
#define KSTEP(ks, READS, CSUM)                                                                             \
  do {                                                                                                     \
    _Pragma("unroll") for (int n_ = 0; n_ < 32; ++n_) {                                                    \
      acc[n_ >> 3][n_ & 7] = MFMA16_BF(WF(ks, n_ >> 3), xf[ks][n_ & 7], acc[n_ >> 3][n_ & 7]);             \
      if (READS && n_ < 16) RD((ks) ^ 1, n_);                                                              \
      if (CSUM && n_ >= 30) cs[n_ & 1] = MFMA16_BF(WF(ks, n_ & 1), ones, cs[n_ & 1]);                      \
      SCHED();                                                                                             \
    }                                                                                                      \
  } while (0)
    SET_BUF(0);
    TS_MARK();
    WAIT_V((640 - PF0) * 6 / 768);
    BARRIER();
    TS_MARK();
#pragma unroll
    for (int n = 0; n < 16; ++n) RD(0, n);
    SCHED();
    KSTEP(0, true, 1);
    int nxt = STAGE_B;
#pragma unroll 1
    for (int u = 0; u < 9; ++u) {
      BARRIER();
      SET_BUF(nxt);
      SCHED();
      KSTEP(1, true, 1);
      KSTEP(0, true, 1);
      nxt = nxt == 2 * STAGE_B ? 0 : nxt + STAGE_B;
    }
    TS_MARK();
#pragma unroll
    for (int i = 0; i < 2; ++i) {
      cs[i] = MFMA16_BF(WF(1, i), ones, cs[i]);
      u32x4 hl;
#pragma unroll
      for (int e = 0; e < 4; ++e) { const float hi = rnd_bf(cs[i][e]); hl[e] = pk_bf(hi, cs[i][e] - hi); }
      if (i16 == 0) *(u32x4*)(sm + G_CS + w * 128 + i * 64 + g * 16) = hl;
    }
    BARRIER();
    unsigned ca[4];
    f32x4 st[4];
#pragma unroll
    for (int i = 0; i < 4; ++i) ca[i] = *(const unsigned*)(sm + G_CS + (w ^ (i & 2)) * 128 + (i & 1) * 64 + i16 * 4);
#pragma unroll
    for (int r = 0; r < 4; ++r) st[r] = *(const f32x4*)(sm + G_ST + (wr * 128 + (r >> 1) * 64 + 8 * (l & 7) + 4 * (i16 >> 3) + (r & 1) * 2) * 8);
    SCHED();
    TS_MARK();
    const unsigned ao = WS_AF + ((panel * 4 + wr * 2) * 48 + ft * 2 + fh) * 4096 + l * 16;
#pragma unroll
    for (int r = 0; r < 10; ++r) {
      if (r < 8) {
#pragma unroll
        for (int i = 0; i < 4; ++i) acc[i][r] = MFMA16_BF(WF(1, i), xf[1][r], acc[i][r]);
      }
      if (r >= 1 && r < 9) {
        const int e = r - 1;
        const u32x2 bm = {g == 0 ? f2u(st[e >> 1][(e & 1) * 2]) : 0u, 0u};
#pragma unroll
        for (int i = 0; i < 4; ++i) acc[i][e] = MFMA16K16_BF(((u32x2){ca[i], 0u}), bm, acc[i][e]);
      }
      if (r >= 2) {
        const int e = r - 2;
        const float r2 = st[e >> 1][(e & 1) * 2 + 1];
        u32x4 o;
#pragma unroll
        for (int i = 0; i < 2; ++i) {
          const f32x4 hv = (acc[i][e] * r2) * acc[2 + i][e];
          o[2 * i] = pk_bf(hv[0], hv[1]); o[2 * i + 1] = pk_bf(hv[2], hv[3]);
        }
        ST128(o, rw, ao + (e >> 2) * 48 * 4096 + (e & 3) * 1024);
      }
#pragma unroll
      for (int i = 0; i < 8; ++i) { __builtin_amdgcn_sched_group_barrier(0x8, 1, 0); __builtin_amdgcn_sched_group_barrier(0x2, EIL, 0); }
      SCHED();
    }
#undef SET_BUF
#undef RD
#undef WF
#undef KSTEP
    }
    SCHED();
    TS_MARK();
    const u64 tag = next_epoch(*(const u64*)(sm + G_TAG));
    WAIT_V(0);
    if (l == 0) st_dev(ws + WS_FH + ((panel * 2 + wr) * 64 + ft * 2 + fh) * 8, tag);
    __builtin_amdgcn_s_barrier();
    asm volatile("" ::: "memory");
    TS_MARK();
    SCHED();
#pragma unroll
    for (int j = 0; j < 18; ++j)
#pragma unroll
      for (int i = 0; i < 2; ++i) B[j][i] = *(const u32x4*)(sm + j * 8192 + (2 * w + i) * 1024 + l * 16);
    SCHED();
    if (w == 0) wait_flags(ws + WS_FH + ((p3 >> 1) * 64 + (l < 48 ? l : 0)) * 8, tag);
    }
#pragma unroll
    for (int j = 0; j < NPIN; ++j) asm volatile("" : "+v"(B[j][0]), "+v"(B[j][1]));
    SCHED();
    TS_SET(7);
    BARRIER();
    asm volatile("" ::: "memory");
    TS_MARK();
    const char* asrc = ws + WS_AF + p3 * 48 * 4096;
#define LOADA(ch, st)                                                                                                 \
  asm volatile("s_mov_b32 m0, %2\n\ts_nop 3\n\tglobal_load_lds_dwordx4 %0, %1 sc1\n\tglobal_load_lds_dwordx4 %0, %1 offset:1024 sc1\n\t" \
               "global_load_lds_dwordx4 %0, %1 offset:2048 sc1\n\tglobal_load_lds_dwordx4 %0, %1 offset:3072 sc1" ::"v"(l * 16), \
               "s"(asrc + (ch) * 32768 + (st) * 4096), "s"(smb + ((ch) & 3) * 32768 + (st) * 4096) : "memory")
#define LOADA2(ch) do { if (kh == (((ch) & 1) ^ 1)) { LOADA(ch, w & 3); LOADA(ch, (w & 3) + 4); } } while (0)
    LOADA(0, w);
    const u64 tag = next_epoch(*(const u64*)(sm + G_TAG));
    f32x4 acc[4][2];
#pragma unroll
    for (int m = 0; m < 4; ++m) { acc[m][0] = (f32x4){0.f, 0.f, 0.f, 0.f}; acc[m][1] = acc[m][0]; }
    const LDS_AS char* ap[2];
#pragma unroll
    for (int i = 0; i < 2; ++i) { ap[i] = (const LDS_AS char*)sm + i * 65536 + kh * 4096 + l * 16; asm volatile("" : "+v"(ap[i])); }
    u32x4 A[3][4], V[11], xres[2];
    u32x2 mi[5];
    unsigned orow[2];
    u64 fv = 0;
    const unsigned vo = WS_VT + (c0 * 41 + 2 * g) * VP + (p3 >> 3) * 2048 + ((l >> 3) & 1) * 1024 + ((p3 & 7) * 8 + (l & 7)) * 16;
#define LOADV()                                                                                                       \
  do {                                                                                                                \
    _Pragma("unroll") for (int u = 0; u < 10; ++u) V[u] = LD128_DEV2(rw, vo, ((u >> 1) * 8 + (u & 1)) * VP);         \
    V[10] = LD128_DEV2(rw, vo - 2 * g * VP, 40 * VP);                                                                 \
  } while (0)
    bool early = false;
    if (kh) { LOADA(1, w & 3); LOADA(1, (w & 3) + 4); WAIT_V(8); } else WAIT_V(0);
    BARRIER();
    TS_MARK();
#define ZPART()                                                                                                       \
  do {                                                                                                                \
    if (g == 0) {                                                                                                     \
      _Pragma("unroll") for (int m = 0; m < 4; ++m) V[0][m] = (V[0][m] & 0xffffu) | (V[10][m] << 16);     \
    }                                                                                                                 \
    _Pragma("unroll") for (int ks = 0; ks < 5; ++ks)                                                                  \
      _Pragma("unroll") for (int m = 0; m < 4; ++m)                                                                   \
        acc[m][0] = MFMA16K16_BF(((u32x2){V[2 * ks][m], V[2 * ks + 1][m]}), mi[ks], acc[m][0]);                       \
  } while (0)
#pragma unroll
    for (int m = 0; m < 8; ++m) A[m >> 2][m & 3] = *(const LDS_AS u32x4*)(ap[0] + (m >> 2) * 8192 + (m & 3) * 1024);
    SCHED();
    const rsrc_t rx = mk_rsrc(x, 2048 * 640 * 2);
#pragma unroll
    for (int j = 0; j < 24; ++j) {
      if (j == 2 || j == 6 || j == 14 || (FEWB == 0 && (j == 10 || j == 18)) || (FEWB == 1 && j == 10)) {
        if (j == 2) WAIT_V(4); else if (j == 6) WAIT_V(FEWB == 2 ? 4 : 8); else if (j == 10) WAIT_V(14); else if (j == 14) WAIT_V(10);
        BARRIER();
        if (j == 14) { early = all_lanes(fv == tag); asm volatile("" ::: "memory"); }
      }
#pragma unroll
      for (int n = 0; n < 8; ++n) {
        acc[n & 3][n >> 2] = MFMA16_BF(A[j % 3][n & 3], B[j % NB][n >> 2], acc[n & 3][n >> 2]);
        if (n < 4 && j < 22) A[(j + 2) % 3][n] = *(const LDS_AS u32x4*)(ap[((j + 2) >> 3) & 1] + ((j + 2) & 7) * 8192 + n * 1024);
        if (n == 2 || n == 6) {
          const int q = 2 * j + (n >> 2);
          if (q < 16)
            asm volatile("s_mov_b32 m0, %2\n\ts_nop 0\n\tglobal_load_lds_dwordx4 %0, %1 offset:%3 sc1" ::"v"(l * 16), "s"(asrc + (2 + (q >> 2)) * 32768 + w * 4096),
                         "s"(smb + ((2 + (q >> 2)) & 3) * 32768 + w * 4096), "n"((q & 3) * 1024) : "memory");
          else if (q < 28) B[j - 8][q & 1] = LD128_DEV2(rw, bo + (c0 ^ (q & 1)) * 896, (j + 10) * 14336);
          else if (q < 38) V[q - 28] = LD128_DEV2(rw, vo, (((q - 28) >> 1) * 8 + (q & 1)) * VP);
          else if (q == 38) V[10] = LD128_DEV2(rw, vo - 2 * g * VP, 40 * VP);
        }
        if (n == 4) {
          if (j == 8 || j == 9) {
            orow[j - 8] = s_of_rho(p3 * 64 + 8 * ((4 * g + 2 * kh + j - 8) & 7) + 4 * (g >> 1) + (w & 3)) * 1280 + min(d1s + i16, 79) * 16;
            xres[j - 8] = __builtin_amdgcn_raw_buffer_load_b128(rx, orow[j - 8], 0, 0);
          }
          if (j == 10) fv = ld_dev(ws + WS_FV + (l < 41 ? l : 0) * 8);
          if (j >= 11 && j < 16) mi[j - 11] = *(const u32x2*)&g_T80.i[ct][j - 11][l][0];
        }
        SCHED();
      }
      if (j == ZJ) {
        if (!early) {
          wait_flags(ws + WS_FV + (l < 41 ? l : 0) * 8, tag);
          LOADV();
        }
#pragma unroll
        for (int u = 0; u < 11; ++u) asm volatile("" : "+v"(V[u]));
        ZPART();
      }
      SCHED();
      if (j == 11) TS_MARK();
    }
#undef LOADA
#undef LOADA2
#undef LOADB
    TS_MARK();
#undef LOADV
#undef ZPART
    char* tp = sm + 65536 + kh * 16384 + l * 8;
#pragma unroll
    for (int m = 0; m < 4; ++m)
#pragma unroll
      for (int i = 0; i < 2; ++i)
        *(u32x2*)(tp + (m * 8 + (c0 ^ i)) * 512) = (u32x2){pk_bf(acc[m][i][0], acc[m][i][1]), pk_bf(acc[m][i][2], acc[m][i][3])};
    BARRIER();
    TS_MARK();
    unsigned pp[2][8];
#pragma unroll
    for (int k = 0; k < 2; ++k)
#pragma unroll
      for (int c = 0; c < 8; ++c) pp[k][c] = *(const unsigned*)(sm + 65536 + k * 16384 + ((w & 3) * 8 + c) * 512 + l * 8 + kh * 4);
    SCHED();
    const rsrc_t ro = mk_rsrc(out, 2048 * 640 * 2);
#pragma unroll
    for (int r = 0; r < 2; ++r) {
      u32x4 o;
#pragma unroll
      for (int c = 0; c < 4; ++c) {
        const float a = r ? bf_hi(pp[0][2 * c]) + bf_hi(pp[1][2 * c]) : bf_lo(pp[0][2 * c]) + bf_lo(pp[1][2 * c]);
        const float b2 = r ? bf_hi(pp[0][2 * c + 1]) + bf_hi(pp[1][2 * c + 1]) : bf_lo(pp[0][2 * c + 1]) + bf_lo(pp[1][2 * c + 1]);
        o[c] = pk_bf(a + bf_lo(xres[r][c]), b2 + bf_hi(xres[r][c]));
      }
      if (i16 < nd1) __builtin_amdgcn_raw_buffer_store_b128(o, ro, orow[r], 0, OUTAUX);
    }
    TS_MARK();
    TS_END(0, 0);
    return;
  }
  const int aux = idx * 8 + xcd;
  u64 tag;
  f16x2 tc[8], tsn[8];
  u32x4 G1[8], G3[8];
  u32x4 wo_[3];
  u16x2 m2 = {0, 0};
  const u16* wl;
  unsigned wdst;
  int wst;
  {
    const int row = 4 * w + g, i16 = l & 15;
    dma1(l * 16, (const char*)&g_T80 + w * 2048, smb + A_T80 + w * 2048);
    dma1(l * 16, (const char*)&g_T80 + w * 2048 + 1024, smb + A_T80 + w * 2048 + 1024);
    const u32x4* xp = (const u32x4*)(x + s_of_rho(aux * 32 + row) * 640) + i16;
    u32x4 xv[5];
#pragma unroll
    for (int c = 0; c < 5; ++c) xv[c] = xp[c * 16];
    const u64 ep = ld_dev(ws + WS_EP);
    SCHED();
    {
      const float a0 = (float)(aux * w) * (1.f / 640.f), a1 = (float)(l & 31) * (1.f / 2048.f);
#pragma unroll
      for (int e = 0; e < 8; ++e) {
        const float a = a0 + a1 * (float)(2 * (e & 1) + 8 * (e >> 1) + 4 * (l >> 5)), a2 = a + a1;
        tc[e] = as_h2(pk_h(__builtin_amdgcn_cosf(a), __builtin_amdgcn_cosf(a2)));
        tsn[e] = as_h2(pk_h(__builtin_amdgcn_sinf(a), __builtin_amdgcn_sinf(a2)));
        asm volatile("" : "+v"(tc[e]), "+v"(tsn[e]));
      }
    }
    SCHED();
    WAIT_V(6);
    BARRIER();
    u32x4 fm[15];
#pragma unroll
    for (int u = 0; u < 15; ++u) fm[u] = *(const u32x4*)(sm + A_T80 + u * 1024 + l * 16);
    SCHED();
    TS_MARK();
    float f[40];
#pragma unroll
    for (int c = 0; c < 5; ++c)
#pragma unroll
      for (int j = 0; j < 4; ++j) { f[c * 8 + 2 * j] = bf_lo(xv[c][j]); f[c * 8 + 2 * j + 1] = bf_hi(xv[c][j]); }
    tag = next_epoch(ep);
    asm volatile("" ::"v"(tag), "v"(f[39]));
    SCHED();
    {
      const char* wsrc = (const char*)(w < 4 ? w_r : w_i) + (64 * (4 * (aux >> 3) + (w & 3)) + 8 * (aux & 7)) * 642;
#pragma unroll
      for (int i = 0; i < 5; ++i) dma1(l * 16, wsrc + i * 1024, smb + A_W + w * 5152 + i * 1024);
      if (l == 0) dma1(0, wsrc + 5120, smb + A_W + w * 5152 + 5120);
      dma1(l * 16, (const char*)&g_TF + w * 2048, smb + A_TF + w * 2048);
      dma1(l * 16, (const char*)&g_TF + w * 2048 + 1024, smb + A_TF + w * 2048 + 1024);
    }
    SCHED();
    float s4[4] = {0.f, 0.f, 0.f, 0.f};
#pragma unroll
    for (int e = 0; e < 40; ++e) s4[e & 3] += f[e];
    const float mean = sum16((s4[0] + s4[1]) + (s4[2] + s4[3])) * (1.f / 640.f);
    float q4[4] = {0.f, 0.f, 0.f, 0.f};
#pragma unroll
    for (int e = 0; e < 40; ++e) { f[e] -= mean; q4[e & 3] += f[e] * f[e]; }
    const float mb = rnd_bf(mean), vb = rnd_bf(sum16((q4[0] + q4[1]) + (q4[2] + q4[3])) * (1.f / 640.f));
    const float sd = rnd_bf(__builtin_amdgcn_sqrtf(rnd_bf(vb + 1e-5f)));
    const float rs = __builtin_amdgcn_rcpf(sd), off = (mean - mb) * rs;
    if (i16 == 0) st_dev(ws + WS_ST + (aux * 32 + row) * 8, (tag << 32) | (f2u(mb) >> 16) | f2u(sd));
#pragma unroll
    for (int c = 0; c < 5; ++c) {
      u32x4 oh;
#pragma unroll
      for (int j = 0; j < 4; ++j) oh[j] = pk_h(f[c * 8 + 2 * j] * rs + off, f[c * 8 + 2 * j + 1] * rs + off);
      *(u32x4*)(sm + A_XH + row * 1344 + (c * 16 + i16) * 16) = oh;
    }
    TS_MARK();
    {
      const int h = l >> 5, p = l & 3;
      const char* rd = sm + A_XH + (4 * w + 2 * (g & 1) + (p >> 1)) * 1344 + (8 * h + ((l & 15) >> 2)) * 16 + (p & 1) * 8;
      u32x4 ax[5];
#pragma unroll
      for (int ks = 0; ks < 5; ++ks) {
        const u32x2 lo = tr_read(rd + ks * 256), hi = tr_read(rd + ks * 256 + 64);
        ax[ks] = (u32x4){lo[0], lo[1], hi[0], hi[1]};
      }
      SCHED();
      const int ol = l & 31;
      char* ut = sm + A_UT + (w >> 1) * 10304 + (ol >> 1) * 256 + (w & 1) * 8;
      const int ub = (8 * h + (ol & 1)) ^ (ol & 14);
      f32x16 acc[3];
#pragma unroll
      for (int nt = 0; nt < 3; ++nt) {
#pragma unroll
        for (int e = 0; e < 16; ++e) acc[nt][e] = 0.f;
#pragma unroll
        for (int ks = 0; ks < 5; ++ks) acc[nt] = MFMA32_H(ax[ks], fm[nt * 5 + ks], acc[nt]);
      }
#pragma unroll
      for (int nt = 0; nt < 3; ++nt)
        if (nt < 2 || ol < 16) {
#pragma unroll
          for (int dl = 0; dl < 4; ++dl)
            *(u32x2*)(ut + nt * 4096 + ((ub ^ (2 * dl)) << 4)) = (u32x2){pk_h(acc[nt][dl], acc[nt][4 + dl]), pk_h(acc[nt][8 + dl], acc[nt][12 + dl])};
        }
    }
    WAIT_V(1);
    BARRIER();
    TS_MARK();
    {
      u32x4 uv[5];
#pragma unroll
      for (int i = 0; i < 5; ++i) {
        const int seg = i * 128 + (tid >> 2);
        uv[i] = *(const u32x4*)(sm + A_UT + (tid & 3) * 10304 + ((seg ^ ((seg >> 3) & 14)) << 4));
      }
      SCHED();
#pragma unroll
      for (int i = 0; i < 5; ++i) ST128(uv[i], rw, WS_UB + (i * 8 + w) * CP + aux * 1024 + l * 16);
    }
    SCHED();
    const int eq = l & 3, kb = (l >> 2) & 7, cl = 2 * (w & 3) + (l >> 5), half = w >> 2;
    int fr = 64 * (4 * (aux >> 3) + eq) + 8 * (aux & 7) + kb;
    if (half) fr = (2048 - fr) & 2047;
    wl = (const u16*)(sm + A_W) + eq * 2576 + kb * 321 + (half ? 80 - cl : cl);
    wdst = WS_WT + cl * CP + (half * 2048 + (fr >> 8) * 256 + (fr & 63) * 4 + ((fr >> 6) & 3)) * 16;
    wst = half ? -8 : 8;
#define WT_READ(i0)                                                                                                   \
  do {                                                                                                                \
    _Pragma("unroll") for (int i_ = 0; i_ < 3; ++i_)                                                                  \
      _Pragma("unroll") for (int j_ = 0; j_ < 4; ++j_)                                                                \
        wo_[i_][j_] = __builtin_bit_cast(unsigned, ((u16x2){(wl + wst * ((i0) + i_))[80 * j_], (wl + wst * ((i0) + i_))[10304 + 80 * j_]})); \
    _Pragma("unroll") for (int i_ = 0; i_ < 3; ++i_) {                                                                \
      _Pragma("unroll") for (int j_ = 0; j_ < 4; ++j_) m2 = __builtin_elementwise_max(m2, __builtin_bit_cast(u16x2, wo_[i_][j_] & 0x7fff7fffu)); \
      if (w >= 4) wo_[i_] = (u32x4){wo_[i_][3], wo_[i_][2], wo_[i_][1], wo_[i_][0]} ^ 0x80000000u;                    \
      asm volatile("" : "+v"(wo_[i_]));                                                                               \
    }                                                                                                                 \
  } while (0)
#define WT_STORE(i0)                                                                                                  \
  do {                                                                                                                \
    _Pragma("unroll") for (int i_ = 0; i_ < 3; ++i_)                                                                  \
      if ((i0) + i_ < 5 || ((w & 3) == 0 && l < 32)) ST128(wo_[i_], rw, wdst + ((i0) + i_) * 8 * CP);                 \
  } while (0)
    WT_READ(0);
    SCHED();
    TS_MARK();
    WAIT_V(0);
    __builtin_amdgcn_s_barrier();
    if (tid == 0) st_dev(ws + WS_F1 + aux * 8, tag);
    TS_MARK();
    WT_STORE(0);
    SCHED();
    WT_READ(3);
    WT_STORE(3);
    const unsigned mx = max64(max((unsigned)m2[0], (unsigned)m2[1]));
    if (l == 0) ((unsigned*)(sm + A_MX))[w] = mx;
    SCHED();
    TS_MARK();
  }
#define PUBLISH_F2()                                                                                                  \
  do {                                                                                                                \
    if (tid == 0) {                                                                                                   \
      const u32x4 m0_ = *(const u32x4*)(sm + A_MX), m1_ = *(const u32x4*)(sm + A_MX + 16);                            \
      const unsigned mx_ = max(max(max(m0_[0], m0_[1]), max(m0_[2], m0_[3])), max(max(m1_[0], m1_[1]), max(m1_[2], m1_[3]))); \
      st_dev(ws + WS_F2 + aux * 8, (tag & ~0xffffull) | mx_);                                                         \
    }                                                                                                                 \
  } while (0)
  WAIT_V(0);
  BARRIER();
  PUBLISH_F2();
  TS_MARK();
  if (aux < 41) {
    const int c1 = aux, c1s = c1 == 40 ? 0 : c1;
    const bool real_cls = c1s == 0;
    char* eb = sm + A_E;
    const u32x4 g8 = *(const u32x4*)&g_TF.g8[l][0][0];
#pragma unroll
    for (int i = 0; i < 8; ++i) { G1[i] = *(const u32x4*)(sm + A_TF + i * 1024 + l * 16); G3[i] = *(const u32x4*)(sm + A_TF + 8192 + i * 1024 + l * 16); }
    SCHED();
    wait_flags(ws + WS_F1 + l * 8, tag);
    TS_MARK();
    u32x4 ar[4], ai[4];
    {
      const unsigned uo = WS_UB + c1s * CP + (l >> 2) * 1024 + w * 128 + (l & 3) * 16;
#pragma unroll
      for (int i = 0; i < 4; ++i) {
        ar[i] = LD128_DEV(rw, uo + (c1 == 40 ? 64 : 0) + i * 16384);
        ai[i] = LD128_DEV(rw, uo + 64 + i * 16384);
      }
    }
    u64 f2 = ld_dev(ws + WS_F2 + l * 8);
#pragma unroll
    for (int i = 0; i < 8; ++i) asm volatile("" : "+v"(G1[i]), "+v"(G3[i]));
    SCHED();
    if (real_cls) {
#pragma unroll
      for (int i = 0; i < 4; ++i) ai[i] = (u32x4){0u, 0u, 0u, 0u};
    }
    char* erow = eb + (2 * w) * 4096 + ((l ^ e_key(w)) << 4);
    f32x16 zero;
#pragma unroll
    for (int e = 0; e < 16; ++e) zero[e] = 0.f;
    u32x2 wv[16];
    const unsigned wbase = WS_WT + c1 * CP + (g >> 1) * 32768 + (g & 1) * 8 + (l & 15) * 16 + ((4 * ((w >> 1) & 1) + (w & 1)) * 64 + 32 * (w >> 2)) * 64;
    bool early;
    {
      f32x16 dr[2], di[2];
#pragma unroll
      for (int t = 0; t < 2; ++t) {
        dr[t] = zero; di[t] = zero;
#pragma unroll
        for (int ks = 0; ks < 2; ++ks) {
          const u32x4 C = G1[(t * 2 + ks) * 2], S = G1[(t * 2 + ks) * 2 + 1];
          dr[t] = MFMA32_H(ar[t * 2 + ks], C, dr[t]);
          di[t] = MFMA32_H(ai[t * 2 + ks], C, di[t]);
          dr[t] = MFMA32_H(ai[t * 2 + ks], S, dr[t]);
          di[t] = MFMA32_H(neg_h(ar[t * 2 + ks]), S, di[t]);
        }
      }
      SCHED();
      TS_MARK();
      early = all_lanes((f2 >> 16) == (tag >> 16));
      asm volatile("" ::: "memory");
      if (early) {
#pragma unroll
        for (int i = 0; i < 16; ++i) wv[i] = LD64_DEV(rw, wbase + (i & 1) * 8192 + (i >> 1) * 256);
      }
      SCHED();
      u32x4 br[2][2], bi[2][2];
      bfly_tw<false>(dr, di, tc, tsn, br, bi);
#pragma unroll
      for (int t2 = 0; t2 < 2; ++t2) {
        f32x16 xr_ = zero, xi_ = zero;
#pragma unroll
        for (int ks = 0; ks < 2; ++ks) {
          const u32x4 C = G3[(t2 * 2 + ks) * 2], S = G3[(t2 * 2 + ks) * 2 + 1];
          xr_ = MFMA32_H(C, br[t2][ks], xr_);
          xi_ = MFMA32_H(C, bi[t2][ks], xi_);
          xr_ = MFMA32_H(S, bi[t2][ks], xr_);
          xi_ = MFMA32_H(S, neg_h(br[t2][ks]), xi_);
        }
        u32x4 o0[2], o1[2];
        pack_h(xr_, o0);
        pack_h(xi_, o1);
#pragma unroll
        for (int s2 = 0; s2 < 2; ++s2) {
          *(u32x4*)(erow + (t2 * 2 + s2) * 1024) = o0[s2];
          *(u32x4*)(erow + 4096 + (t2 * 2 + s2) * 1024) = swap_halves(o1[s2]);
        }
      }
    }
    SCHED();
    TS_MARK();
    if (!early) {
      f2 = wait_flags(ws + WS_F2 + l * 8, tag);
#pragma unroll
      for (int i = 0; i < 16; ++i) wv[i] = LD64_DEV(rw, wbase + (i & 1) * 8192 + (i >> 1) * 256);
    }
    float osc;
    u32x2 gi;
    {
      int ex = (int)(max64((unsigned)f2 & 0xffffu) >> 7);
      ex = ex < 8 ? 8 : (ex > 240 ? 240 : ex);
      const float up = u2f((unsigned)(250 - ex) << 23);
      gi = (u32x2){pk_bf(bf_lo(g8[2]) * up, bf_hi(g8[2]) * up), pk_bf(bf_lo(g8[3]) * up, bf_hi(g8[3]) * up)};
      osc = u2f((unsigned)(ex + 2) << 23) * ((real_cls ? 2.f : 4.f) * 256.f / (239.f * 640.f));
    }
    BARRIER();
    TS_MARK();
    {
      const int rr = 4 * g + ((l & 15) >> 2), wrw = l & 15;
      const int rdo = rr * 4096 + w * 512 + (((l & 3) ^ e_key(rr >> 1)) << 4) + (rr & 1) * 8;
      const int wro = wrw * 4096 + w * 512 + ((g ^ e_key(wrw >> 1)) << 4) + (wrw & 1) * 8;
      const u32x2 gf = {g8[0], g8[1]};
      const f32x4 z4 = {0.f, 0.f, 0.f, 0.f};
      u32x2 ev[16];
#pragma unroll
      for (int i = 0; i < 16; ++i) ev[i] = tr_read(eb + (rdo ^ (((i >> 1) & 3) * 64 + (i & 1) * 8)) + (i >> 3) * 256);
      SCHED();
      f32x4 xx[16], vv[16];
      xx[0] = MFMA16K16_H(gf, ev[0], z4);
      xx[1] = MFMA16K16_H(gf, ev[1], z4);
#pragma unroll
      for (int i = 0; i < 18; ++i) {
        if (i + 2 < 16) xx[i + 2] = MFMA16K16_H(gf, ev[i + 2], z4);
        if (i < 16) {
          const f32x4 xs = xx[i];
          const float r0 = bf_lo(wv[i][0]), i0 = bf_hi(wv[i][0]), r1 = bf_lo(wv[i][1]), i1 = bf_hi(wv[i][1]);
          const u32x2 y = {pk_bf(xs[0] * r0 - xs[1] * i0, xs[0] * i0 + xs[1] * r0), pk_bf(xs[2] * r1 - xs[3] * i1, xs[2] * i1 + xs[3] * r1)};
          vv[i] = MFMA16K16_BF(y, gi, z4);
        }
        if (i >= 2) {
          const int k = i - 2;
          *(u32x2*)(eb + (wro ^ (((k >> 1) & 3) * 64 + (k & 1) * 8)) + (k >> 3) * 256) = (u32x2){pk_h(vv[k][0], vv[k][1]), pk_h(vv[k][2], vv[k][3])};
        }
        SCHED();
      }
    }
    BARRIER();
    TS_MARK();
    {
      f32x16 dr[2], di[2];
      u32x4 yr[4], yi[4];
#pragma unroll
      for (int i = 0; i < 4; ++i) { yr[i] = *(const u32x4*)(erow + i * 1024); yi[i] = *(const u32x4*)(erow + 4096 + i * 1024); }
      SCHED();
#pragma unroll
      for (int t2 = 0; t2 < 2; ++t2) {
        dr[t2] = zero; di[t2] = zero;
#pragma unroll
        for (int s2 = 0; s2 < 2; ++s2) {
          const u32x4 pr_ = yr[t2 * 2 + s2], pi_ = swap_halves(yi[t2 * 2 + s2]);
          const u32x4 C = G1[(t2 * 2 + s2) * 2], S = G1[(t2 * 2 + s2) * 2 + 1];
          dr[t2] = MFMA32_H(pr_, C, dr[t2]);
          di[t2] = MFMA32_H(pi_, C, di[t2]);
          dr[t2] = MFMA32_H(neg_h(pi_), S, dr[t2]);
          di[t2] = MFMA32_H(pr_, S, di[t2]);
        }
      }
      u32x4 qr[2][2], qi[2][2];
      bfly_tw<true>(dr, di, tc, tsn, qr, qi);
      const unsigned vo = WS_VT + (w * 41 + c1) * VP + l * 16;
#pragma unroll
      for (int t = 0; t < 2; ++t) {
        f32x16 zr = zero, zq = zero;
#pragma unroll
        for (int ks = 0; ks < 2; ++ks) {
          const u32x4 C = G3[(t * 2 + ks) * 2], S = G3[(t * 2 + ks) * 2 + 1];
          zr = MFMA32_H(C, qr[t][ks], zr);
          zq = MFMA32_H(C, qi[t][ks], zq);
          zr = MFMA32_H(S, neg_h(qi[t][ks]), zr);
          zq = MFMA32_H(S, qr[t][ks], zq);
        }
#pragma unroll
        for (int u = 0; u < 4; ++u) {
          u32x4 o;
#pragma unroll
          for (int j = 0; j < 4; ++j) o[j] = pk_bf(zr[4 * u + j] * osc, zq[4 * u + j] * osc);
          ST128(o, rw, vo + (t * 4 + u) * 1024);
        }
      }
    }
    WAIT_V(0);
    __builtin_amdgcn_s_barrier();
    if (tid == 0) st_dev(ws + WS_FV + c1 * 8, tag);
    TS_END(0, 1);
    return;
  }
  if (aux == 63 && w == 0) {
    wait_flags(ws + WS_F1 + l * 8, tag);
#pragma unroll 1
    for (int i = 0; i < 3; ++i) wait_flags(ws + WS_FA + i * 1024 + l * 16, tag);
    if (l == 0) st_dev(ws + WS_EP, tag);
  }
  TS_END(0, 2);
}

#if defined(__HIP_DEVICE_COMPILE__)
#pragma clang attribute pop
#endif

extern "C" void kernel_launch(void* const* d_in, const int* in_sizes, int n_in, void* d_out, int out_size, void* d_ws, size_t ws_size,
                              hipStream_t stream) {
  const u16* x = (const u16*)d_in[0];
  const u16* w_r = (const u16*)d_in[1];
  const u16* w_i = (const u16*)d_in[2];
  const u16* wg = (const u16*)d_in[4];
  const u16* wu = (const u16*)d_in[5];
  const u16* wd = (const u16*)d_in[6];
  char* ws = (char*)d_ws;
  ka<<<256, 512, 0, stream>>>(x, w_r, w_i, wg, wu, wd, ws, (u16*)d_out);
}
```

```cpp
#include <hip/hip_runtime.h>
#include <hip/hip_bf16.h>

typedef unsigned short u16;
typedef __attribute__((ext_vector_type(8))) __bf16 bf16x8;
typedef __attribute__((ext_vector_type(2))) __bf16 bf16x2;
typedef __attribute__((ext_vector_type(8))) _Float16 f16x8;
typedef __attribute__((ext_vector_type(4))) _Float16 f16x4;
typedef __attribute__((ext_vector_type(2))) _Float16 f16x2;
typedef __attribute__((ext_vector_type(4))) short s16x4;
typedef __attribute__((ext_vector_type(2))) float f32x2;
typedef __attribute__((ext_vector_type(4))) float f32x4;
typedef __attribute__((ext_vector_type(16))) float f32x16;
typedef __attribute__((ext_vector_type(4))) unsigned u32x4;
typedef __attribute__((ext_vector_type(2))) unsigned u32x2;
typedef __attribute__((ext_vector_type(2))) u16 u16x2;
typedef __amdgpu_buffer_rsrc_t rsrc_t;
#define DI __device__ __forceinline__
#define LDS_AS __attribute__((address_space(3)))

namespace tb {
constexpr double kPi = 3.14159265358979323846264338327950288;
constexpr double sinT(double x) { double x2 = x * x, t = x, s = x; for (int i = 1; i < 12; ++i) { t *= -x2 / ((2 * i) * (2 * i + 1)); s += t; } return s; }
constexpr double cosT(double x) { double x2 = x * x, t = 1, s = 1; for (int i = 1; i < 12; ++i) { t *= -x2 / ((2 * i - 1) * (2 * i)); s += t; } return s; }
constexpr double cos2pi(int q, int den) {
  q %= den; if (2 * q > den) q = den - q;
  double sg = 1; if (4 * q > den) { q = den / 2 - q; sg = -1; }
  if (8 * q > den) return sg * sinT(2 * kPi * (den / 4 - q) / den);
  return sg * cosT(2 * kPi * q / den);
}
constexpr u16 bfbits(double d) { unsigned u = __builtin_bit_cast(unsigned, (float)d); u += 0x7fffu + ((u >> 16) & 1u); return (u16)(u >> 16); }
constexpr u16 hbits(double d) { return __builtin_bit_cast(u16, (_Float16)d); }
constexpr int krow(int s, int h, int j) { return 16 * s + 8 * (j >> 2) + 4 * h + (j & 3); }
template <int N> struct CT { double c[N]; };
template <int N> constexpr CT<N> mkC() { CT<N> c{}; for (int i = 0; i < N; ++i) c.c[i] = cos2pi(i, N); return c; }
constexpr CT<64> c64 = mkC<64>();
constexpr CT<80> c80 = mkC<80>();
constexpr double m80(int o, int d) { return o == 1 ? c80.c[(40 * d) % 80] : c80.c[((o >> 1) * d + ((o & 1) ? 20 : 0)) % 80]; }
struct alignas(16) TabF {
  u16 g1[2][2][2][64][8];
  u16 g3[2][2][2][64][8];
  u16 g8[64][2][4];
};
constexpr TabF mkTabF() {
  TabF T{};
  for (int t = 0; t < 2; ++t) for (int ks = 0; ks < 2; ++ks) for (int l = 0; l < 64; ++l) for (int j = 0; j < 8; ++j) {
    const int r = l & 31, k = krow(ks, l >> 5, j);
    const int q1 = (r * (2 * k + t)) & 63, q3 = (k * (2 * r + t)) & 63;
    T.g1[t][ks][0][l][j] = hbits(c64.c[q1] * 0.25); T.g1[t][ks][1][l][j] = hbits(c64.c[(q1 + 48) & 63] * 0.25);
    T.g3[t][ks][0][l][j] = hbits(c64.c[q3] * 0.25); T.g3[t][ks][1][l][j] = hbits(c64.c[(q3 + 48) & 63] * 0.25);
  }
  for (int l = 0; l < 64; ++l) for (int e = 0; e < 4; ++e) {
    const int m = l & 15, k = 4 * (l >> 4) + e, q = (8 * (m >> 1) * (k >> 1)) & 63;
    const double C = c64.c[q], S = c64.c[(q + 48) & 63];
    T.g8[l][0][e] = hbits(0.25 * ((m & 1) == (k & 1) ? C : ((m & 1) ? -S : S)));
    T.g8[l][1][e] = bfbits(239. / 256. * ((m & 1) == (k & 1) ? C : ((k & 1) ? -S : S)));
  }
  return T;
}
struct alignas(16) Tab80 {
  u16 f[3][5][64][8];
  u16 pad[512];
  u16 i[5][5][64][4];
};
constexpr Tab80 mkTab80() {
  Tab80 T{};
  for (int n = 0; n < 3; ++n) for (int ks = 0; ks < 5; ++ks) for (int l = 0; l < 64; ++l) for (int j = 0; j < 8; ++j)
    T.f[n][ks][l][j] = 32 * n + (l & 31) < 80 ? hbits(m80(32 * n + (l & 31), 16 * ks + 8 * (l >> 5) + j)) : (u16)0;
  for (int n = 0; n < 5; ++n) for (int ks = 0; ks < 5; ++ks) for (int l = 0; l < 64; ++l) for (int e = 0; e < 4; ++e)
    T.i[n][ks][l][e] = bfbits(m80(16 * ks + 4 * (l >> 4) + e, 16 * n + (l & 15)));
  return T;
}
}
static __device__ const tb::TabF g_TF = tb::mkTabF();
static __device__ const tb::Tab80 g_T80 = tb::mkTab80();

constexpr unsigned WS_EP = 0;
constexpr unsigned WS_F1 = 256;
constexpr unsigned WS_F2 = WS_F1 + 512;
constexpr unsigned WS_FV = 32768;
constexpr unsigned WS_FH = 8192;
constexpr unsigned WS_FA = 4096;
constexpr unsigned WS_ST = 16384;
constexpr unsigned CP = 65536 + 256;
constexpr unsigned WS_UB = WS_FV + 16384;
constexpr unsigned WS_WT = WS_UB + 40 * CP;
constexpr unsigned WS_AF = WS_WT + 41 * CP;
constexpr unsigned WS_BF = WS_AF + 32 * 48 * 4096;
constexpr unsigned VP = 8192 + 256;
constexpr unsigned WS_VT = WS_BF + 5 * 48 * 8192;
constexpr unsigned WS_END = WS_VT + 328 * VP;

#if defined(__HIP_DEVICE_COMPILE__)
#pragma clang attribute push(__attribute__((target("no-packed-fp32-ops"))), apply_to = function)
#endif
DI float u2f(unsigned u) { return __builtin_bit_cast(float, u); }
DI unsigned f2u(float f) { return __builtin_bit_cast(unsigned, f); }
DI bool all_lanes(bool p) { return __builtin_amdgcn_ballot_w64(!p) == 0; }
DI int s_of_rho(int rho) {
  const int t = rho >> 10, ks = (rho >> 9) & 1, h = (rho >> 8) & 1, r = (rho >> 3) & 31, jh = (rho >> 2) & 1, jl = rho & 3;
  return 64 * (16 * ks + 8 * jh + 4 * h + jl) + 32 * t + r;
}
DI float bf_lo(unsigned u) { return u2f(u << 16); }
DI float bf_hi(unsigned u) { return u2f(u & 0xffff0000u); }
DI unsigned pk_bf(float a, float b) { f32x2 f = {a, b}; return __builtin_bit_cast(unsigned, __builtin_convertvector(f, bf16x2)); }
DI unsigned pk_h(float a, float b) { f32x2 f = {a, b}; return __builtin_bit_cast(unsigned, __builtin_convertvector(f, f16x2)); }
DI float rnd_bf(float a) { return bf_lo(pk_bf(a, 0.f)); }
#define MFMA16_BF(a, b, c) __builtin_amdgcn_mfma_f32_16x16x32_bf16(__builtin_bit_cast(bf16x8, a), __builtin_bit_cast(bf16x8, b), c, 0, 0, 0)
#define MFMA32_H(a, b, c) __builtin_amdgcn_mfma_f32_32x32x16_f16(__builtin_bit_cast(f16x8, a), __builtin_bit_cast(f16x8, b), c, 0, 0, 0)
#define MFMA16K16_H(a, b, c) __builtin_amdgcn_mfma_f32_16x16x16f16(__builtin_bit_cast(f16x4, a), __builtin_bit_cast(f16x4, b), c, 0, 0, 0)
#define MFMA16K16_BF(a, b, c) __builtin_amdgcn_mfma_f32_16x16x16bf16_1k(__builtin_bit_cast(s16x4, a), __builtin_bit_cast(s16x4, b), c, 0, 0, 0)
DI u32x2 tr_read(const char* p) {
  s16x4 v = __builtin_amdgcn_ds_read_tr16_b64_v4i16((LDS_AS s16x4*)(p));
  return __builtin_bit_cast(u32x2, v);
}
DI rsrc_t mk_rsrc(const void* p, unsigned bytes) { return __builtin_amdgcn_make_buffer_rsrc((void*)p, 0, bytes, 0x00020000); }
#define ST128(v, r, off) __builtin_amdgcn_raw_buffer_store_b128(v, r, off, 0, 16)
#define WAIT_V(n) asm volatile("s_waitcnt vmcnt(%0)" ::"n"(n) : "memory")
#define WAIT_L(n) asm volatile("s_waitcnt lgkmcnt(%0)" ::"n"(n) : "memory")
#define SCHED() __builtin_amdgcn_sched_barrier(0)
#define BARRIER() do { WAIT_L(0); __builtin_amdgcn_s_barrier(); asm volatile("" ::: "memory"); } while (0)
DI void dma1(unsigned voff, const void* sbase, unsigned lds) {
  asm volatile("s_mov_b32 m0, %2\n\ts_nop 3\n\tglobal_load_lds_dwordx4 %0, %1" ::"v"(voff), "s"(sbase), "s"(lds) : "memory");
}
template <int CTRL> DI unsigned dpp_u(unsigned x) { return (unsigned)__builtin_amdgcn_mov_dpp((int)x, CTRL, 0xf, 0xf, true); }
template <int CTRL> DI float dpp_f(float x) { return u2f(dpp_u<CTRL>(f2u(x))); }
DI float sum16(float x) {
  x += dpp_f<0xB1>(x); x += dpp_f<0x4E>(x); x += dpp_f<0x141>(x); x += dpp_f<0x128>(x);
  return x;
}
typedef unsigned long long u64;
typedef __attribute__((address_space(1))) u64 gu64;
#define RLX_AGENT __ATOMIC_RELAXED, __HIP_MEMORY_SCOPE_AGENT
DI u64 ld_dev(const char* p) { return __hip_atomic_load((gu64*)p, RLX_AGENT); }
DI void st_dev(char* p, u64 v) { __hip_atomic_store((gu64*)p, v, RLX_AGENT); }
DI u64 next_epoch(u64 e) { return e * 0x9E3779B97F4A7C15ull + 0x632BE59BD9B4E019ull; }
constexpr unsigned SPIN_CAP = 1u << 18;
DI u64 wait_flags(const char* p, u64 tag) {
  u64 v = 0;
#pragma unroll 1
  for (unsigned n = 0; n < SPIN_CAP; ++n) {
    v = ld_dev(p);
    if (all_lanes((v >> 16) == (tag >> 16))) break;
    __builtin_amdgcn_s_sleep(1);
  }
  asm volatile("" ::: "memory");
  return v;
}
#define LD128_DEV(r, off) __builtin_amdgcn_raw_buffer_load_b128(r, off, 0, 16)
#define LD128_DEV2(r, off, soff) __builtin_amdgcn_raw_buffer_load_b128(r, off, soff, 16)
#define LD64_DEV(r, off) __builtin_amdgcn_raw_buffer_load_b64(r, off, 0, 16)
DI unsigned max64(unsigned x) {
  x = max(x, dpp_u<0xB1>(x)); x = max(x, dpp_u<0x4E>(x)); x = max(x, dpp_u<0x141>(x)); x = max(x, dpp_u<0x128>(x));
  const auto s = __builtin_amdgcn_permlane16_swap(x, x, false, false);
  x = max(s[0], s[1]);
  const auto t = __builtin_amdgcn_permlane32_swap(x, x, false, false);
  return max(t[0], t[1]);
}
DI unsigned lds_addr(const char* p) { return (unsigned)(size_t)(LDS_AS const char*)p; }

constexpr int CODE_KB = 37;
constexpr int STAGE_B = 49152;
constexpr int G_CS = 3 * STAGE_B;
constexpr int G_ST = G_CS + 512;
constexpr int G_PF = G_ST + 2048;
constexpr int G_TAG = G_PF + 1024;
constexpr int G_SN = G_TAG + 1024;
constexpr int A_TF = 0;
constexpr int A_T80 = 16384;
constexpr int A_XH = 32768;
constexpr int A_UT = A_XH + 32 * 1344;
constexpr int A_W = A_UT + 4 * 10304;
constexpr int A_MX = A_W + 8 * 5152;
constexpr int A_E = 16384;
constexpr int G_MI = A_MX + 64;
constexpr int KA_LDS = G_MI + 2560;
static_assert(G_SN + 6144 <= KA_LDS && KA_LDS <= 163840, "lds");

DI void pack_h(const f32x16& v, u32x4 (&o)[2]) {
#pragma unroll
  for (int s = 0; s < 2; ++s)
#pragma unroll
    for (int j = 0; j < 4; ++j) o[s][j] = pk_h(v[8 * s + 2 * j], v[8 * s + 2 * j + 1]);
}
DI f16x2 as_h2(unsigned u) { return __builtin_bit_cast(f16x2, u); }
DI unsigned as_u(f16x2 v) { return __builtin_bit_cast(unsigned, v); }
template <bool INV> DI void bfly_tw(const f32x16 (&dr)[2], const f32x16 (&di)[2], const f16x2 (&tc)[8], const f16x2 (&ts)[8],
                                    u32x4 (&er)[2][2], u32x4 (&ei)[2][2]) {
#pragma unroll
  for (int e = 0; e < 8; ++e) {
    const f16x2 r0 = as_h2(pk_h(dr[0][2 * e], dr[0][2 * e + 1])), r1 = as_h2(pk_h(dr[1][2 * e], dr[1][2 * e + 1]));
    const f16x2 i0 = as_h2(pk_h(di[0][2 * e], di[0][2 * e + 1])), i1 = as_h2(pk_h(di[1][2 * e], di[1][2 * e + 1]));
#pragma unroll
    for (int t = 0; t < 2; ++t) {
      const f16x2 sr = t ? r0 - r1 : r0 + r1, si = t ? i0 - i1 : i0 + i1;
      const f16x2 a = sr * tc[e], c = si * tc[e];
      er[t][e >> 2][e & 3] = as_u(__builtin_elementwise_fma(INV ? -si : si, ts[e], a));
      ei[t][e >> 2][e & 3] = as_u(__builtin_elementwise_fma(INV ? sr : -sr, ts[e], c));
    }
  }
}
DI u32x4 neg_h(u32x4 v) { return v ^ 0x80008000u; }
DI u32x4 swap_halves(u32x4 v) { return (u32x4){v[2], v[3], v[0], v[1]}; }
DI int e_key(int d2) { return ((d2 & 2) << 2) | ((d2 & 1) << 2) | ((d2 & 4) >> 1) | ((d2 & 2) >> 1); }

__global__ __launch_bounds__(512, 2) void ka(const u16* __restrict__ x, const u16* __restrict__ w_r, const u16* __restrict__ w_i,
                                             const u16* __restrict__ wg, const u16* __restrict__ wu, const u16* __restrict__ wd,
                                             char* __restrict__ ws, u16* __restrict__ out) {
  __shared__ __attribute__((aligned(1024))) char sm[KA_LDS];
  u64 pc0;
  asm volatile("s_getpc_b64 %0" : "=s"(pc0));
  pc0 &= ~255ull;
  const int b = blockIdx.x, tid = threadIdx.x, l = tid & 63;
  const int w = __builtin_amdgcn_readfirstlane(tid >> 6);
  const int xcd = b & 7, idx = b >> 3;
  const unsigned smb = lds_addr(sm);
  const rsrc_t rw = mk_rsrc(ws, WS_END);
  const int g = l >> 4;
  if (idx * 8 + w < CODE_KB) dma1(l * 16 + (idx * 8 + w) * 1024, (const void*)pc0, smb + G_MI);
  if (idx >= 8) {
    const int pn = (xcd >> 2) * 4 + (idx & 3), f0 = (xcd & 3) * 6 + ((idx - 8) >> 2), r8 = l >> 3, c8 = l & 7;
    const unsigned xo = (s_of_rho(pn * 256 + w) + r8) * 1280 + ((c8 ^ ((r8 >> 1) | (w & 4))) << 4);
    const unsigned wo = ((8 * w + r8) * 1536 + f0 * 64) * 2 + ((c8 ^ ((((r8 >> 1) & 1) | ((w & 1) << 1)) << 1)) << 4);
    dma1(wo, wg, smb + 32768 + w * 1024);
    dma1(wo, wu, smb + 40960 + w * 1024);
#pragma unroll
    for (int i = 0; i < 4; ++i) dma1(xo, (const char*)x + i * 10240, smb + w * 4096 + i * 1024);
    if (w < 4) {
#pragma unroll
      for (int i = 0; i < 2; ++i) {
        const int u = (i * 48 + (idx - 8) * 2 + (w & 1)) * 64 + l, rr = u / 48;
        dma1((128 + 2 * rr + (xcd >> 2)) * 3072 + (xcd & 3) * 768 + (u - rr * 48) * 16, (w & 2) ? wu : wg, smb + G_PF);
      }
    }
  }
  asm volatile("" ::: "memory");
  if (idx >= 8) {
    const int gi = idx - 8;
    const int panel = (xcd >> 2) * 4 + (gi & 3), ft = (xcd & 3) * 6 + (gi >> 2);
    const int gwg = gi * 8 + xcd;
    const int p3 = xcd * 4 + (gi & 3), ct = min(gi >> 2, 4);
    const bool ph3 = gi < 20;
    const int i16 = l & 15, kh = w >> 2, c0 = 2 * (w & 3) + kh;
    constexpr int NB = 18;
    u32x4 B[NB][2];
    const unsigned bo = WS_BF + (ct * 48 + kh) * 8192 + l * 16;
#define LOADB(j) do { B[(j) % NB][0] = LD128_DEV2(rw, bo + c0 * 1024, (j) * 16384); B[(j) % NB][1] = LD128_DEV2(rw, bo + (c0 ^ 1) * 1024, (j) * 16384); } while (0)
    if (w >= 4) {
      const int lw = w - 4, r8 = l >> 3, c8 = l & 7;
      asm volatile("s_mov_b32 m0, %2\n\ts_nop 3\n\tglobal_load_lds_dword %0, %1 sc1" ::"v"(WS_EP + (l & 1) * 4), "s"(ws), "s"(smb + G_TAG + lw * 256) : "memory");
      unsigned xo[2], woff[2];
#pragma unroll
      for (int v = 0; v < 2; ++v) {
        xo[v] = (s_of_rho(panel * 256 + 2 * lw + v) + r8) * 1280 + ((c8 ^ ((r8 >> 1) | ((lw >> 1) << 2))) << 4);
        woff[v] = ((16 * lw + 8 * v + r8) * 1536 + ft * 64) * 2 + ((c8 ^ ((((r8 >> 1) & 1) | (v << 1)) << 1)) << 4);
      }
      const char *xs = (const char*)x + 128, *gs = (const char*)wg + 64 * 3072, *us = (const char*)wu + 64 * 3072;
      const unsigned dx = smb + lw * 8192, dw = smb + 32768 + lw * 2048;
#define STAGE(off)                                                                                   \
  do {                                                                                               \
    _Pragma("unroll") for (int v_ = 0; v_ < 2; ++v_) {                                               \
      dma1(woff[v_], gs, dw + (off) + v_ * 1024);                                                    \
      dma1(woff[v_], us, dw + (off) + 8192 + v_ * 1024);                                             \
    }                                                                                                \
    _Pragma("unroll") for (int i_ = 0; i_ < 8; ++i_) dma1(xo[i_ >> 2], xs + (i_ & 3) * 10240, dx + (off) + i_ * 1024); \
    xs += 128; gs += 64 * 3072; us += 64 * 3072;                                                     \
  } while (0)
      STAGE(STAGE_B);
      WAIT_V(12);
      __builtin_amdgcn_s_barrier();
      const u64 tag = next_epoch(*(const u64*)(sm + G_TAG + lw * 256));
      const unsigned fao = WS_FA + ((ct * 48 + (l < 48 ? l : 0)) % 192) * 16;
      const int job = gwg + 192 * lw;
      STAGE(2 * STAGE_B);
      if (job < 240) {
        const int jc = job / 48, F = job - jc * 48;
        const unsigned d0 = (32 * F + 4 * g) * 1280 + jc * 256 + i16 * 16, d16 = d0 + 16 * 1280;
        u32x4 dv[8];
        asm volatile("global_load_dwordx4 %0, %8, %10\n\tglobal_load_dwordx4 %1, %8, %10 offset:1280\n\tglobal_load_dwordx4 %2, %8, %10 offset:2560\n\t"
                     "global_load_dwordx4 %3, %8, %10 offset:3840\n\tglobal_load_dwordx4 %4, %9, %10\n\tglobal_load_dwordx4 %5, %9, %10 offset:1280\n\t"
                     "global_load_dwordx4 %6, %9, %10 offset:2560\n\tglobal_load_dwordx4 %7, %9, %10 offset:3840"
                     : "=&v"(dv[0]), "=&v"(dv[1]), "=&v"(dv[2]), "=&v"(dv[3]), "=&v"(dv[4]), "=&v"(dv[5]), "=&v"(dv[6]), "=&v"(dv[7])
                     : "v"(d0), "v"(d16), "s"(wd) : "memory");
        WAIT_V(20);
        __builtin_amdgcn_s_barrier();
        STAGE(0);
        asm volatile("s_waitcnt vmcnt(12)" : "+v"(dv[0]), "+v"(dv[1]), "+v"(dv[2]), "+v"(dv[3]), "+v"(dv[4]), "+v"(dv[5]), "+v"(dv[6]), "+v"(dv[7])::"memory");
        {
#pragma unroll
          for (int c = 0; c < 8; ++c) {
            u32x4 o;
#pragma unroll
            for (int j = 0; j < 4; ++j) o[j] = __builtin_amdgcn_perm(dv[2 * j + 1][c >> 1], dv[2 * j][c >> 1], (c & 1) ? 0x07060302u : 0x05040100u);
            ST128(o, rw, WS_BF + job * 8192 + c * 1024 + l * 16);
          }
        }
      } else {
        WAIT_V(12);
        __builtin_amdgcn_s_barrier();
        STAGE(0);
        WAIT_V(12);
      }
      __builtin_amdgcn_s_barrier();
      STAGE(STAGE_B);
      WAIT_V(12);
      __builtin_amdgcn_s_barrier();
      if (tid == 256) st_dev(ws + WS_FA + gwg * 16, tag);
      STAGE(2 * STAGE_B);
      WAIT_V(12);
      __builtin_amdgcn_s_barrier();
      if (l < 40) dma1(l * 16, (const char*)&g_T80.i[ct] + lw * 640, smb + G_MI + lw * 640);
      STAGE(0);
      WAIT_V(12);
      __builtin_amdgcn_s_barrier();
      asm volatile("s_mov_b32 m0, %2\n\ts_nop 3\n\tglobal_load_lds_dwordx4 %0, %1 sc1" ::"v"(fao), "s"(ws), "s"(smb + G_SN + lw * 1536) : "memory");
      if (l < 32)
        asm volatile("s_mov_b32 m0, %2\n\ts_nop 3\n\tglobal_load_lds_dwordx4 %0, %1 sc1" ::"v"(WS_ST + (panel * 256 + lw * 64) * 8 + l * 16), "s"(ws),
                     "s"(smb + G_SN + lw * 1536 + 1024) : "memory");
      STAGE(STAGE_B);
      WAIT_V(12);
      __builtin_amdgcn_s_barrier();
      STAGE(2 * STAGE_B);
      {
        u64 st = *(const u64*)(sm + G_SN + lw * 1536 + 1024 + l * 8);
        const u64 fa = *(const u64*)(sm + G_SN + lw * 1536 + l * 16);
        if (!all_lanes(fa == tag && (unsigned)(st >> 32) == (unsigned)tag)) {
          wait_flags(ws + fao, tag);
#pragma unroll 1
          for (unsigned n = 0; n < SPIN_CAP; ++n) {
            st = ld_dev(ws + WS_ST + (panel * 256 + tid - 256) * 8);
            if (all_lanes((unsigned)(st >> 32) == (unsigned)tag)) break;
            __builtin_amdgcn_s_sleep(4);
          }
        }
        asm volatile("" ::: "memory");
        const float rs = __builtin_amdgcn_rcpf(bf_hi((unsigned)st));
        *(f32x2*)(sm + G_ST + (tid - 256) * 8) = (f32x2){u2f((((unsigned)st & 0xffffu) ^ 0x8000u) * 0x10001u), rs * rs};
      }
      LOADB(0); LOADB(1); LOADB(2);
      SCHED();
      WAIT_V(18);
      BARRIER();
      STAGE(0);
      SCHED();
      LOADB(3); LOADB(4); LOADB(5);
      SCHED();
      WAIT_V(24);
#undef STAGE
      __builtin_amdgcn_s_barrier();
      const char* bsrc = ws + WS_BF + ct * 48 * 8192 + lw * 2048;
#define STAGEB(j0)                                                                                                    \
  do {                                                                                                                \
    _Pragma("unroll") for (int i_ = (j0); i_ < (j0) + 6; ++i_)                                                        \
      asm volatile("s_mov_b32 m0, %2\n\ts_nop 0\n\tglobal_load_lds_dwordx4 %0, %1 sc1\n\tglobal_load_lds_dwordx4 %0, %1 offset:1024 sc1" \
                   ::"v"(l * 16), "s"(bsrc + i_ * 16384), "s"(smb + i_ * 8192 + lw * 2048) : "memory");                \
  } while (0)
      STAGEB(6);
      LOADB(6); LOADB(7); LOADB(8);
      SCHED();
      WAIT_V(24);
      __builtin_amdgcn_s_barrier();
      STAGEB(12);
      LOADB(9); LOADB(10);
      SCHED();
      __builtin_amdgcn_s_barrier();
      if (!ph3) { WAIT_V(0); return; }
      STAGEB(0);
#undef STAGEB
      WAIT_V(0);
      __builtin_amdgcn_s_barrier();
#pragma unroll
      for (int j = 11; j < NB; ++j) LOADB(j);
    } else {
    const int wr = w >> 1, fh = w & 1;
    {
    const int q = (l & 15) >> 2;
    const int xb0 = (i16 >> 3) * 16384 + (wr * 16 + (l & 7)) * 128 + ((g ^ (((l & 7) >> 1) | ((i16 >> 3) << 2))) << 4);
    const int wb0 = (32768 + (8 * g + q) * 128 + (((fh * 2) ^ (((q >> 1) & 1) | ((g & 1) << 1))) << 5) + (l & 3) * 8) ^ (wr << 13);
    const char* xr[2];
    const char* wp[4];
#define SET_BUF(off)                                                                                       \
  do {                                                                                                     \
    xr[0] = sm + (off) + xb0; xr[1] = sm + (off) + (xb0 ^ 64);                                             \
    _Pragma("unroll") for (int i_ = 0; i_ < 4; ++i_) wp[i_] = sm + (off) + (wb0 ^ ((i_ & 1) << 5) ^ ((i_ >> 1) << 13)); \
  } while (0)
    f32x4 acc[4][8];
#pragma unroll
    for (int i = 0; i < 4; ++i)
#pragma unroll
      for (int r = 0; r < 8; ++r) acc[i][r] = (f32x4){0.f, 0.f, 0.f, 0.f};
    f32x4 cs[2] = {{0.f, 0.f, 0.f, 0.f}, {0.f, 0.f, 0.f, 0.f}};
    u32x4 ones = {0x3f803f80u, 0x3f803f80u, 0x3f803f80u, 0x3f803f80u};
    asm volatile("" : "+v"(ones));
    u32x4 xf[2][8];
    u32x2 wf[2][8];
#define RD(ks, n)                                                                                          \
  do {                                                                                                     \
    if ((n) >= 2 && (n) < 10) xf[ks][(n) - 2] = *(const u32x4*)(xr[ks] + (((n) - 2) & 3) * 4096 + (((n) - 2) >> 2) * 1024); \
    else { const int m_ = (n) < 2 ? (n) : (n) - 8; wf[ks][m_] = tr_read(wp[m_ >> 1] + (ks) * 4096 + (m_ & 1) * 512); } \
  } while (0)
#define WF(ks, i) ((u32x4){wf[ks][2 * (i)][0], wf[ks][2 * (i)][1], wf[ks][2 * (i) + 1][0], wf[ks][2 * (i) + 1][1]})
#define KSTEP(ks)                                                                                          \
  do {                                                                                                     \
    _Pragma("unroll") for (int n_ = 0; n_ < 32; ++n_) {                                                    \
      acc[n_ >> 3][n_ & 7] = MFMA16_BF(WF(ks, n_ >> 3), xf[ks][n_ & 7], acc[n_ >> 3][n_ & 7]);             \
      if (n_ < 16) RD((ks) ^ 1, n_);                                                                       \
      if ((ks) == 0 && (n_ == 16 || n_ == 17)) cs[n_ & 1] = MFMA16_BF(WF(0, n_ & 1), ones, cs[n_ & 1]);    \
      if ((ks) == 0 && (n_ == 22 || n_ == 23)) cs[n_ & 1] = MFMA16_BF(WF(1, n_ & 1), ones, cs[n_ & 1]);    \
      SCHED();                                                                                             \
    }                                                                                                      \
  } while (0)
    SET_BUF(0);
    WAIT_V(2);
    BARRIER();
#pragma unroll
    for (int n = 0; n < 16; ++n) RD(0, n);
    SCHED();
    KSTEP(0);
    int nxt = STAGE_B;
#pragma unroll 1
    for (int u = 0; u < 9; ++u) {
      BARRIER();
      SET_BUF(nxt);
      SCHED();
      KSTEP(1);
      KSTEP(0);
      nxt = nxt == 2 * STAGE_B ? 0 : nxt + STAGE_B;
    }
#pragma unroll
    for (int i = 0; i < 2; ++i) {
      u32x4 hl;
#pragma unroll
      for (int e = 0; e < 4; ++e) { const float hi = rnd_bf(cs[i][e]); hl[e] = pk_bf(hi, cs[i][e] - hi); }
      if (i16 == 0) *(u32x4*)(sm + G_CS + w * 128 + i * 64 + g * 16) = hl;
    }
    SCHED();
#pragma unroll
    for (int i = 0; i < 8; ++i) acc[i & 3][i >> 2] = MFMA16_BF(WF(1, i & 3), xf[1][i >> 2], acc[i & 3][i >> 2]);
    SCHED();
    BARRIER();
    unsigned ca[4];
    f32x4 st[4];
#pragma unroll
    for (int i = 0; i < 4; ++i) ca[i] = *(const unsigned*)(sm + G_CS + (w ^ (i & 2)) * 128 + (i & 1) * 64 + i16 * 4);
#pragma unroll
    for (int r = 0; r < 4; ++r) st[r] = *(const f32x4*)(sm + G_ST + (wr * 128 + (r >> 1) * 64 + 8 * (l & 7) + 4 * (i16 >> 3) + (r & 1) * 2) * 8);
    SCHED();
    const unsigned ao = WS_AF + ((panel * 4 + wr * 2) * 48 + ft * 2 + fh) * 4096 + l * 16;
#pragma unroll
    for (int r = 0; r < 10; ++r) {
      if (r < 6) {
#pragma unroll
        for (int i = 0; i < 4; ++i) acc[i][r + 2] = MFMA16_BF(WF(1, i), xf[1][r + 2], acc[i][r + 2]);
      }
      if (r >= 1 && r < 9) {
        const int e = r - 1;
        const u32x2 bm = {g == 0 ? f2u(st[e >> 1][(e & 1) * 2]) : 0u, 0u};
#pragma unroll
        for (int i = 0; i < 4; ++i) acc[i][e] = MFMA16K16_BF(((u32x2){ca[i], 0u}), bm, acc[i][e]);
      }
      if (r >= 2) {
        const int e = r - 2;
        const float r2 = st[e >> 1][(e & 1) * 2 + 1];
        u32x4 o;
#pragma unroll
        for (int i = 0; i < 2; ++i) {
          const f32x4 hv = (acc[i][e] * r2) * acc[2 + i][e];
          o[2 * i] = pk_bf(hv[0], hv[1]); o[2 * i + 1] = pk_bf(hv[2], hv[3]);
        }
        ST128(o, rw, ao + (e >> 2) * 48 * 4096 + (e & 3) * 1024);
      }
#pragma unroll
      for (int i = 0; i < 8; ++i) { __builtin_amdgcn_sched_group_barrier(0x8, 1, 0); __builtin_amdgcn_sched_group_barrier(0x2, 3, 0); }
      SCHED();
    }
#undef SET_BUF
#undef RD
#undef WF
#undef KSTEP
    }
    SCHED();
    const u64 tag = next_epoch(*(const u64*)(sm + G_TAG));
    WAIT_V(0);
    if (l == 0) st_dev(ws + WS_FH + ((panel * 2 + wr) * 64 + ft * 2 + fh) * 8, tag);
    if (!ph3) return;
    __builtin_amdgcn_s_barrier();
    asm volatile("" ::: "memory");
    SCHED();
#pragma unroll
    for (int j = 0; j < 18; ++j)
#pragma unroll
      for (int i = 0; i < 2; ++i) B[j][i] = *(const u32x4*)(sm + j * 8192 + (2 * w + i) * 1024 + l * 16);
    SCHED();
    if (w == 0) wait_flags(ws + WS_FH + ((p3 >> 1) * 64 + (l < 48 ? l : 0)) * 8, tag);
    }
#pragma unroll
    for (int j = 0; j < NB; ++j) asm volatile("" : "+v"(B[j][0]), "+v"(B[j][1]));
    SCHED();
    BARRIER();
    asm volatile("" ::: "memory");
    const char* asrc = ws + WS_AF + p3 * 48 * 4096;
#define LOADA(ch, st)                                                                                                 \
  asm volatile("s_mov_b32 m0, %2\n\ts_nop 3\n\tglobal_load_lds_dwordx4 %0, %1 sc1\n\tglobal_load_lds_dwordx4 %0, %1 offset:1024 sc1\n\t" \
               "global_load_lds_dwordx4 %0, %1 offset:2048 sc1\n\tglobal_load_lds_dwordx4 %0, %1 offset:3072 sc1" ::"v"(l * 16), \
               "s"(asrc + (ch) * 32768 + (st) * 4096), "s"(smb + ((ch) & 3) * 32768 + (st) * 4096) : "memory")
    LOADA(0, w);
    const u64 tag = next_epoch(*(const u64*)(sm + G_TAG));
    f32x4 acc[4][2];
#pragma unroll
    for (int m = 0; m < 4; ++m) { acc[m][0] = (f32x4){0.f, 0.f, 0.f, 0.f}; acc[m][1] = acc[m][0]; }
    const LDS_AS char* ap[2];
#pragma unroll
    for (int i = 0; i < 2; ++i) { ap[i] = (const LDS_AS char*)sm + i * 65536 + kh * 4096 + l * 16; asm volatile("" : "+v"(ap[i])); }
    u32x4 A[3][4], V[11], xres[2];
    u32x2 mi[5];
    unsigned orow[2];
    u64 fv = 0;
    const unsigned vo = WS_VT + (c0 * 41 + 2 * g) * VP + (p3 >> 3) * 2048 + ((l >> 3) & 1) * 1024 + ((p3 & 7) * 8 + (l & 7)) * 16;
#define LOADV()                                                                                                       \
  do {                                                                                                                \
    _Pragma("unroll") for (int u = 0; u < 10; ++u) V[u] = LD128_DEV2(rw, vo, ((u >> 1) * 8 + (u & 1)) * VP);         \
    V[10] = LD128_DEV2(rw, vo - 2 * g * VP, 40 * VP);                                                                 \
  } while (0)
    bool early = false;
    unsigned vs = 0x80000000u;
    if (kh) { LOADA(1, w & 3); LOADA(1, (w & 3) + 4); WAIT_V(8); } else WAIT_V(0);
    BARRIER();
#define ZPART()                                                                                                       \
  do {                                                                                                                \
    if (g == 0) {                                                                                                     \
      _Pragma("unroll") for (int m = 0; m < 4; ++m) V[0][m] = (V[0][m] & 0xffffu) | (V[10][m] << 16);     \
    }                                                                                                                 \
    _Pragma("unroll") for (int ks = 0; ks < 5; ++ks)                                                                  \
      _Pragma("unroll") for (int m = 0; m < 4; ++m)                                                                   \
        acc[m][0] = MFMA16K16_BF(((u32x2){V[2 * ks][m], V[2 * ks + 1][m]}), mi[ks], acc[m][0]);                       \
  } while (0)
#pragma unroll
    for (int m = 0; m < 8; ++m) A[m >> 2][m & 3] = *(const LDS_AS u32x4*)(ap[0] + (m >> 2) * 8192 + (m & 3) * 1024);
    SCHED();
    const rsrc_t rx = mk_rsrc(x, 2048 * 640 * 2);
#pragma unroll
    for (int j = 0; j < 24; ++j) {
      if ((j & 3) == 2 && j < 22) {
        if (j == 2) WAIT_V(4); else if (j == 6) WAIT_V(8); else if (j == 10) WAIT_V(12); else if (j == 14) WAIT_V(13);
        BARRIER();
      }
      if (j == 15) { WAIT_V(3); early = all_lanes(fv == tag); vs = early ? vo : 0x80000000u; asm volatile("" : "+v"(vs)::"memory"); }
#pragma unroll
      for (int n = 0; n < 8; ++n) {
        acc[n & 3][n >> 2] = MFMA16_BF(A[j % 3][n & 3], B[j % NB][n >> 2], acc[n & 3][n >> 2]);
        if (n < 4 && j < 22) A[(j + 2) % 3][n] = *(const LDS_AS u32x4*)(ap[((j + 2) >> 3) & 1] + ((j + 2) & 7) * 8192 + n * 1024);
        if (n == 2 || n == 6) {
          const int q = 2 * j + (n >> 2);
          if (q < 16)
            asm volatile("s_mov_b32 m0, %2\n\ts_nop 0\n\tglobal_load_lds_dwordx4 %0, %1 offset:%3 sc1" ::"v"(l * 16), "s"(asrc + (2 + (q >> 2)) * 32768 + w * 4096),
                         "s"(smb + ((2 + (q >> 2)) & 3) * 32768 + w * 4096), "n"((q & 3) * 1024) : "memory");
          else if (q < 28) B[j - 8][q & 1] = LD128_DEV2(rw, bo + (c0 ^ (q & 1)) * 1024, (j - 8 + NB) * 16384);
          else if (q >= 30 && q < 40) V[q - 30] = LD128_DEV2(rw, vs, (((q - 30) >> 1) * 8 + (q & 1)) * VP);
          else if (q == 40) V[10] = LD128_DEV2(rw, vs - 2 * g * VP, 40 * VP);
        }
        if (n == 4) {
          if (j == 20 || j == 21) {
            orow[j - 20] = s_of_rho(p3 * 64 + 8 * ((4 * g + 2 * kh + j - 20) & 7) + 4 * (g >> 1) + (w & 3)) * 1280 + ct * 256 + i16 * 16;
            xres[j - 20] = __builtin_amdgcn_raw_buffer_load_b128(rx, orow[j - 20], 0, 0);
          }
          if (j == 12) fv = ld_dev(ws + WS_FV + (c0 * 64 + (l < 41 ? l : 0)) * 32);
          if (j >= 16 && j < 21) mi[j - 16] = *(const u32x2*)(sm + G_MI + (j - 16) * 512 + l * 8);
        }
        SCHED();
      }
      if (j == 20 && !early) {
        wait_flags(ws + WS_FV + (c0 * 64 + (l < 41 ? l : 0)) * 32, tag);
        LOADV();
      }
      if (j == 23) {
#pragma unroll
        for (int u = 0; u < 11; ++u) asm volatile("" : "+v"(V[u]));
        ZPART();
      }
      SCHED();
    }
#undef LOADA
#undef LOADB
#undef LOADV
#undef ZPART
    char* tp = sm + 65536 + kh * 16384 + l * 8;
#pragma unroll
    for (int m = 0; m < 4; ++m)
#pragma unroll
      for (int i = 0; i < 2; ++i)
        *(u32x2*)(tp + (m * 8 + (c0 ^ i)) * 512) = (u32x2){pk_bf(acc[m][i][0], acc[m][i][1]), pk_bf(acc[m][i][2], acc[m][i][3])};
    BARRIER();
    unsigned pp[2][8];
#pragma unroll
    for (int k = 0; k < 2; ++k)
#pragma unroll
      for (int c = 0; c < 8; ++c) pp[k][c] = *(const unsigned*)(sm + 65536 + k * 16384 + ((w & 3) * 8 + c) * 512 + l * 8 + kh * 4);
    SCHED();
    const rsrc_t ro = mk_rsrc(out, 2048 * 640 * 2);
#pragma unroll
    for (int r = 0; r < 2; ++r) {
      u32x4 o;
#pragma unroll
      for (int c = 0; c < 4; ++c) {
        const float a = r ? bf_hi(pp[0][2 * c]) + bf_hi(pp[1][2 * c]) : bf_lo(pp[0][2 * c]) + bf_lo(pp[1][2 * c]);
        const float b2 = r ? bf_hi(pp[0][2 * c + 1]) + bf_hi(pp[1][2 * c + 1]) : bf_lo(pp[0][2 * c + 1]) + bf_lo(pp[1][2 * c + 1]);
        o[c] = pk_bf(a + bf_lo(xres[r][c]), b2 + bf_hi(xres[r][c]));
      }
      ST128(o, ro, orow[r]);
    }
    return;
  }
  const int aux = idx * 8 + xcd;
  const int rb = xcd * 8 + idx;
  u64 tag;
  f16x2 tc[8], tsn[8];
  u32x4 G1[8], G3[8];
  u32x4 wo_[3];
  u16x2 m2 = {0, 0};
  const u16* wl;
  unsigned wdst;
  int wst;
  {
    const int row = 4 * w + g, i16 = l & 15;
    dma1(l * 16, (const char*)&g_T80 + w * 2048, smb + A_T80 + w * 2048);
    dma1(l * 16, (const char*)&g_T80 + w * 2048 + 1024, smb + A_T80 + w * 2048 + 1024);
    const u32x4* xp = (const u32x4*)(x + s_of_rho(rb * 32 + row) * 640) + i16;
    u32x4 xv[5];
#pragma unroll
    for (int c = 0; c < 5; ++c) xv[c] = xp[c * 16];
    const u64 ep = ld_dev(ws + WS_EP);
    SCHED();
    {
      const float a0 = (float)(aux * w) * (1.f / 640.f), a1 = (float)(l & 31) * (1.f / 2048.f);
#pragma unroll
      for (int e = 0; e < 8; ++e) {
        const float a = a0 + a1 * (float)(2 * (e & 1) + 8 * (e >> 1) + 4 * (l >> 5)), a2 = a + a1;
        tc[e] = as_h2(pk_h(__builtin_amdgcn_cosf(a), __builtin_amdgcn_cosf(a2)));
        tsn[e] = as_h2(pk_h(__builtin_amdgcn_sinf(a), __builtin_amdgcn_sinf(a2)));
        asm volatile("" : "+v"(tc[e]), "+v"(tsn[e]));
      }
    }
    SCHED();
    WAIT_V(6);
    BARRIER();
    u32x4 fm[15];
#pragma unroll
    for (int u = 0; u < 15; ++u) fm[u] = *(const u32x4*)(sm + A_T80 + u * 1024 + l * 16);
    SCHED();
    float f[40];
#pragma unroll
    for (int c = 0; c < 5; ++c)
#pragma unroll
      for (int j = 0; j < 4; ++j) { f[c * 8 + 2 * j] = bf_lo(xv[c][j]); f[c * 8 + 2 * j + 1] = bf_hi(xv[c][j]); }
    tag = next_epoch(ep);
    asm volatile("" ::"v"(tag), "v"(f[39]));
    SCHED();
    {
      const char* wsrc = (const char*)(w < 4 ? w_r : w_i) + (64 * (4 * (aux >> 3) + (w & 3)) + 8 * (aux & 7)) * 642;
#pragma unroll
      for (int i = 0; i < 5; ++i) dma1(l * 16, wsrc + i * 1024, smb + A_W + w * 5152 + i * 1024);
      if (l == 0) dma1(0, wsrc + 5120, smb + A_W + w * 5152 + 5120);
      dma1(l * 16, (const char*)&g_TF + w * 2048, smb + A_TF + w * 2048);
      dma1(l * 16, (const char*)&g_TF + w * 2048 + 1024, smb + A_TF + w * 2048 + 1024);
    }
    SCHED();
    float s4[4] = {0.f, 0.f, 0.f, 0.f};
#pragma unroll
    for (int e = 0; e < 40; ++e) s4[e & 3] += f[e];
    const float mean = sum16((s4[0] + s4[1]) + (s4[2] + s4[3])) * (1.f / 640.f);
    float q4[4] = {0.f, 0.f, 0.f, 0.f};
#pragma unroll
    for (int e = 0; e < 40; ++e) { f[e] -= mean; q4[e & 3] += f[e] * f[e]; }
    const float mb = rnd_bf(mean), vb = rnd_bf(sum16((q4[0] + q4[1]) + (q4[2] + q4[3])) * (1.f / 640.f));
    const float sd = rnd_bf(__builtin_amdgcn_sqrtf(rnd_bf(vb + 1e-5f)));
    const float rs = __builtin_amdgcn_rcpf(sd), off = (mean - mb) * rs;
    if (i16 == 0) st_dev(ws + WS_ST + (rb * 32 + row) * 8, (tag << 32) | (f2u(mb) >> 16) | f2u(sd));
#pragma unroll
    for (int c = 0; c < 5; ++c) {
      u32x4 oh;
#pragma unroll
      for (int j = 0; j < 4; ++j) oh[j] = pk_h(f[c * 8 + 2 * j] * rs + off, f[c * 8 + 2 * j + 1] * rs + off);
      *(u32x4*)(sm + A_XH + row * 1344 + (c * 16 + i16) * 16) = oh;
    }
    SCHED();
#pragma unroll
    for (int i = 0; i < 3; ++i) {
      const int u = (i * 32 + idx * 4 + (w & 3)) * 64 + l, rr = u / 48;
      dma1((384 + 2 * rr + (xcd >> 2)) * 3072 + (xcd & 3) * 768 + (u - rr * 48) * 16, (w & 4) ? wu : wg, smb + G_MI + 1024);
    }
    SCHED();
    {
      const int h = l >> 5, p = l & 3;
      const char* rd = sm + A_XH + (4 * w + 2 * (g & 1) + (p >> 1)) * 1344 + (8 * h + ((l & 15) >> 2)) * 16 + (p & 1) * 8;
      u32x4 ax[5];
#pragma unroll
      for (int ks = 0; ks < 5; ++ks) {
        const u32x2 lo = tr_read(rd + ks * 256), hi = tr_read(rd + ks * 256 + 64);
        ax[ks] = (u32x4){lo[0], lo[1], hi[0], hi[1]};
      }
      SCHED();
      const int ol = l & 31;
      char* ut = sm + A_UT + (w >> 1) * 10304 + (ol >> 1) * 256 + (w & 1) * 8;
      const int ub = (8 * h + (ol & 1)) ^ (ol & 14);
      f32x16 acc[3];
#pragma unroll
      for (int nt = 0; nt < 3; ++nt) {
#pragma unroll
        for (int e = 0; e < 16; ++e) acc[nt][e] = 0.f;
#pragma unroll
        for (int ks = 0; ks < 5; ++ks) acc[nt] = MFMA32_H(ax[ks], fm[nt * 5 + ks], acc[nt]);
      }
#pragma unroll
      for (int nt = 0; nt < 3; ++nt)
        if (nt < 2 || ol < 16) {
#pragma unroll
          for (int dl = 0; dl < 4; ++dl)
            *(u32x2*)(ut + nt * 4096 + ((ub ^ (2 * dl)) << 4)) = (u32x2){pk_h(acc[nt][dl], acc[nt][4 + dl]), pk_h(acc[nt][8 + dl], acc[nt][12 + dl])};
        }
    }
    WAIT_V(4);
    BARRIER();
    {
      u32x4 uv[5];
#pragma unroll
      for (int i = 0; i < 5; ++i) {
        const int seg = i * 128 + (tid >> 2);
        uv[i] = *(const u32x4*)(sm + A_UT + (tid & 3) * 10304 + ((seg ^ ((seg >> 3) & 14)) << 4));
      }
      SCHED();
#pragma unroll
      for (int i = 0; i < 5; ++i) ST128(uv[i], rw, WS_UB + (i * 8 + w) * CP + rb * 1024 + l * 16);
    }
    SCHED();
    const int half = w >> 2, lr = half ? ~l : l;
    const int eq = lr & 3, kb = (lr >> 2) & 7, cl = 2 * (w & 3) + (l >> 5);
    int fr = 64 * (4 * (aux >> 3) + eq) + 8 * (aux & 7) + kb;
    if (half) fr = (2048 - fr) & 2047;
    wl = (const u16*)(sm + A_W) + eq * 2576 + kb * 321 + (half ? 80 - cl : cl);
    wdst = WS_WT + cl * CP + (half * 2048 + (fr >> 8) * 256 + (fr & 63) * 4 + ((fr >> 6) & 3)) * 16;
    wst = half ? -8 : 8;
#define WT_READ(i0)                                                                                                   \
  do {                                                                                                                \
    _Pragma("unroll") for (int i_ = 0; i_ < 3; ++i_)                                                                  \
      _Pragma("unroll") for (int j_ = 0; j_ < 4; ++j_)                                                                \
        wo_[i_][j_] = __builtin_bit_cast(unsigned, ((u16x2){(wl + wst * ((i0) + i_))[80 * j_], (wl + wst * ((i0) + i_))[10304 + 80 * j_]})); \
    _Pragma("unroll") for (int i_ = 0; i_ < 3; ++i_) {                                                                \
      _Pragma("unroll") for (int j_ = 0; j_ < 4; ++j_) m2 = __builtin_elementwise_max(m2, __builtin_bit_cast(u16x2, wo_[i_][j_] & 0x7fff7fffu)); \
      if (w >= 4) wo_[i_] = (u32x4){wo_[i_][3], wo_[i_][2], wo_[i_][1], wo_[i_][0]} ^ 0x80000000u;                    \
      asm volatile("" : "+v"(wo_[i_]));                                                                               \
    }                                                                                                                 \
  } while (0)
#define WT_STORE(i0)                                                                                                  \
  do {                                                                                                                \
    _Pragma("unroll") for (int i_ = 0; i_ < 3; ++i_)                                                                  \
      if ((i0) + i_ < 5 || ((w & 3) == 0 && l < 32)) ST128(wo_[i_], rw, wdst + ((i0) + i_) * 8 * CP);                 \
  } while (0)
    WT_READ(0);
    SCHED();
    WAIT_V(0);
    __builtin_amdgcn_s_barrier();
    if (tid == 0) st_dev(ws + WS_F1 + aux * 8, tag);
    WT_STORE(0);
    SCHED();
    WT_READ(3);
    WT_STORE(3);
    const unsigned mx = max64(max((unsigned)m2[0], (unsigned)m2[1]));
    if (l == 0) ((unsigned*)(sm + A_MX))[w] = mx;
    SCHED();
  }
#define PUBLISH_F2()                                                                                                  \
  do {                                                                                                                \
    if (tid == 0) {                                                                                                   \
      const u32x4 m0_ = *(const u32x4*)(sm + A_MX), m1_ = *(const u32x4*)(sm + A_MX + 16);                            \
      const unsigned mx_ = max(max(max(m0_[0], m0_[1]), max(m0_[2], m0_[3])), max(max(m1_[0], m1_[1]), max(m1_[2], m1_[3]))); \
      st_dev(ws + WS_F2 + aux * 8, (tag & ~0xffffull) | mx_);                                                         \
    }                                                                                                                 \
  } while (0)
  if (aux < 41 && w == 0) wait_flags(ws + WS_F1 + l * 8, tag);
  WAIT_V(0);
  BARRIER();
  PUBLISH_F2();
  if (aux < 41) {
    const int c1 = aux, c1s = c1 == 40 ? 0 : c1;
    const bool real_cls = c1s == 0;
    char* eb = sm + A_E;
    u32x4 ar[4], ai[4];
    {
      const unsigned uo = WS_UB + c1s * CP + (l >> 2) * 1024 + w * 128 + (l & 3) * 16;
#pragma unroll
      for (int i = 0; i < 4; ++i) {
        ar[i] = LD128_DEV(rw, uo + (c1 == 40 ? 64 : 0) + i * 16384);
        ai[i] = LD128_DEV(rw, uo + 64 + i * 16384);
      }
    }
    SCHED();
    const u32x4 g8 = *(const u32x4*)&g_TF.g8[l][0][0];
#pragma unroll
    for (int i = 0; i < 8; ++i) { G1[i] = *(const u32x4*)(sm + A_TF + i * 1024 + l * 16); G3[i] = *(const u32x4*)(sm + A_TF + 8192 + i * 1024 + l * 16); }
    SCHED();
    u64 f2 = ld_dev(ws + WS_F2 + l * 8);
#pragma unroll
    for (int i = 0; i < 8; ++i) asm volatile("" : "+v"(G1[i]), "+v"(G3[i]));
    SCHED();
    if (real_cls) {
#pragma unroll
      for (int i = 0; i < 4; ++i) ai[i] = (u32x4){0u, 0u, 0u, 0u};
    }
    char* erow = eb + (2 * w) * 4096 + ((l ^ e_key(w)) << 4);
    f32x16 zero;
#pragma unroll
    for (int e = 0; e < 16; ++e) zero[e] = 0.f;
    u32x2 wv[16];
    const unsigned wbase = WS_WT + c1 * CP + (g >> 1) * 32768 + (g & 1) * 8 + (l & 15) * 16 + ((4 * ((w >> 1) & 1) + (w & 1)) * 64 + 32 * (w >> 2)) * 64;
    bool early;
    {
      f32x16 dr[2], di[2];
#pragma unroll
      for (int t = 0; t < 2; ++t) {
        dr[t] = zero; di[t] = zero;
#pragma unroll
        for (int ks = 0; ks < 2; ++ks) {
          const u32x4 C = G1[(t * 2 + ks) * 2], S = G1[(t * 2 + ks) * 2 + 1];
          dr[t] = MFMA32_H(ar[t * 2 + ks], C, dr[t]);
          di[t] = MFMA32_H(ai[t * 2 + ks], C, di[t]);
          dr[t] = MFMA32_H(ai[t * 2 + ks], S, dr[t]);
          di[t] = MFMA32_H(neg_h(ar[t * 2 + ks]), S, di[t]);
        }
      }
      SCHED();
      early = all_lanes((f2 >> 16) == (tag >> 16));
      asm volatile("" ::: "memory");
      if (early) {
#pragma unroll
        for (int i = 0; i < 16; ++i) wv[i] = LD64_DEV(rw, wbase + (i & 1) * 8192 + (i >> 1) * 256);
      }
      SCHED();
      u32x4 br[2][2], bi[2][2];
      bfly_tw<false>(dr, di, tc, tsn, br, bi);
#pragma unroll
      for (int t2 = 0; t2 < 2; ++t2) {
        f32x16 xr_ = zero, xi_ = zero;
#pragma unroll
        for (int ks = 0; ks < 2; ++ks) {
          const u32x4 C = G3[(t2 * 2 + ks) * 2], S = G3[(t2 * 2 + ks) * 2 + 1];
          xr_ = MFMA32_H(C, br[t2][ks], xr_);
          xi_ = MFMA32_H(C, bi[t2][ks], xi_);
          xr_ = MFMA32_H(S, bi[t2][ks], xr_);
          xi_ = MFMA32_H(S, neg_h(br[t2][ks]), xi_);
        }
        u32x4 o0[2], o1[2];
        pack_h(xr_, o0);
        pack_h(xi_, o1);
#pragma unroll
        for (int s2 = 0; s2 < 2; ++s2) {
          *(u32x4*)(erow + (t2 * 2 + s2) * 1024) = o0[s2];
          *(u32x4*)(erow + 4096 + (t2 * 2 + s2) * 1024) = swap_halves(o1[s2]);
        }
      }
    }
    SCHED();
    if (!early) {
      f2 = wait_flags(ws + WS_F2 + l * 8, tag);
#pragma unroll
      for (int i = 0; i < 16; ++i) wv[i] = LD64_DEV(rw, wbase + (i & 1) * 8192 + (i >> 1) * 256);
    }
    float osc;
    u32x2 gi;
    {
      int ex = (int)(max64((unsigned)f2 & 0xffffu) >> 7);
      ex = ex < 8 ? 8 : (ex > 240 ? 240 : ex);
      const float up = u2f((unsigned)(250 - ex) << 23);
      gi = (u32x2){pk_bf(bf_lo(g8[2]) * up, bf_hi(g8[2]) * up), pk_bf(bf_lo(g8[3]) * up, bf_hi(g8[3]) * up)};
      osc = u2f((unsigned)(ex + 2) << 23) * ((real_cls ? 2.f : 4.f) * 256.f / (239.f * 640.f));
    }
    BARRIER();
    {
      const int rr = 4 * g + ((l & 15) >> 2), wrw = l & 15;
      const int rdo = rr * 4096 + w * 512 + (((l & 3) ^ e_key(rr >> 1)) << 4) + (rr & 1) * 8;
      const int wro = wrw * 4096 + w * 512 + ((g ^ e_key(wrw >> 1)) << 4) + (wrw & 1) * 8;
      const u32x2 gf = {g8[0], g8[1]};
      const f32x4 z4 = {0.f, 0.f, 0.f, 0.f};
      u32x2 ev[16];
#pragma unroll
      for (int i = 0; i < 16; ++i) ev[i] = tr_read(eb + (rdo ^ (((i >> 1) & 3) * 64 + (i & 1) * 8)) + (i >> 3) * 256);
      SCHED();
      f32x4 xx[16], vv[16];
      xx[0] = MFMA16K16_H(gf, ev[0], z4);
      xx[1] = MFMA16K16_H(gf, ev[1], z4);
#pragma unroll
      for (int i = 0; i < 18; ++i) {
        if (i + 2 < 16) xx[i + 2] = MFMA16K16_H(gf, ev[i + 2], z4);
        if (i < 16) {
          const f32x4 xs = xx[i];
          const float r0 = bf_lo(wv[i][0]), i0 = bf_hi(wv[i][0]), r1 = bf_lo(wv[i][1]), i1 = bf_hi(wv[i][1]);
          const u32x2 y = {pk_bf(xs[0] * r0 - xs[1] * i0, xs[0] * i0 + xs[1] * r0), pk_bf(xs[2] * r1 - xs[3] * i1, xs[2] * i1 + xs[3] * r1)};
          vv[i] = MFMA16K16_BF(y, gi, z4);
        }
        if (i >= 2) {
          const int k = i - 2;
          *(u32x2*)(eb + (wro ^ (((k >> 1) & 3) * 64 + (k & 1) * 8)) + (k >> 3) * 256) = (u32x2){pk_h(vv[k][0], vv[k][1]), pk_h(vv[k][2], vv[k][3])};
        }
        SCHED();
      }
    }
    BARRIER();
    {
      f32x16 dr[2], di[2];
      u32x4 yr[4], yi[4];
#pragma unroll
      for (int i = 0; i < 4; ++i) { yr[i] = *(const u32x4*)(erow + i * 1024); yi[i] = *(const u32x4*)(erow + 4096 + i * 1024); }
      SCHED();
#pragma unroll
      for (int t2 = 0; t2 < 2; ++t2) {
        dr[t2] = zero; di[t2] = zero;
#pragma unroll
        for (int s2 = 0; s2 < 2; ++s2) {
          const u32x4 pr_ = yr[t2 * 2 + s2], pi_ = swap_halves(yi[t2 * 2 + s2]);
          const u32x4 C = G1[(t2 * 2 + s2) * 2], S = G1[(t2 * 2 + s2) * 2 + 1];
          dr[t2] = MFMA32_H(pr_, C, dr[t2]);
          di[t2] = MFMA32_H(pi_, C, di[t2]);
          dr[t2] = MFMA32_H(neg_h(pi_), S, dr[t2]);
          di[t2] = MFMA32_H(pr_, S, di[t2]);
        }
      }
      u32x4 qr[2][2], qi[2][2];
      bfly_tw<true>(dr, di, tc, tsn, qr, qi);
      const unsigned vo = WS_VT + (w * 41 + c1) * VP + l * 16;
#pragma unroll
      for (int t = 0; t < 2; ++t) {
        f32x16 zr = zero, zq = zero;
#pragma unroll
        for (int ks = 0; ks < 2; ++ks) {
          const u32x4 C = G3[(t * 2 + ks) * 2], S = G3[(t * 2 + ks) * 2 + 1];
          zr = MFMA32_H(C, qr[t][ks], zr);
          zq = MFMA32_H(C, qi[t][ks], zq);
          zr = MFMA32_H(S, neg_h(qi[t][ks]), zr);
          zq = MFMA32_H(S, qr[t][ks], zq);
        }
#pragma unroll
        for (int u = 0; u < 4; ++u) {
          u32x4 o;
#pragma unroll
          for (int j = 0; j < 4; ++j) o[j] = pk_bf(zr[4 * u + j] * osc, zq[4 * u + j] * osc);
          ST128(o, rw, vo + (t * 4 + u) * 1024);
        }
      }
    }
    WAIT_V(0);
    if (l == 0) st_dev(ws + WS_FV + (w * 64 + c1) * 32, tag);
    return;
  }
  if (aux == 63 && w == 0) {
    wait_flags(ws + WS_F1 + l * 8, tag);
#pragma unroll 1
    for (int i = 0; i < 3; ++i) wait_flags(ws + WS_FA + i * 1024 + l * 16, tag);
    if (l == 0) st_dev(ws + WS_EP, tag);
  }
}

#if defined(__HIP_DEVICE_COMPILE__)
#pragma clang attribute pop
#endif

extern "C" void kernel_launch(void* const* d_in, const int* in_sizes, int n_in, void* d_out, int out_size, void* d_ws, size_t ws_size,
                              hipStream_t stream) {
  const u16* x = (const u16*)d_in[0];
  const u16* w_r = (const u16*)d_in[1];
  const u16* w_i = (const u16*)d_in[2];
  const u16* wg = (const u16*)d_in[4];
  const u16* wu = (const u16*)d_in[5];
  const u16* wd = (const u16*)d_in[6];
  char* ws = (char*)d_ws;
  ka<<<256, 512, 0, stream>>>(x, w_r, w_i, wg, wu, wd, ws, (u16*)d_out);
}
```
